```python
import torch

def _softmax(x, axis=-1):
    return torch.softmax(x, dim=axis)
def _gelu(x):
    return torch.nn.functional.gelu(x, approximate="tanh")
def _silu(x):
    return torch.nn.functional.silu(x)
def _sigmoid(x):
    return torch.sigmoid(x)
def _layernorm(x, axis=-1, eps=1e-5):
    m = x.mean(dim=axis, keepdim=True)
    v = x.var(dim=axis, keepdim=True, unbiased=False)
    return (x - m) / torch.sqrt(v + eps)
def _scatter_add(tgt, idx, src, axis=0):
    return tgt.clone().index_add_(axis, idx, src)
def _segment_sum(src, seg, n):
    out = torch.zeros((n,) + src.shape[1:], dtype=src.dtype)
    return out.index_add_(0, seg, src)
def _pad(x, pw):
    flat = tuple(v for pair in reversed(pw) for v in pair)
    return torch.nn.functional.pad(x, flat)

kernel_name = 'parallel_sum__sliding_local_dilated__glu_gated_reglu__prompt'

def setup_inputs():

    _w = (lambda a: (lambda t: t.to(torch.bfloat16) if t.is_floating_point() else t)(torch.as_tensor(a)))
    def _A_setup():
        B, S, D = 1, 2048, 576
        return {
            "x": _w((lambda *s: torch.randn(*s, dtype=torch.bfloat16) * 0.05)(B, S, D)),
            "wqkv": _w((lambda *s: torch.randn(*s, dtype=torch.bfloat16) * 0.05)(D, 3 * D)),
            "wo": _w((lambda *s: torch.randn(*s, dtype=torch.bfloat16) * 0.05)(D, D)),
        }
    _A_in = _A_setup()

    def _B_setup():
        B, S, D, F = 1, 2048, 576, 4608
        return {
            "x": _w((lambda *s: torch.randn(*s, dtype=torch.bfloat16) * 0.05)(B, S, D)),
            "W_gate": _w((lambda *s: torch.randn(*s, dtype=torch.bfloat16) * 0.8)(D, F) * (2.0 / D) ** 0.5),
            "W_up": _w((lambda *s: torch.randn(*s, dtype=torch.bfloat16) * 0.8)(D, F) * (2.0 / D) ** 0.5),
            "W_down": _w((lambda *s: torch.randn(*s, dtype=torch.bfloat16) * 0.8)(F, D) * (1.0 / F) ** 0.5),
        }
    _B_in = _B_setup()

    return {
        "A_x": _A_in["x"],
        "A_wqkv": _A_in["wqkv"],
        "A_wo": _A_in["wo"],
        "B_x": _B_in["x"],
        "B_W_gate": _B_in["W_gate"],
        "B_W_up": _B_in["W_up"],
        "B_W_down": _B_in["W_down"],
    }

def reference(A_x, A_wqkv, A_wo, B_x, B_W_gate, B_W_up, B_W_down):
    def _A_ref(x, wqkv, wo):
        B, S, D = x.shape
        H = 32; Dh = D // H
        W, dil = 8, 2
        qkv = (lambda x, *d: x.permute(*d))((x @ wqkv).reshape(B, S, 3, H, Dh), 2, 0, 3, 1, 4)
        q, k, v = qkv[0], qkv[1], qkv[2]
        scale = Dh ** -0.5
        pos = torch.arange(S)
        off = torch.arange(W) * dil
        idx = pos[:, None] - off[None, :]
        mask = idx >= 0
        idx_c = torch.where(mask, idx, 0).reshape(-1)
        kg = k[:, :, idx_c, :].reshape(B, H, S, W, Dh)
        vg = v[:, :, idx_c, :].reshape(B, H, S, W, Dh)
        logits = torch.einsum("bhsd,bhswd->bhsw", q, kg) * scale
        logits = torch.where(mask[None, None, :, :], logits, -1e9)
        attn = _softmax(logits, axis=-1)
        o = torch.einsum("bhsw,bhswd->bhsd", attn, vg)
        return (lambda x, *d: x.permute(*d))(o, 0, 2, 1, 3).reshape(B, S, D) @ wo

    def _B_ref(x, W_gate, W_up, W_down):
        gate = torch.einsum("bsd,df->bsf", x, W_gate)
        up = torch.einsum("bsd,df->bsf", x, W_up)
        gate = gate * (lambda x, dt: x.to(dt))(gate > 0, gate.dtype)
        h = gate * up
        return torch.einsum("bsf,fd->bsd", h, W_down)

    x = A_x
    xn = _layernorm(x, axis=-1)
    return x + _A_ref(xn, A_wqkv, A_wo) + _B_ref(xn, B_W_gate, B_W_up, B_W_down)
```

```cpp
#include <hip/hip_runtime.h>
#include <hip/hip_bf16.h>
#include <stdint.h>

typedef unsigned short u16;
typedef short bf16x8 __attribute__((ext_vector_type(8)));
typedef short s16x4 __attribute__((ext_vector_type(4)));
typedef float f32x4 __attribute__((ext_vector_type(4)));
typedef float f32x2 __attribute__((ext_vector_type(2)));
typedef __bf16 bf16x2_t __attribute__((ext_vector_type(2)));
typedef unsigned u32x4 __attribute__((ext_vector_type(4)));
typedef unsigned u32x2 __attribute__((ext_vector_type(2)));
#define LAS __attribute__((address_space(3)))
#define GAS __attribute__((address_space(1)))

constexpr int SEQ = 2048, DM = 576, FF = 4608;
constexpr int NKB_X = 18;
constexpr int NKB_H = 162;
constexpr size_t BLK_ELEMS = (size_t)SEQ * 32;

__device__ __forceinline__ unsigned pk_bf16(float lo, float hi) {
    f32x2 v = {lo, hi};
    bf16x2_t b = __builtin_convertvector(v, bf16x2_t);
    return __builtin_bit_cast(unsigned, b);
}
__device__ __forceinline__ float bf_lo(unsigned u) { return __uint_as_float(u << 16); }
__device__ __forceinline__ float bf_hi(unsigned u) { return __uint_as_float(u & 0xffff0000u); }
__device__ __forceinline__ float rbf(float x) { return bf_lo(pk_bf16(x, x)); }

__device__ __forceinline__ void glds16(const void* sbase, unsigned voff, unsigned lds) {
    unsigned sv;
    asm volatile("s_mov_b32 %0, m0\n\ts_mov_b32 m0, %3\n\ts_nop 0\n\tglobal_load_lds_dwordx4 %1, %2\n\ts_mov_b32 m0, %0"
                 : "=&s"(sv) : "v"(voff), "s"(sbase), "s"(lds) : "memory");
}
__device__ __forceinline__ void glds16_q(const void* sbase, unsigned voff, unsigned lds) {
    unsigned sv; unsigned long long ex;
    asm volatile("s_mov_b32 %0, m0\n\ts_mov_b64 %1, exec\n\ts_mov_b32 m0, %4\n\ts_mov_b64 exec, 0xffff\n\tglobal_load_lds_dwordx4 %2, %3\n\ts_mov_b64 exec, %1\n\ts_mov_b32 m0, %0"
                 : "=&s"(sv), "=&s"(ex) : "v"(voff), "s"(sbase), "s"(lds) : "memory");
}
#define SCHED() __builtin_amdgcn_sched_barrier(0)
__device__ __forceinline__ s16x4 tr_read(LAS char* p) {
    return __builtin_bit_cast(s16x4, __builtin_amdgcn_ds_read_tr16_b64_v4i16((LAS s16x4*)p));
}
__device__ __forceinline__ bf16x8 tr_frag(LAS char* p, int off_t1) {
    s16x4 lo = tr_read(p), hi = tr_read(p + off_t1);
    return __builtin_shufflevector(lo, hi, 0, 1, 2, 3, 4, 5, 6, 7);
}
__device__ __forceinline__ bf16x8 row_frag(LAS char* p) { return *(LAS bf16x8*)p; }
#define MFMA16(a, b, c) __builtin_amdgcn_mfma_f32_16x16x32_bf16((a), (b), (c), 0, 0, 0)
#define WAIT_V(n) asm volatile("s_waitcnt vmcnt(" #n ")" ::: "memory")
#define BAR() do { asm volatile("" ::: "memory"); __builtin_amdgcn_s_barrier(); asm volatile("" ::: "memory"); } while (0)

__device__ __forceinline__ int blk_off(int row, int chunk) { return row * 64 + ((chunk ^ (((row >> 3) & 1) << 1)) << 4); }

constexpr int K0_A_BYTES = NKB_X * 80 * 64;
constexpr int K0_SLOT = 16384;
constexpr int K0_LDS = K0_A_BYTES + 4 * K0_SLOT;
constexpr int QS = 456;

__global__ __launch_bounds__(512, 2) void k0_ln_qkv_attn(const u16* __restrict__ x, const u16* __restrict__ wqkv,
                                                         u16* __restrict__ xnb, u16* __restrict__ hb) {
    extern __shared__ __attribute__((aligned(16))) char smem[];
    LAS char* lds = (LAS char*)smem;
    const int tid = threadIdx.x, lane = tid & 63;
    const int w = __builtin_amdgcn_readfirstlane(tid >> 6);
    const int b = blockIdx.x, xcd = b & 7, idx = b >> 3;
    const int mt = 4 * xcd + (idx & 3), hg = idx >> 2;
    const int row0 = 64 * mt - 16;

    unsigned srcB[2];
#pragma unroll
    for (int p = 0; p < 2; ++p) {
        const int r = 2 * (2 * w + p) + (lane >> 5);
        const int swz3 = (r & 3) | (((r >> 3) & 1) << 2);
        int c = (lane & 31) ^ (swz3 << 1);
        c = c > 26 ? 26 : c;
        const int part = c / 9, cc = c - part * 9;
        srcB[p] = (unsigned)(r * (3 * DM) + part * DM + 72 * hg + 8 * cc) * 2u;
    }
    LAS char* ldsB = lds + K0_A_BYTES;
    const unsigned lds0 = (unsigned)(size_t)lds;
    auto issue = [&](int t) {
        const unsigned s = lds0 + K0_A_BYTES + (t & 3) * K0_SLOT + (2 * w) * 1024;
        const u16* base = wqkv + (size_t)t * 32 * (3 * DM);
        glds16(base, srcB[0], s);
        glds16(base, srcB[1], s + 1024);
    };
    issue(0); issue(1); issue(2);

    {
        const int i = lane & 7, rsub = lane >> 3;
        u32x4 xa[9], xb[9];
        const int rgA = w + 2;
        const int growA = row0 + rgA * 8 + rsub;
#pragma unroll
        for (int cc = 0; cc < 9; ++cc) xa[cc] = *(const u32x4*)(x + (size_t)growA * DM + (i + 8 * cc) * 8);
        const int growB = row0 + w * 8 + rsub;
        if (w < 2) {
#pragma unroll
            for (int cc = 0; cc < 9; ++cc)
                xb[cc] = growB >= 0 ? *(const u32x4*)(x + (size_t)growB * DM + (i + 8 * cc) * 8) : (u32x4){0u, 0u, 0u, 0u};
        }
        auto process = [&](u32x4 (&xv)[9], int rg, bool store_global) {
            float s = 0.f;
#pragma unroll
            for (int cc = 0; cc < 9; ++cc)
#pragma unroll
                for (int e = 0; e < 4; ++e) s += bf_lo(xv[cc][e]) + bf_hi(xv[cc][e]);
            s += __shfl_xor(s, 1); s += __shfl_xor(s, 2); s += __shfl_xor(s, 4);
            const float mean = s * (1.0f / 576.0f);
            float q = 0.f;
#pragma unroll
            for (int cc = 0; cc < 9; ++cc)
#pragma unroll
                for (int e = 0; e < 4; ++e) { const float a = bf_lo(xv[cc][e]) - mean, c2 = bf_hi(xv[cc][e]) - mean; q += a * a + c2 * c2; }
            q += __shfl_xor(q, 1); q += __shfl_xor(q, 2); q += __shfl_xor(q, 4);
            const float mb = rbf(mean), vb = rbf(q * (1.0f / 576.0f));
            const float sb = rbf(sqrtf(rbf(vb + 1e-5f)));
            const float rs = 1.0f / sb;
            const int lrow = rg * 8 + rsub;
#pragma unroll
            for (int cc = 0; cc < 9; ++cc) {
                u32x4 o;
#pragma unroll
                for (int e = 0; e < 4; ++e) {
                    const unsigned d = pk_bf16(bf_lo(xv[cc][e]) - mb, bf_hi(xv[cc][e]) - mb);
                    o[e] = pk_bf16(bf_lo(d) * rs, bf_hi(d) * rs);
                }
                const int blk = 2 * cc + (i >> 2), off = blk_off(lrow, i & 3);
                *(LAS u32x4*)(lds + blk * 5120 + off) = o;
                if (store_global) *(u32x4*)((char*)xnb + (size_t)blk * (BLK_ELEMS * 2) + blk_off(row0 + lrow, i & 3)) = o;
            }
        };
        process(xa, rgA, w == hg);
        if (w < 2) process(xb, w, false);
    }

    const int fr = lane & 15, g = lane >> 4, q4 = (lane >> 2) & 3, p4 = lane & 3;
    const int aoff = blk_off(fr, g);
    const int swz3 = q4 | ((g & 1) << 2);
    const int boff0 = (8 * g + q4) * 512 + ((w ^ swz3) << 5) + p4 * 8;
    const int boff1 = (8 * g + q4) * 512 + (((w + 8) ^ swz3) << 5) + p4 * 8;
    f32x4 acc[2][5];
#pragma unroll
    for (int n = 0; n < 2; ++n)
#pragma unroll
        for (int m = 0; m < 5; ++m) acc[n][m] = (f32x4){0.f, 0.f, 0.f, 0.f};
    asm volatile("s_waitcnt lgkmcnt(0)" ::: "memory");
    for (int t = 0; t < NKB_X; ++t) {
        if (t + 2 < NKB_X) { WAIT_V(4); } else if (t + 1 < NKB_X) { WAIT_V(2); } else { WAIT_V(0); }
        BAR();
        if (t + 3 < NKB_X) issue(t + 3);
        LAS char* sb = ldsB + (t & 3) * K0_SLOT;
        LAS char* sa = lds + t * 5120 + aoff;
        bf16x8 b0 = tr_frag(sb + boff0, 2048);
        bf16x8 b1;
        if (w < 6) b1 = tr_frag(sb + boff1, 2048);
#pragma unroll
        for (int m = 0; m < 5; ++m) {
            bf16x8 a = row_frag(sa + m * 1024);
            acc[0][m] = MFMA16(b0, a, acc[0][m]);
            if (w < 6) acc[1][m] = MFMA16(b1, a, acc[1][m]);
        }
    }
    BAR();
    LAS char* qt = ldsB;
#pragma unroll
    for (int n = 0; n < 2; ++n) {
        if (n == 0 || w < 6) {
#pragma unroll
            for (int m = 0; m < 5; ++m) {
                u32x2 v; v[0] = pk_bf16(acc[n][m][0], acc[n][m][1]); v[1] = pk_bf16(acc[n][m][2], acc[n][m][3]);
                *(LAS u32x2*)(qt + (16 * m + fr) * QS + (16 * (w + 8 * n) + 4 * g) * 2) = v;
            }
        }
    }
    asm volatile("s_waitcnt lgkmcnt(0)" ::: "memory");
    BAR();
    {
        const int item = tid >> 1, half = tid & 1;
        const int hd = item & 3, s = item >> 2;
        const int lrow = 16 + s, grow = 64 * mt + s;
        LAS const unsigned* qp = (LAS const unsigned*)(qt + lrow * QS + hd * 36);
        unsigned qv[9];
#pragma unroll
        for (int i = 0; i < 9; ++i) qv[i] = qp[i];
        float lg[4];
#pragma unroll
        for (int ww = 0; ww < 4; ++ww) {
            const int wi = 4 * half + ww;
            LAS const unsigned* kp = (LAS const unsigned*)(qt + (lrow - 2 * wi) * QS + 144 + hd * 36);
            float a = 0.f;
#pragma unroll
            for (int i = 0; i < 9; ++i) { const unsigned kv = kp[i]; a += bf_lo(qv[i]) * bf_lo(kv); a += bf_hi(qv[i]) * bf_hi(kv); }
            a = rbf(rbf(a) * 0.23570226039551584f);
            lg[ww] = (grow - 2 * wi >= 0) ? a : -1e9f;
        }
        float mx = fmaxf(fmaxf(lg[0], lg[1]), fmaxf(lg[2], lg[3]));
        mx = fmaxf(mx, __shfl_xor(mx, 1));
        float ex[4], sum = 0.f;
#pragma unroll
        for (int ww = 0; ww < 4; ++ww) { ex[ww] = __expf(lg[ww] - mx); sum += ex[ww]; }
        sum += __shfl_xor(sum, 1);
        const float inv = 1.0f / sum;
        float pr[8];
#pragma unroll
        for (int ww = 0; ww < 4; ++ww) {
            const float mine = rbf(ex[ww] * inv), other = __shfl_xor(mine, 1);
            pr[ww] = half ? other : mine; pr[4 + ww] = half ? mine : other;
        }
        const int i0 = half * 5;
#pragma unroll
        for (int ii = 0; ii < 5; ++ii) {
            const int i = i0 + ii;
            if (i < 9) {
                float o0 = 0.f, o1 = 0.f;
#pragma unroll
                for (int wi = 0; wi < 8; ++wi) {
                    const unsigned vv = *(LAS const unsigned*)(qt + (lrow - 2 * wi) * QS + 288 + hd * 36 + 4 * i);
                    const float pw = (grow - 2 * wi >= 0) ? pr[wi] : 0.f;
                    o0 += pw * bf_lo(vv); o1 += pw * bf_hi(vv);
                }
                const int col = FF + 72 * hg + 18 * hd + 2 * i;
                *(unsigned*)((char*)hb + (size_t)(col >> 5) * (BLK_ELEMS * 2) + blk_off(grow, (col >> 3) & 3) + (col & 7) * 2) = pk_bf16(o0, o1);
            }
        }
    }
}

constexpr int SLOT = 34816;
constexpr int K12_LDS = 4 * SLOT;

__global__ __launch_bounds__(512, 2) void k1_gateup(const u16* __restrict__ xnb, const u16* __restrict__ Wg,
                                                    const u16* __restrict__ Wu, u16* __restrict__ hb) {
    extern __shared__ __attribute__((aligned(16))) char smem[];
    LAS char* lds = (LAS char*)smem;
    const int tid = threadIdx.x, lane = tid & 63;
    const int w = __builtin_amdgcn_readfirstlane(tid >> 6);
    const int b = blockIdx.x, xcd = b & 7, idx = b >> 3;
    const int j = 4 * xcd + (idx & 3), mt = idx >> 2;
    const int wm = w >> 1, wn = w & 1;

    const unsigned srcA = (unsigned)((256 * mt + 32 * w) * 32 + lane * 8) * 2u;
    const int koct = w >> 1, mat = w & 1;
    const u16* W = mat ? Wu : Wg;
    unsigned srcB, srcE;
    {
        const int r = lane >> 3, swz = ((r >> 1) & 1) | ((koct & 1) << 1), c = (lane & 7) ^ (swz << 1);
        srcB = (unsigned)((8 * koct + r) * FF + 128 * j + 8 * c) * 2u;
        const int re = ((lane >> 1) & 7) ^ ((koct & 1) << 2), ce = (lane & 1) ^ mat;
        srcE = (unsigned)((8 * koct + re) * FF + 4096 + 16 * j + 8 * ce) * 2u;
    }
    const unsigned srcA1 = srcA + 1024, srcB1 = srcB + 128;
    const unsigned lds0 = (unsigned)(size_t)lds;
    auto issue = [&](int t) {
        const unsigned s = lds0 + (t & 3) * SLOT;
        const u16* ba = xnb + (size_t)t * BLK_ELEMS;
        const u16* bw = W + (size_t)t * 32 * FF;
        glds16(ba, srcA, s + (2 * w) * 1024);
        glds16(ba, srcA1, s + (2 * w + 1) * 1024);
        glds16(bw, srcB, s + 16384 + (2 * w) * 1024);
        glds16(bw, srcB1, s + 16384 + (2 * w + 1) * 1024);
        glds16_q(bw, srcE, s + 32768 + (mat * 4 + koct) * 256);
    };
    issue(0); issue(1); issue(2);

    const int fr = lane & 15, g = lane >> 4, q4 = (lane >> 2) & 3, p4 = lane & 3;
    const int aoff = blk_off(64 * wm + fr, g);
    const int swz = ((q4 >> 1) & 1) | ((g & 1) << 1);
    int boff[4];
#pragma unroll
    for (int gt = 0; gt < 4; ++gt) boff[gt] = 16384 + (g * 4 + wn) * 1024 + q4 * 128 + ((gt ^ swz) << 5) + p4 * 8;
    const int emat = p4 >> 1;
    const int eoff0 = 32768 + (emat * 4 + g) * 256 + ((q4 ^ ((g & 1) << 2)) * 32) + ((wn ^ emat) * 16) + (p4 & 1) * 8;
    const int eoff1 = 32768 + (emat * 4 + g) * 256 + (((4 + q4) ^ ((g & 1) << 2)) * 32) + ((wn ^ emat) * 16) + (p4 & 1) * 8;

    f32x4 acc[9][4];
#pragma unroll
    for (int n = 0; n < 9; ++n)
#pragma unroll
        for (int m = 0; m < 4; ++m) acc[n][m] = (f32x4){0.f, 0.f, 0.f, 0.f};

    auto readB = [&](LAS char* s, int nf) -> bf16x8 {
        if (nf < 8) return tr_frag(s + boff[nf >> 1] + (nf & 1) * 2048, 512);
        s16x4 lo = tr_read(s + eoff0), hi = tr_read(s + eoff1);
        return __builtin_shufflevector(lo, hi, 0, 1, 2, 3, 4, 5, 6, 7);
    };

    WAIT_V(5); BAR();
    bf16x8 fa[4], fan[4], fb[3];
#pragma unroll
    for (int m = 0; m < 4; ++m) fa[m] = row_frag(lds + aoff + m * 1024);
    fb[0] = readB(lds, 0); fb[1] = readB(lds, 1);
    for (int t = 0; t < NKB_X; ++t) {
        if (t + 3 < NKB_X) issue(t + 3);
        LAS char* s0 = lds + (t & 3) * SLOT;
        LAS char* s1 = lds + ((t + 1) & 3) * SLOT;
#pragma unroll
        for (int nf = 0; nf < 9; ++nf) {
            fb[(nf + 2) % 3] = (nf + 2 < 9) ? readB(s0, nf + 2) : readB(s1, nf + 2 - 9);
            if (nf >= 3 && nf < 7) fan[nf - 3] = row_frag(s1 + aoff + (nf - 3) * 1024);
#pragma unroll
            for (int m = 0; m < 4; ++m) acc[nf][m] = MFMA16(fb[nf % 3], fa[m], acc[nf][m]);
            SCHED();
        }
#pragma unroll
        for (int m = 0; m < 4; ++m) fa[m] = fan[m];
        if (t + 3 < NKB_X) { WAIT_V(5); } else { WAIT_V(0); }
        BAR();
    }
#pragma unroll
    for (int m = 0; m < 4; ++m) {
        const int row = 256 * mt + 64 * wm + 16 * m + fr;
#pragma unroll
        for (int gt = 0; gt < 4; ++gt) {
            float hv[4];
#pragma unroll
            for (int e = 0; e < 4; ++e) { const float gg = rbf(acc[2 * gt][m][e]), uu = rbf(acc[2 * gt + 1][m][e]); hv[e] = (gg > 0.f ? gg : 0.f) * uu; }
            u32x2 v; v[0] = pk_bf16(hv[0], hv[1]); v[1] = pk_bf16(hv[2], hv[3]);
            const int blk = 4 * j + 2 * wn + (gt >> 1), chunk = (gt & 1) * 2 + (g >> 1);
            *(u32x2*)((char*)hb + (size_t)blk * (BLK_ELEMS * 2) + blk_off(row, chunk) + (g & 1) * 8) = v;
        }
        {
            float hv[4];
#pragma unroll
            for (int e = 0; e < 4; ++e) {
                const float mine = rbf(acc[8][m][e]), oth = __shfl_xor(mine, 32);
                hv[e] = (mine > 0.f ? mine : 0.f) * oth;
            }
            if (g < 2) {
                u32x2 v; v[0] = pk_bf16(hv[0], hv[1]); v[1] = pk_bf16(hv[2], hv[3]);
                const int blk = 128 + (j >> 1), chunk = (j & 1) * 2 + wn;
                *(u32x2*)((char*)hb + (size_t)blk * (BLK_ELEMS * 2) + blk_off(row, chunk) + g * 8) = v;
            }
        }
    }
}

__global__ __launch_bounds__(512, 2) void k2_down(const u16* __restrict__ hb, const u16* __restrict__ Wd,
                                                  const u16* __restrict__ wo, float* __restrict__ part) {
    extern __shared__ __attribute__((aligned(16))) char smem[];
    LAS char* lds = (LAS char*)smem;
    const int tid = threadIdx.x, lane = tid & 63;
    const int w = __builtin_amdgcn_readfirstlane(tid >> 6);
    const int b = blockIdx.x, xcd = b & 7, idx = b >> 3;
    const int split = xcd >> 1, mt = (xcd & 1) * 8 + (idx >> 2), nt = idx & 3;
    const int wm = w >> 1, kh = w & 1;
    const int T0 = split == 0 ? 0 : 21 + 20 * (split - 1), NT = split == 0 ? 21 : 20;

    const unsigned srcA = (unsigned)((w >> 2) * (int)BLK_ELEMS + (128 * mt + 32 * (w & 3)) * 32 + lane * 8) * 2u;
    const int koct = w >> 1, sub = w & 1;
    unsigned offB, offE;
    {
        const int r = lane >> 3, swz = ((r >> 1) & 1) | ((koct & 1) << 1), c = (lane & 7) ^ (swz << 1);
        offB = (unsigned)((32 * sub + 8 * koct + r) * DM + 128 * nt + 8 * c) * 2u;
        const int re = ((lane >> 1) & 7) ^ ((koct & 1) << 2), ce = (lane & 1);
        offE = (unsigned)((32 * sub + 8 * koct + re) * DM + 512 + 16 * nt + 8 * ce) * 2u;
    }
    const unsigned srcA1 = srcA + 1024, offB1 = offB + 128;
    const unsigned lds0 = (unsigned)(size_t)lds;
    auto issue = [&](int t) {
        const unsigned s = lds0 + (t & 3) * SLOT;
        const int T = T0 + t;
        const u16* Wt = T < 72 ? Wd + (size_t)T * 64 * DM : wo + (size_t)(T - 72) * 64 * DM;
        const u16* ba = hb + (size_t)T * 2 * BLK_ELEMS;
        glds16(ba, srcA, s + (2 * w) * 1024);
        glds16(ba, srcA1, s + (2 * w + 1) * 1024);
        glds16(Wt, offB, s + 16384 + (2 * w) * 1024);
        glds16(Wt, offB1, s + 16384 + (2 * w + 1) * 1024);
        glds16_q(Wt, offE, s + 32768 + (sub * 4 + koct) * 256);
    };
    issue(0); issue(1); issue(2);

    const int fr = lane & 15, g = lane >> 4, q4 = (lane >> 2) & 3, p4 = lane & 3;
    const int aoff = kh * 8192 + blk_off(32 * wm + fr, g);
    const int swz = ((q4 >> 1) & 1) | ((g & 1) << 1);
    int boff[4];
#pragma unroll
    for (int u = 0; u < 4; ++u) boff[u] = 16384 + (g * 4 + kh * 2) * 1024 + q4 * 128 + ((u ^ swz) << 5) + p4 * 8;
    const int eoff0 = 32768 + (kh * 4 + g) * 256 + ((q4 ^ ((g & 1) << 2)) * 32) + p4 * 8;
    const int eoff1 = 32768 + (kh * 4 + g) * 256 + (((4 + q4) ^ ((g & 1) << 2)) * 32) + p4 * 8;

    f32x4 acc[9][2];
#pragma unroll
    for (int n = 0; n < 9; ++n)
#pragma unroll
        for (int m = 0; m < 2; ++m) acc[n][m] = (f32x4){0.f, 0.f, 0.f, 0.f};

    auto readB = [&](LAS char* s, int nf) -> bf16x8 {
        if (nf < 8) return tr_frag(s + boff[nf & 3] + (nf >> 2) * 1024, 512);
        s16x4 lo = tr_read(s + eoff0), hi = tr_read(s + eoff1);
        return __builtin_shufflevector(lo, hi, 0, 1, 2, 3, 4, 5, 6, 7);
    };

    WAIT_V(5); BAR();
    bf16x8 fa[2], fan[2], fb[3];
#pragma unroll
    for (int m = 0; m < 2; ++m) fa[m] = row_frag(lds + aoff + m * 1024);
    fb[0] = readB(lds, 0); fb[1] = readB(lds, 1);
    for (int t = 0; t < NT; ++t) {
        if (t + 3 < NT) issue(t + 3);
        LAS char* s0 = lds + (t & 3) * SLOT;
        LAS char* s1 = lds + ((t + 1) & 3) * SLOT;
#pragma unroll
        for (int nf = 0; nf < 9; ++nf) {
            fb[(nf + 2) % 3] = (nf + 2 < 9) ? readB(s0, nf + 2) : readB(s1, nf + 2 - 9);
            if (nf >= 3 && nf < 5) fan[nf - 3] = row_frag(s1 + aoff + (nf - 3) * 1024);
#pragma unroll
            for (int m = 0; m < 2; ++m) acc[nf][m] = MFMA16(fb[nf % 3], fa[m], acc[nf][m]);
            SCHED();
        }
#pragma unroll
        for (int m = 0; m < 2; ++m) fa[m] = fan[m];
        if (t + 3 < NT) { WAIT_V(5); } else { WAIT_V(0); }
        BAR();
    }
    {
        LAS char* xw = lds + w * 10240;
        LAS char* xr = lds + (w ^ 1) * 10240;
#pragma unroll
        for (int n = 0; n < 9; ++n) {
            const bool give = kh ? (n < 5) : (n >= 5);
            if (give) {
#pragma unroll
                for (int m = 0; m < 2; ++m) *(LAS f32x4*)(xw + ((kh ? n : n - 5) * 2 + m) * 1024 + lane * 16) = acc[n][m];
            }
        }
        asm volatile("s_waitcnt lgkmcnt(0)" ::: "memory");
        BAR();
        float* pp = part + (size_t)split * SEQ * DM;
#pragma unroll
        for (int n = 0; n < 9; ++n) {
            const bool keep = kh ? (n >= 5) : (n < 5);
            if (keep) {
#pragma unroll
                for (int m = 0; m < 2; ++m) {
                    const f32x4 o = *(LAS f32x4*)(xr + ((kh ? n - 5 : n) * 2 + m) * 1024 + lane * 16);
                    const int row = 128 * mt + 32 * wm + 16 * m + fr;
                    const int col = (n < 8 ? 128 * nt + 16 * n : 512 + 16 * nt) + 4 * g;
                    *(f32x4*)(pp + (size_t)row * DM + col) = acc[n][m] + o;
                }
            }
        }
    }
}

__global__ __launch_bounds__(256) void k3_reduce(const u16* __restrict__ x, const float* __restrict__ part, u16* __restrict__ out) {
    const int i = blockIdx.x * 256 + threadIdx.x;
    const size_t e = (size_t)i * 8;
    constexpr size_t PS = (size_t)SEQ * DM;
    const u32x4 xv = *(const u32x4*)(x + e);
    f32x4 a0 = *(const f32x4*)(part + e), a1 = *(const f32x4*)(part + e + 4);
#pragma unroll
    for (int s = 1; s < 4; ++s) { a0 += *(const f32x4*)(part + s * PS + e); a1 += *(const f32x4*)(part + s * PS + e + 4); }
    u32x4 o;
    o[0] = pk_bf16(a0[0] + bf_lo(xv[0]), a0[1] + bf_hi(xv[0]));
    o[1] = pk_bf16(a0[2] + bf_lo(xv[1]), a0[3] + bf_hi(xv[1]));
    o[2] = pk_bf16(a1[0] + bf_lo(xv[2]), a1[1] + bf_hi(xv[2]));
    o[3] = pk_bf16(a1[2] + bf_lo(xv[3]), a1[3] + bf_hi(xv[3]));
    *(u32x4*)(out + e) = o;
}

extern "C" void kernel_launch(void* const* d_in, const int* in_sizes, int n_in, void* d_out, int out_size, void* d_ws,
                              size_t ws_size, hipStream_t stream) {
    const u16* x = (const u16*)d_in[0];
    const u16* wqkv = (const u16*)d_in[1];
    const u16* wo = (const u16*)d_in[2];
    const u16* Wg = (const u16*)d_in[4];
    const u16* Wu = (const u16*)d_in[5];
    const u16* Wd = (const u16*)d_in[6];
    char* ws = (char*)d_ws;
    u16* xnb = (u16*)ws;
    u16* hb = (u16*)(ws + (4u << 20));
    float* part = (float*)(ws + (32u << 20));
    hipFuncSetAttribute((const void*)k0_ln_qkv_attn, hipFuncAttributeMaxDynamicSharedMemorySize, K0_LDS);
    hipFuncSetAttribute((const void*)k1_gateup, hipFuncAttributeMaxDynamicSharedMemorySize, K12_LDS);
    hipFuncSetAttribute((const void*)k2_down, hipFuncAttributeMaxDynamicSharedMemorySize, K12_LDS);
    k0_ln_qkv_attn<<<256, 512, K0_LDS, stream>>>(x, wqkv, xnb, hb);
    k1_gateup<<<256, 512, K12_LDS, stream>>>(xnb, Wg, Wu, hb);
    k2_down<<<256, 512, K12_LDS, stream>>>(hb, Wd, wo, part);
    k3_reduce<<<SEQ * DM / 8 / 256, 256, 0, stream>>>(x, part, (u16*)d_out);
}
```

```cpp
#include <hip/hip_runtime.h>
#include <hip/hip_bf16.h>
#include <stdint.h>

typedef unsigned short u16;
typedef short bf16x8 __attribute__((ext_vector_type(8)));
typedef short s16x4 __attribute__((ext_vector_type(4)));
typedef float f32x4 __attribute__((ext_vector_type(4)));
typedef float f32x2 __attribute__((ext_vector_type(2)));
typedef __bf16 bf16x2_t __attribute__((ext_vector_type(2)));
typedef unsigned u32x4 __attribute__((ext_vector_type(4)));
typedef unsigned u32x2 __attribute__((ext_vector_type(2)));
#define LAS __attribute__((address_space(3)))
#define GAS __attribute__((address_space(1)))

constexpr int SEQ = 2048, DM = 576, FF = 4608;
constexpr int NKB_X = 18;
constexpr int NKB_H = 162;
constexpr size_t BLK_ELEMS = (size_t)SEQ * 32;

__device__ __forceinline__ unsigned pk_bf16(float lo, float hi) {
    f32x2 v = {lo, hi};
    bf16x2_t b = __builtin_convertvector(v, bf16x2_t);
    return __builtin_bit_cast(unsigned, b);
}
__device__ __forceinline__ float bf_lo(unsigned u) { return __uint_as_float(u << 16); }
__device__ __forceinline__ float bf_hi(unsigned u) { return __uint_as_float(u & 0xffff0000u); }
__device__ __forceinline__ float rbf(float x) { return bf_lo(pk_bf16(x, x)); }

__device__ __forceinline__ const void* uni(const void* p) {
    const unsigned long long v = (unsigned long long)p;
    const unsigned lo = __builtin_amdgcn_readfirstlane((unsigned)v), hi = __builtin_amdgcn_readfirstlane((unsigned)(v >> 32));
    return (const void*)(((unsigned long long)hi << 32) | lo);
}
__device__ __forceinline__ void glds16(const void* sbase, unsigned voff, unsigned lds) {
    sbase = uni(sbase); lds = __builtin_amdgcn_readfirstlane(lds);
    unsigned sv;
    asm volatile("s_mov_b32 %0, m0\n\ts_mov_b32 m0, %3\n\ts_nop 3\n\tglobal_load_lds_dwordx4 %1, %2\n\ts_mov_b32 m0, %0"
                 : "=&s"(sv) : "v"(voff), "s"(sbase), "s"(lds) : "memory");
}
__device__ __forceinline__ void glds16_q(const void* sbase, unsigned voff, unsigned lds) {
    sbase = uni(sbase); lds = __builtin_amdgcn_readfirstlane(lds);
    unsigned sv; unsigned long long ex;
    asm volatile("s_mov_b32 %0, m0\n\ts_mov_b64 %1, exec\n\ts_mov_b32 m0, %4\n\ts_mov_b64 exec, 0xffff\n\ts_nop 1\n\tglobal_load_lds_dwordx4 %2, %3\n\ts_mov_b64 exec, %1\n\ts_mov_b32 m0, %0"
                 : "=&s"(sv), "=&s"(ex) : "v"(voff), "s"(sbase), "s"(lds) : "memory");
}
#define SCHED() __builtin_amdgcn_sched_barrier(0)
#define STAMP(kid, i) do {} while (0)
#define WSTAMP(i) do {} while (0)
__device__ __forceinline__ s16x4 tr_read(LAS char* p) {
    return __builtin_bit_cast(s16x4, __builtin_amdgcn_ds_read_tr16_b64_v4i16((LAS s16x4*)p));
}
__device__ __forceinline__ bf16x8 tr_frag(LAS char* p, int off_t1) {
    s16x4 lo = tr_read(p), hi = tr_read(p + off_t1);
    return __builtin_shufflevector(lo, hi, 0, 1, 2, 3, 4, 5, 6, 7);
}
__device__ __forceinline__ bf16x8 row_frag(LAS char* p) { return *(LAS bf16x8*)p; }
#define MFMA16(a, b, c) __builtin_amdgcn_mfma_f32_16x16x32_bf16((a), (b), (c), 0, 0, 0)
#define WAIT_V(n) asm volatile("s_waitcnt vmcnt(" #n ")" ::: "memory")
#define BAR() do { asm volatile("" ::: "memory"); __builtin_amdgcn_s_barrier(); asm volatile("" ::: "memory"); } while (0)

__device__ __forceinline__ int blk_off(int row, int chunk) { return row * 64 + ((chunk ^ (((row >> 3) & 1) << 1)) << 4); }

constexpr int K0_A_BYTES = NKB_X * 80 * 64;
constexpr int K0_SLOT = 16384;
constexpr int K0_LDS = K0_A_BYTES + 4 * K0_SLOT;
constexpr int QS = 456;

__global__ __launch_bounds__(512, 2) void k0_ln_qkv_attn(const u16* __restrict__ x, const u16* __restrict__ wqkv,
                                                         u16* __restrict__ xnb, u16* __restrict__ hb) {
    extern __shared__ __attribute__((aligned(16))) char smem[];
    LAS char* lds = (LAS char*)smem;
    const int tid = threadIdx.x, lane = tid & 63;
    const int w = __builtin_amdgcn_readfirstlane(tid >> 6);
    const int b = blockIdx.x, xcd = b & 7, idx = b >> 3;
    const int mt = 4 * xcd + (idx & 3), hg = idx >> 2;
    const int row0 = 64 * mt - 16;

    unsigned srcB[2];
#pragma unroll
    for (int p = 0; p < 2; ++p) {
        const int r = 2 * (2 * w + p) + (lane >> 5);
        const int swz3 = (r & 3) | (((r >> 3) & 1) << 2);
        int c = (lane & 31) ^ (swz3 << 1);
        c = c > 26 ? 26 : c;
        const int part = c / 9, cc = c - part * 9;
        srcB[p] = (unsigned)(r * (3 * DM) + part * DM + 72 * hg + 8 * cc) * 2u;
    }
    LAS char* ldsB = lds + K0_A_BYTES;
    const unsigned lds0 = (unsigned)(size_t)lds;
    auto issue = [&](int t) {
        const unsigned s = lds0 + K0_A_BYTES + (t & 3) * K0_SLOT + (2 * w) * 1024;
        const u16* base = wqkv + (size_t)t * 32 * (3 * DM);
        glds16(base, srcB[0], s);
        glds16(base, srcB[1], s + 1024);
    };
    STAMP(0, 0);
    issue(0); issue(1); issue(2);

    {
        const int i = lane & 7, rsub = lane >> 3;
        u32x4 xa[9], xb[9];
        const int rgA = w + 2;
        const int growA = row0 + rgA * 8 + rsub;
#pragma unroll
        for (int cc = 0; cc < 9; ++cc) xa[cc] = *(const u32x4*)(x + (size_t)growA * DM + (i + 8 * cc) * 8);
        const int growB = row0 + w * 8 + rsub;
        if (w < 2) {
#pragma unroll
            for (int cc = 0; cc < 9; ++cc)
                xb[cc] = growB >= 0 ? *(const u32x4*)(x + (size_t)growB * DM + (i + 8 * cc) * 8) : (u32x4){0u, 0u, 0u, 0u};
        }
        auto process = [&](u32x4 (&xv)[9], int rg, bool store_global) {
            float s = 0.f;
#pragma unroll
            for (int cc = 0; cc < 9; ++cc)
#pragma unroll
                for (int e = 0; e < 4; ++e) s += bf_lo(xv[cc][e]) + bf_hi(xv[cc][e]);
            s += __shfl_xor(s, 1); s += __shfl_xor(s, 2); s += __shfl_xor(s, 4);
            const float mean = s * (1.0f / 576.0f);
            float q = 0.f;
#pragma unroll
            for (int cc = 0; cc < 9; ++cc)
#pragma unroll
                for (int e = 0; e < 4; ++e) { const float a = bf_lo(xv[cc][e]) - mean, c2 = bf_hi(xv[cc][e]) - mean; q += a * a + c2 * c2; }
            q += __shfl_xor(q, 1); q += __shfl_xor(q, 2); q += __shfl_xor(q, 4);
            const float mb = rbf(mean), vb = rbf(q * (1.0f / 576.0f));
            const float sb = rbf(sqrtf(rbf(vb + 1e-5f)));
            const float rs = 1.0f / sb;
            const int lrow = rg * 8 + rsub;
#pragma unroll
            for (int cc = 0; cc < 9; ++cc) {
                u32x4 o;
#pragma unroll
                for (int e = 0; e < 4; ++e) {
                    const unsigned d = pk_bf16(bf_lo(xv[cc][e]) - mb, bf_hi(xv[cc][e]) - mb);
                    o[e] = pk_bf16(bf_lo(d) * rs, bf_hi(d) * rs);
                }
                const int blk = 2 * cc + (i >> 2), off = blk_off(lrow, i & 3);
                *(LAS u32x4*)(lds + blk * 5120 + off) = o;
                if (store_global) *(u32x4*)((char*)xnb + (size_t)blk * (BLK_ELEMS * 2) + blk_off(row0 + lrow, i & 3)) = o;
            }
        };
        process(xa, rgA, w == hg);
        if (w < 2) process(xb, w, false);
    }

    const int fr = lane & 15, g = lane >> 4, q4 = (lane >> 2) & 3, p4 = lane & 3;
    const int aoff = blk_off(fr, g);
    const int swz3 = q4 | ((g & 1) << 2);
    const int boff0 = (8 * g + q4) * 512 + ((w ^ swz3) << 5) + p4 * 8;
    const int boff1 = (8 * g + q4) * 512 + (((w + 8) ^ swz3) << 5) + p4 * 8;
    f32x4 acc[2][5];
#pragma unroll
    for (int n = 0; n < 2; ++n)
#pragma unroll
        for (int m = 0; m < 5; ++m) acc[n][m] = (f32x4){0.f, 0.f, 0.f, 0.f};
    asm volatile("s_waitcnt lgkmcnt(0)" ::: "memory");
    STAMP(0, 1);
    for (int t = 0; t < NKB_X; ++t) {
        if (t + 2 < NKB_X) { WAIT_V(4); } else if (t + 1 < NKB_X) { WAIT_V(2); } else { WAIT_V(0); }
        BAR();
        if (t + 3 < NKB_X) issue(t + 3);
        LAS char* sb = ldsB + (t & 3) * K0_SLOT;
        LAS char* sa = lds + t * 5120 + aoff;
        bf16x8 b0 = tr_frag(sb + boff0, 2048);
        bf16x8 b1;
        if (w < 6) b1 = tr_frag(sb + boff1, 2048);
#pragma unroll
        for (int m = 0; m < 5; ++m) {
            bf16x8 a = row_frag(sa + m * 1024);
            acc[0][m] = MFMA16(b0, a, acc[0][m]);
            if (w < 6) acc[1][m] = MFMA16(b1, a, acc[1][m]);
        }
    }
    BAR();
    STAMP(0, 2);
    LAS char* qt = ldsB;
#pragma unroll
    for (int n = 0; n < 2; ++n) {
        if (n == 0 || w < 6) {
#pragma unroll
            for (int m = 0; m < 5; ++m) {
                u32x2 v; v[0] = pk_bf16(acc[n][m][0], acc[n][m][1]); v[1] = pk_bf16(acc[n][m][2], acc[n][m][3]);
                *(LAS u32x2*)(qt + (16 * m + fr) * QS + (16 * (w + 8 * n) + 4 * g) * 2) = v;
            }
        }
    }
    asm volatile("s_waitcnt lgkmcnt(0)" ::: "memory");
    BAR();
    STAMP(0, 3);
    {
        const int item = tid >> 1, half = tid & 1;
        const int hd = item & 3, s = item >> 2;
        const int lrow = 16 + s, grow = 64 * mt + s;
        LAS const unsigned* qp = (LAS const unsigned*)(qt + lrow * QS + hd * 36);
        unsigned qv[9];
#pragma unroll
        for (int i = 0; i < 9; ++i) qv[i] = qp[i];
        float lg[4];
#pragma unroll
        for (int ww = 0; ww < 4; ++ww) {
            const int wi = 4 * half + ww;
            LAS const unsigned* kp = (LAS const unsigned*)(qt + (lrow - 2 * wi) * QS + 144 + hd * 36);
            float a = 0.f;
#pragma unroll
            for (int i = 0; i < 9; ++i) { const unsigned kv = kp[i]; a += bf_lo(qv[i]) * bf_lo(kv); a += bf_hi(qv[i]) * bf_hi(kv); }
            a = rbf(rbf(a) * 0.23570226039551584f);
            lg[ww] = (grow - 2 * wi >= 0) ? a : -1e9f;
        }
        float mx = fmaxf(fmaxf(lg[0], lg[1]), fmaxf(lg[2], lg[3]));
        mx = fmaxf(mx, __shfl_xor(mx, 1));
        float ex[4], sum = 0.f;
#pragma unroll
        for (int ww = 0; ww < 4; ++ww) { ex[ww] = __expf(lg[ww] - mx); sum += ex[ww]; }
        sum += __shfl_xor(sum, 1);
        const float inv = 1.0f / sum;
        float pr[8];
#pragma unroll
        for (int ww = 0; ww < 4; ++ww) {
            const float mine = rbf(ex[ww] * inv), other = __shfl_xor(mine, 1);
            pr[ww] = half ? other : mine; pr[4 + ww] = half ? mine : other;
        }
        const int i0 = half * 5;
#pragma unroll
        for (int ii = 0; ii < 5; ++ii) {
            const int i = i0 + ii;
            if (i < 9) {
                float o0 = 0.f, o1 = 0.f;
#pragma unroll
                for (int wi = 0; wi < 8; ++wi) {
                    const unsigned vv = *(LAS const unsigned*)(qt + (lrow - 2 * wi) * QS + 288 + hd * 36 + 4 * i);
                    const float pw = (grow - 2 * wi >= 0) ? pr[wi] : 0.f;
                    o0 += pw * bf_lo(vv); o1 += pw * bf_hi(vv);
                }
                const int col = FF + 72 * hg + 18 * hd + 2 * i;
                *(unsigned*)((char*)hb + (size_t)(col >> 5) * (BLK_ELEMS * 2) + blk_off(grow, (col >> 3) & 3) + (col & 7) * 2) = pk_bf16(o0, o1);
            }
        }
    }
    STAMP(0, 15);
}

constexpr int RA = 0, RB = 65536, RE = 131072;
constexpr int K12_LDS = 131072 + 8192;
#define PF 3

__global__ __launch_bounds__(512, 2) void k1_gateup(const u16* __restrict__ xnb, const u16* __restrict__ Wg,
                                                    const u16* __restrict__ Wu, u16* __restrict__ hb) {
    extern __shared__ __attribute__((aligned(16))) char smem[];
    LAS char* lds = (LAS char*)smem;
    const int tid = threadIdx.x, lane = tid & 63;
    const int w = __builtin_amdgcn_readfirstlane(tid >> 6);
    const int b = blockIdx.x, xcd = b & 7, idx = b >> 3;
    const int j = 4 * xcd + (idx & 3), mt = idx >> 2;
    const int wm = w >> 1, wn = w & 1;

    const unsigned srcA = (unsigned)((256 * mt + 32 * w) * 32 + lane * 8) * 2u;
    const int koct = w >> 1, mat = w & 1;
    const u16* W = mat ? Wu : Wg;
    unsigned srcB, srcE;
    {
        const int r = lane >> 3, swz = ((r >> 1) & 1) | ((koct & 1) << 1), c = (lane & 7) ^ (swz << 1);
        srcB = (unsigned)((8 * koct + r) * FF + 128 * j + 8 * c) * 2u;
        const int re = ((lane >> 1) & 7) ^ ((koct & 1) << 2), ce = (lane & 1) ^ mat;
        srcE = (unsigned)((8 * koct + re) * FF + 4096 + 16 * j + 8 * ce) * 2u;
    }
    const unsigned srcA1 = srcA + 1024, srcB1 = srcB + 128;
    const unsigned lds0 = (unsigned)(size_t)lds;
    const unsigned dA = lds0 + RA + (2 * w) * 1024, dB = lds0 + RB + (2 * w) * 1024, dE = lds0 + RE + (mat * 4 + koct) * 256;
    auto issue1 = [&](int t, int i) {
        const int sl = t & 3;
        const u16* ba = xnb + (size_t)t * BLK_ELEMS;
        const u16* bw = W + (size_t)t * 32 * FF;
        if (i == 0) glds16(bw, srcB, dB + sl * 16384);
        if (i == 1) glds16(bw, srcB1, dB + sl * 16384 + 1024);
        if (i == 2) glds16_q(bw, srcE, dE + sl * 2048);
        if (i == 3) glds16(ba, srcA, dA + sl * 16384);
        if (i == 4) glds16(ba, srcA1, dA + sl * 16384 + 1024);
    };
    STAMP(1, 0);
#pragma unroll
    for (int t = 0; t < 3; ++t)
#pragma unroll
        for (int i = 0; i < 5; ++i) issue1(t, i);

    const int fr = lane & 15, g = lane >> 4, q4 = (lane >> 2) & 3, p4 = lane & 3;
    LAS char* pa = lds + RA + blk_off(64 * wm + fr, g);
    const int swz = ((q4 >> 1) & 1) | ((g & 1) << 1);
    LAS char* pb[4];
#pragma unroll
    for (int gt = 0; gt < 4; ++gt) pb[gt] = lds + RB + (g * 4 + wn) * 1024 + q4 * 128 + ((gt ^ swz) << 5) + p4 * 8;
    const int emat = p4 >> 1;
    LAS char* pe0 = lds + RE + (emat * 4 + g) * 256 + ((q4 ^ ((g & 1) << 2)) * 32) + ((wn ^ emat) * 16) + (p4 & 1) * 8;
    LAS char* pe1 = lds + RE + (emat * 4 + g) * 256 + (((4 + q4) ^ ((g & 1) << 2)) * 32) + ((wn ^ emat) * 16) + (p4 & 1) * 8;
    auto rdB = [&](int sl, int nf, int t1) -> s16x4 {
        if (nf < 8) return tr_read(pb[nf >> 1] + sl * 16384 + (nf & 1) * 2048 + t1 * 512);
        return tr_read((t1 ? pe1 : pe0) + sl * 2048);
    };

    f32x4 acc[9][4];
#pragma unroll
    for (int n = 0; n < 9; ++n)
#pragma unroll
        for (int m = 0; m < 4; ++m) acc[n][m] = (f32x4){0.f, 0.f, 0.f, 0.f};

    WAIT_V(10); BAR();
    STAMP(1, 1);
    bf16x8 fa[2][4], fb[9];
#pragma unroll
    for (int m = 0; m < 4; ++m) fa[0][m] = row_frag(pa + m * 1024);
#pragma unroll
    for (int i = 0; i < PF; ++i) { s16x4 lo = rdB(0, i, 0), hi = rdB(0, i, 1); fb[i] = __builtin_shufflevector(lo, hi, 0, 1, 2, 3, 4, 5, 6, 7); }
#pragma unroll
    for (int t = 0; t < NKB_X; ++t) {
        const int sl = t & 3, sn = (t + 1) & 3, cur = t & 1;
#pragma unroll
        for (int nf = 0; nf < 9; ++nf) {
            const int psl = (nf + PF < 9) ? sl : sn, nn = (nf + PF < 9) ? nf + PF : nf + PF - 9;
            const bool pf = (nf + PF < 9) || (t + 1 < NKB_X);
            s16x4 lo, hi;
            acc[nf][0] = MFMA16(fb[nf], fa[cur][0], acc[nf][0]);
            SCHED();
            if (nf == 0) {
                if (t + 2 < NKB_X) { WAIT_V(5); } else { WAIT_V(0); }
                BAR();
            }
            if (pf) lo = rdB(psl, nn, 0);
            SCHED();
            acc[nf][1] = MFMA16(fb[nf], fa[cur][1], acc[nf][1]);
            SCHED();
            if (pf) hi = rdB(psl, nn, 1);
            SCHED();
            acc[nf][2] = MFMA16(fb[nf], fa[cur][2], acc[nf][2]);
            SCHED();
            if (nf >= 3 && nf < 7 && t + 1 < NKB_X) fa[cur ^ 1][nf - 3] = row_frag(pa + sn * 16384 + (nf - 3) * 1024);
            if ((nf < 3 || nf >= 7) && t + 3 < NKB_X) issue1(t + 3, nf < 3 ? nf : nf - 4);
            SCHED();
            acc[nf][3] = MFMA16(fb[nf], fa[cur][3], acc[nf][3]);
            SCHED();
            if (pf) fb[nn] = __builtin_shufflevector(lo, hi, 0, 1, 2, 3, 4, 5, 6, 7);
        }
    }
    STAMP(1, 2);
#pragma unroll
    for (int m = 0; m < 4; ++m) {
        const int row = 256 * mt + 64 * wm + 16 * m + fr;
#pragma unroll
        for (int gt = 0; gt < 4; ++gt) {
            float hv[4];
#pragma unroll
            for (int e = 0; e < 4; ++e) { const float gg = rbf(acc[2 * gt][m][e]), uu = rbf(acc[2 * gt + 1][m][e]); hv[e] = (gg > 0.f ? gg : 0.f) * uu; }
            u32x2 v; v[0] = pk_bf16(hv[0], hv[1]); v[1] = pk_bf16(hv[2], hv[3]);
            const int blk = 4 * j + 2 * wn + (gt >> 1), chunk = (gt & 1) * 2 + (g >> 1);
            *(u32x2*)((char*)hb + (size_t)blk * (BLK_ELEMS * 2) + blk_off(row, chunk) + (g & 1) * 8) = v;
        }
        {
            float hv[4];
#pragma unroll
            for (int e = 0; e < 4; ++e) {
                const float mine = rbf(acc[8][m][e]), oth = __shfl_xor(mine, 32);
                hv[e] = (mine > 0.f ? mine : 0.f) * oth;
            }
            if (g < 2) {
                u32x2 v; v[0] = pk_bf16(hv[0], hv[1]); v[1] = pk_bf16(hv[2], hv[3]);
                const int blk = 128 + (j >> 1), chunk = (j & 1) * 2 + wn;
                *(u32x2*)((char*)hb + (size_t)blk * (BLK_ELEMS * 2) + blk_off(row, chunk) + g * 8) = v;
            }
        }
    }
    STAMP(1, 15);
}

constexpr int K2_NT = 21, K2_LA = 6;
constexpr int K2_RB = 0, K2_RE = 131072, K2_LDS = 131072 + 16384;
__device__ __forceinline__ void gload2(bf16x8& d0, bf16x8& d1, const void* sbase, unsigned voff) {
    sbase = uni(sbase);
    asm volatile("s_nop 4\n\tglobal_load_dwordx4 %0, %2, %3\n\tglobal_load_dwordx4 %1, %2, %3 offset:1024"
                 : "=&v"(d0), "=&v"(d1) : "v"(voff), "s"(sbase) : "memory");
}
__global__ __launch_bounds__(512, 2) void k2_down(const u16* __restrict__ hb, const u16* __restrict__ Wd,
                                                  const u16* __restrict__ wo, float* __restrict__ part) {
    extern __shared__ __attribute__((aligned(16))) char smem[];
    LAS char* lds = (LAS char*)smem;
    const int tid = threadIdx.x, lane = tid & 63;
    const int w = __builtin_amdgcn_readfirstlane(tid >> 6);
    const int b = blockIdx.x, xcd = b & 7, idx = b >> 3;
    const int split = xcd >> 1, mt = (xcd & 1) * 8 + (idx >> 2), nt = idx & 3;
    const int wm = w >> 1, kh = w & 1;
    const int T0 = split == 0 ? 0 : 21 + 20 * (split - 1);

    const unsigned srcA = (unsigned)(kh * (int)BLK_ELEMS * 2 + blk_off(128 * mt + 32 * wm + (lane & 15), lane >> 4));
    const int koct = w >> 1, sub = w & 1;
    unsigned offB, offE;
    {
        const int r = lane >> 3, swz = ((r >> 1) & 1) | ((koct & 1) << 1), c = (lane & 7) ^ (swz << 1);
        offB = (unsigned)((32 * sub + 8 * koct + r) * DM + 128 * nt + 8 * c) * 2u;
        const int re = ((lane >> 1) & 7) ^ ((koct & 1) << 2), ce = (lane & 1);
        offE = (unsigned)((32 * sub + 8 * koct + re) * DM + 512 + 16 * nt + 8 * ce) * 2u;
    }
    const unsigned offB1 = offB + 128;
    const unsigned lds0 = (unsigned)(size_t)lds;
    const unsigned dB = lds0 + K2_RB + (2 * w) * 1024, dE = lds0 + K2_RE + (sub * 4 + koct) * 256;
    bf16x8 fa[8][2];
    auto issue1 = [&](int t, int i) {
        const int sl = t & 7;
        int T = T0 + t; if (t == K2_NT - 1 && split != 0) T -= 1;
        const u16* Wt = T < 72 ? Wd + (size_t)T * 64 * DM : wo + (size_t)(T - 72) * 64 * DM;
        const u16* ba = hb + (size_t)T * 2 * BLK_ELEMS;
        if (i == 0) glds16(Wt, offB, dB + sl * 16384);
        if (i == 1) glds16(Wt, offB1, dB + sl * 16384 + 1024);
        if (i == 2) glds16_q(Wt, offE, dE + sl * 2048);
        if (i == 3) gload2(fa[t & 7][0], fa[t & 7][1], ba, srcA);
    };
    STAMP(2, 0);
#pragma unroll
    for (int t = 0; t < K2_LA; ++t)
#pragma unroll
        for (int i = 0; i < 4; ++i) issue1(t, i);

    const int fr = lane & 15, g = lane >> 4, q4 = (lane >> 2) & 3, p4 = lane & 3;
    const int swz = ((q4 >> 1) & 1) | ((g & 1) << 1);
    LAS char* pb[2][4];
#pragma unroll
    for (int u = 0; u < 4; ++u) { pb[0][u] = lds + K2_RB + (g * 4 + kh * 2) * 1024 + q4 * 128 + ((u ^ swz) << 5) + p4 * 8; pb[1][u] = pb[0][u] + 65536; }
    LAS char* pe0 = lds + K2_RE + (kh * 4 + g) * 256 + ((q4 ^ ((g & 1) << 2)) * 32) + p4 * 8;
    LAS char* pe1 = lds + K2_RE + (kh * 4 + g) * 256 + (((4 + q4) ^ ((g & 1) << 2)) * 32) + p4 * 8;
    auto rdB = [&](int sl, int nf, int t1) -> s16x4 {
        if (nf < 8) return tr_read(pb[sl >> 2][nf & 3] + (sl & 3) * 16384 + (nf >> 2) * 1024 + t1 * 512);
        return tr_read((t1 ? pe1 : pe0) + sl * 2048);
    };

    f32x4 acc[9][2];
#pragma unroll
    for (int n = 0; n < 9; ++n)
#pragma unroll
        for (int m = 0; m < 2; ++m) acc[n][m] = (f32x4){0.f, 0.f, 0.f, 0.f};

    WAIT_V(25); BAR();
    STAMP(2, 1);
    constexpr int PF2 = 4;
    bf16x8 fb[9];
#pragma unroll
    for (int i = 0; i < PF2; ++i) { s16x4 lo = rdB(0, i, 0), hi = rdB(0, i, 1); fb[i] = __builtin_shufflevector(lo, hi, 0, 1, 2, 3, 4, 5, 6, 7); }
#pragma unroll
    for (int t = 0; t < K2_NT; ++t) {
        const int sl = t & 7, sn = (t + 1) & 7;
        asm volatile("" : "+v"(fa[sl][0]), "+v"(fa[sl][1]));
        if (t == K2_NT - 1 && split != 0) {
#pragma unroll
            for (int m = 0; m < 2; ++m) fa[sl][m] = (bf16x8){0, 0, 0, 0, 0, 0, 0, 0};
        }
#pragma unroll
        for (int nf = 0; nf < 9; ++nf) {
            const int psl = (nf + PF2 < 9) ? sl : sn, nn = (nf + PF2 < 9) ? nf + PF2 : nf + PF2 - 9;
            const bool pf = (nf + PF2 < 9) || (t + 1 < K2_NT);
            s16x4 lo, hi;
            acc[nf][0] = MFMA16(fb[nf], fa[sl][0], acc[nf][0]);
            SCHED();
            if (nf == 0) {
                constexpr int dummy = 0; (void)dummy;
                const int last = (t + K2_LA - 1 < K2_NT - 1) ? t + K2_LA - 1 : K2_NT - 1;
                const int n = (last - (t + 1)) * 5;
                if (n >= 20) { WAIT_V(20); } else if (n == 15) { WAIT_V(15); } else if (n == 10) { WAIT_V(10); } else if (n == 5) { WAIT_V(5); } else { WAIT_V(0); }
                BAR();
            }
            if (pf) { lo = rdB(psl, nn, 0); hi = rdB(psl, nn, 1); }
            SCHED();
            acc[nf][1] = MFMA16(fb[nf], fa[sl][1], acc[nf][1]);
            SCHED();
            if (nf >= 1 && nf < 5 && t + K2_LA < K2_NT) issue1(t + K2_LA, nf - 1);
            SCHED();
            if (pf) fb[nn] = __builtin_shufflevector(lo, hi, 0, 1, 2, 3, 4, 5, 6, 7);
        }
    }
    BAR();
    STAMP(2, 2);
    {
        LAS char* xw = lds + w * 10240;
        LAS char* xr = lds + (w ^ 1) * 10240;
#pragma unroll
        for (int n = 0; n < 9; ++n) {
            const bool give = kh ? (n < 5) : (n >= 5);
            if (give) {
#pragma unroll
                for (int m = 0; m < 2; ++m) *(LAS f32x4*)(xw + ((kh ? n : n - 5) * 2 + m) * 1024 + lane * 16) = acc[n][m];
            }
        }
        asm volatile("s_waitcnt lgkmcnt(0)" ::: "memory");
        BAR();
        float* pp = part + (size_t)split * SEQ * DM;
#pragma unroll
        for (int n = 0; n < 9; ++n) {
            const bool keep = kh ? (n >= 5) : (n < 5);
            if (keep) {
#pragma unroll
                for (int m = 0; m < 2; ++m) {
                    const f32x4 o = *(LAS f32x4*)(xr + ((kh ? n - 5 : n) * 2 + m) * 1024 + lane * 16);
                    const int row = 128 * mt + 32 * wm + 16 * m + fr;
                    const int col = (n < 8 ? 128 * nt + 16 * n : 512 + 16 * nt) + 4 * g;
                    *(f32x4*)(pp + (size_t)row * DM + col) = acc[n][m] + o;
                }
            }
        }
    }
    STAMP(2, 15);
}

__global__ __launch_bounds__(256) void k3_reduce(const u16* __restrict__ x, const float* __restrict__ part, u16* __restrict__ out) {
    const int i = blockIdx.x * 256 + threadIdx.x;
    const size_t e = (size_t)i * 8;
    constexpr size_t PS = (size_t)SEQ * DM;
    const u32x4 xv = *(const u32x4*)(x + e);
    f32x4 a0 = *(const f32x4*)(part + e), a1 = *(const f32x4*)(part + e + 4);
#pragma unroll
    for (int s = 1; s < 4; ++s) { a0 += *(const f32x4*)(part + s * PS + e); a1 += *(const f32x4*)(part + s * PS + e + 4); }
    u32x4 o;
    o[0] = pk_bf16(a0[0] + bf_lo(xv[0]), a0[1] + bf_hi(xv[0]));
    o[1] = pk_bf16(a0[2] + bf_lo(xv[1]), a0[3] + bf_hi(xv[1]));
    o[2] = pk_bf16(a1[0] + bf_lo(xv[2]), a1[1] + bf_hi(xv[2]));
    o[3] = pk_bf16(a1[2] + bf_lo(xv[3]), a1[3] + bf_hi(xv[3]));
    *(u32x4*)(out + e) = o;
}

extern "C" void kernel_launch(void* const* d_in, const int* in_sizes, int n_in, void* d_out, int out_size, void* d_ws,
                              size_t ws_size, hipStream_t stream) {
    const u16* x = (const u16*)d_in[0];
    const u16* wqkv = (const u16*)d_in[1];
    const u16* wo = (const u16*)d_in[2];
    const u16* Wg = (const u16*)d_in[4];
    const u16* Wu = (const u16*)d_in[5];
    const u16* Wd = (const u16*)d_in[6];
    char* ws = (char*)d_ws;
    u16* xnb = (u16*)ws;
    u16* hb = (u16*)(ws + (4u << 20));
    float* part = (float*)(ws + (32u << 20));
    (void)hipFuncSetAttribute((const void*)k0_ln_qkv_attn, hipFuncAttributeMaxDynamicSharedMemorySize, K0_LDS);
    (void)hipFuncSetAttribute((const void*)k1_gateup, hipFuncAttributeMaxDynamicSharedMemorySize, K12_LDS);
    (void)hipFuncSetAttribute((const void*)k2_down, hipFuncAttributeMaxDynamicSharedMemorySize, K2_LDS);
    k0_ln_qkv_attn<<<256, 512, K0_LDS, stream>>>(x, wqkv, xnb, hb);
    k1_gateup<<<256, 512, K12_LDS, stream>>>(xnb, Wg, Wu, hb);
    k2_down<<<256, 512, K2_LDS, stream>>>(hb, Wd, wo, part);
    k3_reduce<<<SEQ * DM / 8 / 256, 256, 0, stream>>>(x, part, (u16*)d_out);
}
```

```cpp
#include <hip/hip_runtime.h>
#include <hip/hip_bf16.h>
#include <stdint.h>

typedef unsigned short u16;
typedef short bf16x8 __attribute__((ext_vector_type(8)));
typedef short s16x4 __attribute__((ext_vector_type(4)));
typedef float f32x4 __attribute__((ext_vector_type(4)));
typedef float f32x2 __attribute__((ext_vector_type(2)));
typedef __bf16 bf16x2_t __attribute__((ext_vector_type(2)));
typedef unsigned u32x4 __attribute__((ext_vector_type(4)));
typedef unsigned u32x2 __attribute__((ext_vector_type(2)));
#define LAS __attribute__((address_space(3)))
#define GAS __attribute__((address_space(1)))

constexpr int SEQ = 2048, DM = 576, FF = 4608;
constexpr int NKB_X = 18;
constexpr int NKB_H = 162;
constexpr size_t BLK_ELEMS = (size_t)SEQ * 32;

__device__ __forceinline__ unsigned pk_bf16(float lo, float hi) {
    f32x2 v = {lo, hi};
    bf16x2_t b = __builtin_convertvector(v, bf16x2_t);
    return __builtin_bit_cast(unsigned, b);
}
__device__ __forceinline__ float bf_lo(unsigned u) { return __uint_as_float(u << 16); }
__device__ __forceinline__ float bf_hi(unsigned u) { return __uint_as_float(u & 0xffff0000u); }
__device__ __forceinline__ float rbf(float x) { return bf_lo(pk_bf16(x, x)); }

__device__ __forceinline__ const void* uni(const void* p) {
    const unsigned long long v = (unsigned long long)p;
    const unsigned lo = __builtin_amdgcn_readfirstlane((unsigned)v), hi = __builtin_amdgcn_readfirstlane((unsigned)(v >> 32));
    return (const void*)(((unsigned long long)hi << 32) | lo);
}
__device__ __forceinline__ void glds16(const void* sbase, unsigned voff, unsigned lds) {
    sbase = uni(sbase); lds = __builtin_amdgcn_readfirstlane(lds);
    unsigned sv;
    asm volatile("s_mov_b32 %0, m0\n\ts_mov_b32 m0, %3\n\ts_nop 3\n\tglobal_load_lds_dwordx4 %1, %2\n\ts_mov_b32 m0, %0"
                 : "=&s"(sv) : "v"(voff), "s"(sbase), "s"(lds) : "memory");
}
__device__ __forceinline__ void glds16_q(const void* sbase, unsigned voff, unsigned lds) {
    sbase = uni(sbase); lds = __builtin_amdgcn_readfirstlane(lds);
    unsigned sv; unsigned long long ex;
    asm volatile("s_mov_b32 %0, m0\n\ts_mov_b64 %1, exec\n\ts_mov_b32 m0, %4\n\ts_mov_b64 exec, 0xffff\n\ts_nop 1\n\tglobal_load_lds_dwordx4 %2, %3\n\ts_mov_b64 exec, %1\n\ts_mov_b32 m0, %0"
                 : "=&s"(sv), "=&s"(ex) : "v"(voff), "s"(sbase), "s"(lds) : "memory");
}
#define SCHED() __builtin_amdgcn_sched_barrier(0)
#define STAMP(kid, i) do {} while (0)
#define WSTAMP(i) do {} while (0)
__device__ __forceinline__ s16x4 tr_read(LAS char* p) {
    return __builtin_bit_cast(s16x4, __builtin_amdgcn_ds_read_tr16_b64_v4i16((LAS s16x4*)p));
}
__device__ __forceinline__ bf16x8 tr_frag(LAS char* p, int off_t1) {
    s16x4 lo = tr_read(p), hi = tr_read(p + off_t1);
    return __builtin_shufflevector(lo, hi, 0, 1, 2, 3, 4, 5, 6, 7);
}
__device__ __forceinline__ bf16x8 row_frag(LAS char* p) { return *(LAS bf16x8*)p; }
#define MFMA16(a, b, c) __builtin_amdgcn_mfma_f32_16x16x32_bf16((a), (b), (c), 0, 0, 0)
#define WAIT_V(n) asm volatile("s_waitcnt vmcnt(" #n ")" ::: "memory")
#define BAR() do { asm volatile("" ::: "memory"); __builtin_amdgcn_s_barrier(); asm volatile("" ::: "memory"); } while (0)

__device__ __forceinline__ int blk_off(int row, int chunk) { return row * 64 + ((chunk ^ (((row >> 3) & 1) << 1)) << 4); }

__global__ __launch_bounds__(64) void k0_ln(const u16* __restrict__ x, u16* __restrict__ xnb) {
    const int lane = threadIdx.x, i = lane & 7, row = blockIdx.x * 8 + (lane >> 3);
    u32x4 xv[9];
#pragma unroll
    for (int cc = 0; cc < 9; ++cc) xv[cc] = *(const u32x4*)(x + (size_t)row * DM + (i + 8 * cc) * 8);
    float s = 0.f;
#pragma unroll
    for (int cc = 0; cc < 9; ++cc)
#pragma unroll
        for (int e = 0; e < 4; ++e) s += bf_lo(xv[cc][e]) + bf_hi(xv[cc][e]);
    s += __shfl_xor(s, 1); s += __shfl_xor(s, 2); s += __shfl_xor(s, 4);
    const float mean = s * (1.0f / 576.0f);
    float q = 0.f;
#pragma unroll
    for (int cc = 0; cc < 9; ++cc)
#pragma unroll
        for (int e = 0; e < 4; ++e) { const float a = bf_lo(xv[cc][e]) - mean, c2 = bf_hi(xv[cc][e]) - mean; q += a * a + c2 * c2; }
    q += __shfl_xor(q, 1); q += __shfl_xor(q, 2); q += __shfl_xor(q, 4);
    const float mb = rbf(mean), vb = rbf(q * (1.0f / 576.0f));
    const float sb = rbf(sqrtf(rbf(vb + 1e-5f)));
    const float rs = 1.0f / sb;
#pragma unroll
    for (int cc = 0; cc < 9; ++cc) {
        u32x4 o;
#pragma unroll
        for (int e = 0; e < 4; ++e) {
            const unsigned d = pk_bf16(bf_lo(xv[cc][e]) - mb, bf_hi(xv[cc][e]) - mb);
            o[e] = pk_bf16(bf_lo(d) * rs, bf_hi(d) * rs);
        }
        const int blk = 2 * cc + (i >> 2);
        *(u32x4*)((char*)xnb + (size_t)blk * (BLK_ELEMS * 2) + blk_off(row, i & 3)) = o;
    }
}

constexpr int RA = 0, RB = 65536, RE = 131072;
constexpr int T_A_BYTES = NKB_X * 80 * 64;
constexpr int T_SLOT = 16384;
constexpr int T_DUMMY = T_A_BYTES + 4 * T_SLOT;
constexpr int K1_LDS = T_DUMMY + 1024;
constexpr int QS = 456;
constexpr int PF = 3;

__global__ __launch_bounds__(512, 2) void k1_gateup(const u16* __restrict__ xnb, const u16* __restrict__ Wg,
                                                    const u16* __restrict__ Wu, const u16* __restrict__ wqkv,
                                                    const u16* __restrict__ Wd, u16* __restrict__ hb) {
    extern __shared__ __attribute__((aligned(16))) char smem[];
    LAS char* lds = (LAS char*)smem;
    const int tid = threadIdx.x, lane = tid & 63;
    const int w = __builtin_amdgcn_readfirstlane(tid >> 6);
    const int b = blockIdx.x, xcd = b & 7, idx = b >> 3;
    const int j = 4 * xcd + (idx & 3), mt = idx >> 2;
    const int wm = w >> 1, wn = w & 1;
    const unsigned lds0 = (unsigned)(size_t)lds;
    const int fr = lane & 15, g = lane >> 4, q4 = (lane >> 2) & 3, p4 = lane & 3;

    f32x4 acc[9][4];
    {
        const unsigned srcA = (unsigned)((256 * mt + 32 * w) * 32 + lane * 8) * 2u;
        const int koct = w >> 1, mat = w & 1;
        const u16* W = mat ? Wu : Wg;
        unsigned srcB, srcE;
        {
            const int r = lane >> 3, swz = ((r >> 1) & 1) | ((koct & 1) << 1), c = (lane & 7) ^ (swz << 1);
            srcB = (unsigned)((8 * koct + r) * FF + 128 * j + 8 * c) * 2u;
            const int re = ((lane >> 1) & 7) ^ ((koct & 1) << 2), ce = (lane & 1) ^ mat;
            srcE = (unsigned)((8 * koct + re) * FF + 4096 + 16 * j + 8 * ce) * 2u;
        }
        const unsigned srcA1 = srcA + 1024, srcB1 = srcB + 128;
        const unsigned dA = lds0 + RA + (2 * w) * 1024, dB = lds0 + RB + (2 * w) * 1024, dE = lds0 + RE + (mat * 4 + koct) * 256;
        auto issue1 = [&](int t, int i) {
            const int sl = t & 3;
            const u16* ba = xnb + (size_t)t * BLK_ELEMS;
            const u16* bw = W + (size_t)t * 32 * FF;
            if (i == 0) glds16(bw, srcB, dB + sl * 16384);
            if (i == 1) glds16(bw, srcB1, dB + sl * 16384 + 1024);
            if (i == 2) glds16_q(bw, srcE, dE + sl * 2048);
            if (i == 3) glds16(ba, srcA, dA + sl * 16384);
            if (i == 4) glds16(ba, srcA1, dA + sl * 16384 + 1024);
        };
        STAMP(1, 0);
#pragma unroll
        for (int t = 0; t < 3; ++t)
#pragma unroll
            for (int i = 0; i < 5; ++i) issue1(t, i);

        LAS char* pa = lds + RA + blk_off(64 * wm + fr, g);
        const int swz = ((q4 >> 1) & 1) | ((g & 1) << 1);
        LAS char* pb[4];
#pragma unroll
        for (int gt = 0; gt < 4; ++gt) pb[gt] = lds + RB + (g * 4 + wn) * 1024 + q4 * 128 + ((gt ^ swz) << 5) + p4 * 8;
        const int emat = p4 >> 1;
        LAS char* pe0 = lds + RE + (emat * 4 + g) * 256 + ((q4 ^ ((g & 1) << 2)) * 32) + ((wn ^ emat) * 16) + (p4 & 1) * 8;
        LAS char* pe1 = lds + RE + (emat * 4 + g) * 256 + (((4 + q4) ^ ((g & 1) << 2)) * 32) + ((wn ^ emat) * 16) + (p4 & 1) * 8;
        auto rdB = [&](int sl, int nf, int t1) -> s16x4 {
            if (nf < 8) return tr_read(pb[nf >> 1] + sl * 16384 + (nf & 1) * 2048 + t1 * 512);
            return tr_read((t1 ? pe1 : pe0) + sl * 2048);
        };
#pragma unroll
        for (int n = 0; n < 9; ++n)
#pragma unroll
            for (int m = 0; m < 4; ++m) acc[n][m] = (f32x4){0.f, 0.f, 0.f, 0.f};

        WAIT_V(10); BAR();
        STAMP(1, 1);
        bf16x8 fa[2][4], fb[9];
#pragma unroll
        for (int m = 0; m < 4; ++m) fa[0][m] = row_frag(pa + m * 1024);
#pragma unroll
        for (int i = 0; i < PF; ++i) { s16x4 lo = rdB(0, i, 0), hi = rdB(0, i, 1); fb[i] = __builtin_shufflevector(lo, hi, 0, 1, 2, 3, 4, 5, 6, 7); }
#pragma unroll
        for (int t = 0; t < NKB_X; ++t) {
            const int sl = t & 3, sn = (t + 1) & 3, cur = t & 1;
#pragma unroll
            for (int nf = 0; nf < 9; ++nf) {
                const int psl = (nf + PF < 9) ? sl : sn, nn = (nf + PF < 9) ? nf + PF : nf + PF - 9;
                const bool pf = (nf + PF < 9) || (t + 1 < NKB_X);
                s16x4 lo, hi;
                acc[nf][0] = MFMA16(fb[nf], fa[cur][0], acc[nf][0]);
                SCHED();
                if (nf == 0) {
                    if (t + 2 < NKB_X) { WAIT_V(5); } else { WAIT_V(0); }
                    BAR();
                }
                if (pf) lo = rdB(psl, nn, 0);
                SCHED();
                acc[nf][1] = MFMA16(fb[nf], fa[cur][1], acc[nf][1]);
                SCHED();
                if (pf) hi = rdB(psl, nn, 1);
                SCHED();
                acc[nf][2] = MFMA16(fb[nf], fa[cur][2], acc[nf][2]);
                SCHED();
                if (nf >= 3 && nf < 7 && t + 1 < NKB_X) fa[cur ^ 1][nf - 3] = row_frag(pa + sn * 16384 + (nf - 3) * 1024);
                if ((nf < 3 || nf >= 7) && t + 3 < NKB_X) issue1(t + 3, nf < 3 ? nf : nf - 4);
                SCHED();
                acc[nf][3] = MFMA16(fb[nf], fa[cur][3], acc[nf][3]);
                SCHED();
                if (pf) fb[nn] = __builtin_shufflevector(lo, hi, 0, 1, 2, 3, 4, 5, 6, 7);
            }
        }
    }
    BAR();
    STAMP(1, 2);

    const int hg = xcd, mt2 = idx;
    const int row0 = 64 * mt2 - 16;
    unsigned srcQ[2];
#pragma unroll
    for (int p = 0; p < 2; ++p) {
        const int r = 2 * (2 * w + p) + (lane >> 5);
        const int swz3 = (r & 3) | (((r >> 3) & 1) << 2);
        int c = (lane & 31) ^ (swz3 << 1);
        c = c > 26 ? 26 : c;
        const int part = c / 9, cc = c - part * 9;
        srcQ[p] = (unsigned)(r * (3 * DM) + part * DM + 72 * hg + 8 * cc) * 2u;
    }
    auto issueQ = [&](int t) {
        const unsigned s = lds0 + T_A_BYTES + (t & 3) * T_SLOT + (2 * w) * 1024;
        const u16* base = wqkv + (size_t)t * 32 * (3 * DM);
        glds16(base, srcQ[0], s);
        glds16(base, srcQ[1], s + 1024);
    };
    {
#pragma unroll
        for (int i = 0; i < 12; ++i) {
            int p = w + 8 * i; p = p < 90 ? p : 89;
            const int blk = p / 5, r16 = p - blk * 5;
            int grow = row0 + 16 * r16; grow = grow < 0 ? 0 : grow;
            glds16(xnb + (size_t)blk * BLK_ELEMS + (size_t)grow * 32, (unsigned)lane * 16u, lds0 + p * 1024);
        }
        issueQ(0); issueQ(1); issueQ(2); issueQ(3);
    }
    {
        const __amdgpu_buffer_rsrc_t rh = __builtin_amdgcn_make_buffer_rsrc((void*)hb, 0, (int)(NKB_H * BLK_ELEMS * 2), 0x00020000);
#pragma unroll
        for (int m = 0; m < 4; ++m) {
            const int row = 256 * mt + 64 * wm + 16 * m + fr;
#pragma unroll
            for (int pp = 0; pp < 2; ++pp) {
                unsigned v[2][2];
#pragma unroll
                for (int hh = 0; hh < 2; ++hh) {
                    const int gt = 2 * pp + hh;
                    float hv[4];
#pragma unroll
                    for (int e = 0; e < 4; ++e) { const float gg = rbf(acc[2 * gt][m][e]), uu = rbf(acc[2 * gt + 1][m][e]); hv[e] = (gg > 0.f ? gg : 0.f) * uu; }
                    v[hh][0] = pk_bf16(hv[0], hv[1]); v[hh][1] = pk_bf16(hv[2], hv[3]);
                }
                auto r0 = __builtin_amdgcn_permlane16_swap(v[0][0], v[1][0], false, false);
                auto r1 = __builtin_amdgcn_permlane16_swap(v[0][1], v[1][1], false, false);
                u32x4 o; o[0] = r0[0]; o[1] = r1[0]; o[2] = r0[1]; o[3] = r1[1];
                const int blk = 4 * j + 2 * wn + pp, chunk = ((g & 1) << 1) | (g >> 1);
                __builtin_amdgcn_raw_buffer_store_b128(o, rh, (unsigned)(blk * (int)(BLK_ELEMS * 2) + blk_off(row, chunk)), 0, 16);
            }
            {
                float hv[4];
#pragma unroll
                for (int e = 0; e < 4; ++e) {
                    const unsigned mine = __float_as_uint(rbf(acc[8][m][e]));
                    auto r = __builtin_amdgcn_permlane32_swap(mine, mine, false, false);
                    const float gg = __uint_as_float(r[0]), uu = __uint_as_float(r[1]);
                    hv[e] = (gg > 0.f ? gg : 0.f) * uu;
                }
                u32x2 v; v[0] = pk_bf16(hv[0], hv[1]); v[1] = pk_bf16(hv[2], hv[3]);
                const int blk = 128 + (j >> 1), chunk = (j & 1) * 2 + wn;
                const unsigned off = g < 2 ? (unsigned)(blk * (int)(BLK_ELEMS * 2) + blk_off(row, chunk) + g * 8) : 0xfffffff0u;
                __builtin_amdgcn_raw_buffer_store_b64(v, rh, off, 0, 16);
            }
        }
    }
    STAMP(1, 3);
    LAS char* ldsB = lds + T_A_BYTES;
    f32x4 ac[2][5];
    {
        const int aoff = blk_off(fr, g);
        const int swz3 = q4 | ((g & 1) << 2);
        const int boff0 = (8 * g + q4) * 512 + ((w ^ swz3) << 5) + p4 * 8;
        const int boff1 = (8 * g + q4) * 512 + (((w + 8) ^ swz3) << 5) + p4 * 8;
#pragma unroll
        for (int n = 0; n < 2; ++n)
#pragma unroll
            for (int m = 0; m < 5; ++m) ac[n][m] = (f32x4){0.f, 0.f, 0.f, 0.f};
#pragma unroll
        for (int tt = 0; tt < NKB_X; tt += 2) {
            if (tt == 0) { WAIT_V(16); } else if (tt == 2) { WAIT_V(12); } else { WAIT_V(0); }
            BAR();
            if (tt >= 2 && tt + 2 < NKB_X) { issueQ(tt + 2); issueQ(tt + 3); }
#pragma unroll
            for (int t = tt; t < tt + 2; ++t) {
                LAS char* sb = ldsB + (t & 3) * T_SLOT;
                LAS char* sa = lds + t * 5120 + aoff;
                bf16x8 b0 = tr_frag(sb + boff0, 2048);
                bf16x8 b1;
                if (w < 6) b1 = tr_frag(sb + boff1, 2048);
#pragma unroll
                for (int m = 0; m < 5; ++m) {
                    bf16x8 a = row_frag(sa + m * 1024);
                    ac[0][m] = MFMA16(b0, a, ac[0][m]);
                    if (w < 6) ac[1][m] = MFMA16(b1, a, ac[1][m]);
                }
            }
        }
    }
    BAR();
    STAMP(1, 4);
    {
        const int split = xcd >> 1;
        const size_t base = (size_t)split * 1152 * DM * 2 + (size_t)(idx * 8 + w) * 6144;
#pragma unroll
        for (int i = 0; i < 6; ++i) {
            size_t o = base + i * 1024; o = o + 1024 <= (size_t)FF * DM * 2 ? o : 0;
            glds16((const char*)Wd + o, (unsigned)lane * 16u, lds0 + T_DUMMY);
        }
    }
    LAS char* qt = ldsB;
#pragma unroll
    for (int n = 0; n < 2; ++n) {
        if (n == 0 || w < 6) {
#pragma unroll
            for (int m = 0; m < 5; ++m) {
                u32x2 v; v[0] = pk_bf16(ac[n][m][0], ac[n][m][1]); v[1] = pk_bf16(ac[n][m][2], ac[n][m][3]);
                *(LAS u32x2*)(qt + (16 * m + fr) * QS + (16 * (w + 8 * n) + 4 * g) * 2) = v;
            }
        }
    }
    asm volatile("s_waitcnt lgkmcnt(0)" ::: "memory");
    BAR();
    {
        const int item = tid >> 1, half = tid & 1;
        const int hd = item & 3, s = item >> 2;
        const int lrow = 16 + s, grow = 64 * mt2 + s;
        LAS const unsigned* qp = (LAS const unsigned*)(qt + lrow * QS + hd * 36);
        unsigned qv[9];
#pragma unroll
        for (int i = 0; i < 9; ++i) qv[i] = qp[i];
        float lg[4];
#pragma unroll
        for (int ww = 0; ww < 4; ++ww) {
            const int wi = 4 * half + ww;
            LAS const unsigned* kp = (LAS const unsigned*)(qt + (lrow - 2 * wi) * QS + 144 + hd * 36);
            float a = 0.f;
#pragma unroll
            for (int i = 0; i < 9; ++i) { const unsigned kv = kp[i]; a += bf_lo(qv[i]) * bf_lo(kv); a += bf_hi(qv[i]) * bf_hi(kv); }
            a = rbf(rbf(a) * 0.23570226039551584f);
            lg[ww] = (grow - 2 * wi >= 0) ? a : -1e9f;
        }
        float mx = fmaxf(fmaxf(lg[0], lg[1]), fmaxf(lg[2], lg[3]));
        mx = fmaxf(mx, __shfl_xor(mx, 1));
        float ex[4], sum = 0.f;
#pragma unroll
        for (int ww = 0; ww < 4; ++ww) { ex[ww] = __expf(lg[ww] - mx); sum += ex[ww]; }
        sum += __shfl_xor(sum, 1);
        const float inv = 1.0f / sum;
        float pr[8];
#pragma unroll
        for (int ww = 0; ww < 4; ++ww) {
            const float mine = rbf(ex[ww] * inv), other = __shfl_xor(mine, 1);
            pr[ww] = half ? other : mine; pr[4 + ww] = half ? mine : other;
        }
#pragma unroll
        for (int wi = 0; wi < 8; ++wi) pr[wi] = (grow - 2 * wi >= 0) ? pr[wi] : 0.f;
        const int i0 = half * 5;
#pragma unroll
        for (int ii = 0; ii < 5; ++ii) {
            const int i = i0 + ii;
            if (i < 9) {
                float o0 = 0.f, o1 = 0.f;
#pragma unroll
                for (int wi = 0; wi < 8; ++wi) {
                    const unsigned vv = *(LAS const unsigned*)(qt + (lrow - 2 * wi) * QS + 288 + hd * 36 + 4 * i);
                    o0 += pr[wi] * bf_lo(vv); o1 += pr[wi] * bf_hi(vv);
                }
                const int col = FF + 72 * hg + 18 * hd + 2 * i;
                *(unsigned*)((char*)hb + (size_t)(col >> 5) * (BLK_ELEMS * 2) + blk_off(grow, (col >> 3) & 3) + (col & 7) * 2) = pk_bf16(o0, o1);
            }
        }
    }
    STAMP(1, 15);
}

constexpr int K2_NT = 21, K2_LA = 5;
constexpr int K2_RB = 0, K2_RE = 131072, K2_LDS = 131072 + 16384;
constexpr int NSPLIT = 4;
__device__ __forceinline__ void gload2(bf16x8& d0, bf16x8& d1, const void* sbase, unsigned voff) {
    sbase = uni(sbase);
    asm volatile("s_nop 4\n\tglobal_load_dwordx4 %0, %2, %3\n\tglobal_load_dwordx4 %1, %2, %3 offset:1024"
                 : "=&v"(d0), "=&v"(d1) : "v"(voff), "s"(sbase) : "memory");
}
__global__ __launch_bounds__(512, 2) void k2_down(const u16* __restrict__ hb, const u16* __restrict__ Wd,
                                                  const u16* __restrict__ wo, u16* __restrict__ part) {
    extern __shared__ __attribute__((aligned(16))) char smem[];
    LAS char* lds = (LAS char*)smem;
    const int tid = threadIdx.x, lane = tid & 63;
    const int w = __builtin_amdgcn_readfirstlane(tid >> 6);
    const int b = blockIdx.x, xcd = b & 7, idx = b >> 3;
    const int split = xcd >> 1, mt = (xcd & 1) * 8 + (idx >> 2), nt = idx & 3;
    const int wm = w >> 1, kh = w & 1;
    const int T0 = split == 0 ? 0 : 21 + 20 * (split - 1);

    const unsigned srcA = (unsigned)(kh * (int)BLK_ELEMS * 2 + blk_off(128 * mt + 32 * wm + (lane & 15), lane >> 4));
    const int koct = w >> 1, sub = w & 1;
    unsigned offB, offE;
    {
        const int r = lane >> 3, swz = ((r >> 1) & 1) | ((koct & 1) << 1), c = (lane & 7) ^ (swz << 1);
        offB = (unsigned)((32 * sub + 8 * koct + r) * DM + 128 * nt + 8 * c) * 2u;
        const int re = ((lane >> 1) & 7) ^ ((koct & 1) << 2), ce = (lane & 1);
        offE = (unsigned)((32 * sub + 8 * koct + re) * DM + 512 + 16 * nt + 8 * ce) * 2u;
    }
    const unsigned offB1 = offB + 128;
    const unsigned lds0 = (unsigned)(size_t)lds;
    const unsigned dB = lds0 + K2_RB + (2 * w) * 1024, dE = lds0 + K2_RE + (sub * 4 + koct) * 256;
    bf16x8 fa[8][2];
    auto issue1 = [&](int t, int i) {
        const int sl = t & 7;
        int T = T0 + t; if (t == K2_NT - 1 && split != 0) T -= 1;
        const u16* Wt = T < 72 ? Wd + (size_t)T * 64 * DM : wo + (size_t)(T - 72) * 64 * DM;
        const u16* ba = hb + (size_t)T * 2 * BLK_ELEMS;
        if (i == 0) glds16(Wt, offB, dB + sl * 16384);
        if (i == 1) glds16(Wt, offB1, dB + sl * 16384 + 1024);
        if (i == 2) glds16_q(Wt, offE, dE + sl * 2048);
        if (i == 3) gload2(fa[t & 7][0], fa[t & 7][1], ba, srcA);
    };
    STAMP(2, 0);
#pragma unroll
    for (int t = 0; t < K2_LA; ++t)
#pragma unroll
        for (int i = 0; i < 4; ++i) issue1(t, i);

    const int fr = lane & 15, g = lane >> 4, q4 = (lane >> 2) & 3, p4 = lane & 3;
    const int swz = ((q4 >> 1) & 1) | ((g & 1) << 1);
    LAS char* pb[2][4];
#pragma unroll
    for (int u = 0; u < 4; ++u) { pb[0][u] = lds + K2_RB + (g * 4 + kh * 2) * 1024 + q4 * 128 + ((u ^ swz) << 5) + p4 * 8; pb[1][u] = pb[0][u] + 65536; }
    LAS char* pe0 = lds + K2_RE + (kh * 4 + g) * 256 + ((q4 ^ ((g & 1) << 2)) * 32) + p4 * 8;
    LAS char* pe1 = lds + K2_RE + (kh * 4 + g) * 256 + (((4 + q4) ^ ((g & 1) << 2)) * 32) + p4 * 8;
    auto rdB = [&](int sl, int nf, int t1) -> s16x4 {
        if (nf < 8) return tr_read(pb[sl >> 2][nf & 3] + (sl & 3) * 16384 + (nf >> 2) * 1024 + t1 * 512);
        return tr_read((t1 ? pe1 : pe0) + sl * 2048);
    };

    f32x4 acc[9][2];
#pragma unroll
    for (int n = 0; n < 9; ++n)
#pragma unroll
        for (int m = 0; m < 2; ++m) acc[n][m] = (f32x4){0.f, 0.f, 0.f, 0.f};

    WAIT_V(20); BAR();
    STAMP(2, 1);
    constexpr int PF2 = 4;
    bf16x8 fb[9];
#pragma unroll
    for (int i = 0; i < PF2; ++i) { s16x4 lo = rdB(0, i, 0), hi = rdB(0, i, 1); fb[i] = __builtin_shufflevector(lo, hi, 0, 1, 2, 3, 4, 5, 6, 7); }
#pragma unroll
    for (int t = 0; t < K2_NT; ++t) {
        const int sl = t & 7, sn = (t + 1) & 7;
        asm volatile("" : "+v"(fa[sl][0]), "+v"(fa[sl][1]));
        if (t == K2_NT - 1 && split != 0) {
#pragma unroll
            for (int m = 0; m < 2; ++m) fa[sl][m] = (bf16x8){0, 0, 0, 0, 0, 0, 0, 0};
        }
#pragma unroll
        for (int nf = 0; nf < 9; ++nf) {
            const int psl = (nf + PF2 < 9) ? sl : sn, nn = (nf + PF2 < 9) ? nf + PF2 : nf + PF2 - 9;
            const bool pf = (nf + PF2 < 9) || (t + 1 < K2_NT);
            s16x4 lo, hi;
            acc[nf][0] = MFMA16(fb[nf], fa[sl][0], acc[nf][0]);
            SCHED();
            if (nf == 0 && (t & 1) == 0) {
                const int last = (t + K2_LA - 1 < K2_NT - 1) ? t + K2_LA - 1 : K2_NT - 1;
                const int n = last - (t + 2) > 0 ? (last - (t + 2)) * 5 : 0;
                if (n == 10) { WAIT_V(10); } else if (n == 5) { WAIT_V(5); } else { WAIT_V(0); }
                BAR();
            }
            if (pf) lo = rdB(psl, nn, 0);
            SCHED();
            acc[nf][1] = MFMA16(fb[nf], fa[sl][1], acc[nf][1]);
            SCHED();
            if (pf) hi = rdB(psl, nn, 1);
            if (nf >= 1 && nf < 5 && t + K2_LA < K2_NT) issue1(t + K2_LA, nf - 1);
            SCHED();
            if (pf) fb[nn] = __builtin_shufflevector(lo, hi, 0, 1, 2, 3, 4, 5, 6, 7);
        }
    }
    BAR();
    STAMP(2, 2);
    {
        LAS char* xw = lds + w * 10240;
        LAS char* xr = lds + (w ^ 1) * 10240;
#pragma unroll
        for (int n = 0; n < 9; ++n) {
            const bool give = kh ? (n < 5) : (n >= 5);
            if (give) {
#pragma unroll
                for (int m = 0; m < 2; ++m) *(LAS f32x4*)(xw + ((kh ? n : n - 5) * 2 + m) * 1024 + lane * 16) = acc[n][m];
            }
        }
        asm volatile("s_waitcnt lgkmcnt(0)" ::: "memory");
        BAR();
        u16* pp = part + (size_t)split * SEQ * DM;
#pragma unroll
        for (int n = 0; n < 9; ++n) {
            const bool keep = kh ? (n >= 5) : (n < 5);
            if (keep) {
#pragma unroll
                for (int m = 0; m < 2; ++m) {
                    const f32x4 o = *(LAS f32x4*)(xr + ((kh ? n - 5 : n) * 2 + m) * 1024 + lane * 16) + acc[n][m];
                    const int row = 128 * mt + 32 * wm + 16 * m + fr;
                    const int col = (n < 8 ? 128 * nt + 16 * n : 512 + 16 * nt) + 4 * g;
                    u32x2 v; v[0] = pk_bf16(o[0], o[1]); v[1] = pk_bf16(o[2], o[3]);
                    *(u32x2*)(pp + (size_t)row * DM + col) = v;
                }
            }
        }
    }
    STAMP(2, 15);
}

__global__ __launch_bounds__(256) void k3_reduce(const u16* __restrict__ x, const u16* __restrict__ part, u16* __restrict__ out) {
    const int i = blockIdx.x * 256 + threadIdx.x;
    const size_t e = (size_t)i * 8;
    constexpr size_t PS = (size_t)SEQ * DM;
    const u32x4 xv = *(const u32x4*)(x + e);
    u32x4 pv[NSPLIT];
#pragma unroll
    for (int s = 0; s < NSPLIT; ++s) pv[s] = *(const u32x4*)(part + s * PS + e);
    u32x4 o;
#pragma unroll
    for (int k = 0; k < 4; ++k) {
        float lo = 0.f, hi = 0.f;
#pragma unroll
        for (int s = 0; s < NSPLIT; ++s) { lo += bf_lo(pv[s][k]); hi += bf_hi(pv[s][k]); }
        o[k] = pk_bf16(lo + bf_lo(xv[k]), hi + bf_hi(xv[k]));
    }
    *(u32x4*)(out + e) = o;
}

extern "C" void kernel_launch(void* const* d_in, const int* in_sizes, int n_in, void* d_out, int out_size, void* d_ws,
                              size_t ws_size, hipStream_t stream) {
    const u16* x = (const u16*)d_in[0];
    const u16* wqkv = (const u16*)d_in[1];
    const u16* wo = (const u16*)d_in[2];
    const u16* Wg = (const u16*)d_in[4];
    const u16* Wu = (const u16*)d_in[5];
    const u16* Wd = (const u16*)d_in[6];
    char* ws = (char*)d_ws;
    u16* xnb = (u16*)ws;
    u16* hb = (u16*)(ws + (4u << 20));
    u16* part = (u16*)(ws + (32u << 20));
    (void)hipFuncSetAttribute((const void*)k1_gateup, hipFuncAttributeMaxDynamicSharedMemorySize, K1_LDS);
    (void)hipFuncSetAttribute((const void*)k2_down, hipFuncAttributeMaxDynamicSharedMemorySize, K2_LDS);
    k0_ln<<<256, 64, 0, stream>>>(x, xnb);
    k1_gateup<<<256, 512, K1_LDS, stream>>>(xnb, Wg, Wu, wqkv, Wd, hb);
    k2_down<<<256, 512, K2_LDS, stream>>>(hb, Wd, wo, part);
    k3_reduce<<<SEQ * DM / 8 / 256, 256, 0, stream>>>(x, part, (u16*)d_out);
}
```

```cpp
#include <hip/hip_runtime.h>
#include <hip/hip_bf16.h>
#include <stdint.h>

typedef unsigned short u16;
typedef short bf16x8 __attribute__((ext_vector_type(8)));
typedef short s16x4 __attribute__((ext_vector_type(4)));
typedef float f32x4 __attribute__((ext_vector_type(4)));
typedef float f32x2 __attribute__((ext_vector_type(2)));
typedef __bf16 bf16x2_t __attribute__((ext_vector_type(2)));
typedef unsigned u32x4 __attribute__((ext_vector_type(4)));
typedef unsigned u32x2 __attribute__((ext_vector_type(2)));
#define LAS __attribute__((address_space(3)))
#define GAS __attribute__((address_space(1)))

constexpr int SEQ = 2048, DM = 576, FF = 4608;
constexpr int NKB_X = 18;
constexpr int NKB_H = 162;
constexpr size_t BLK_ELEMS = (size_t)SEQ * 32;

__device__ __forceinline__ unsigned pk_bf16(float lo, float hi) {
    f32x2 v = {lo, hi};
    bf16x2_t b = __builtin_convertvector(v, bf16x2_t);
    return __builtin_bit_cast(unsigned, b);
}
__device__ __forceinline__ float bf_lo(unsigned u) { return __uint_as_float(u << 16); }
__device__ __forceinline__ float bf_hi(unsigned u) { return __uint_as_float(u & 0xffff0000u); }
__device__ __forceinline__ float rbf(float x) { return bf_lo(pk_bf16(x, x)); }

__device__ __forceinline__ const void* uni(const void* p) {
    const unsigned long long v = (unsigned long long)p;
    const unsigned lo = __builtin_amdgcn_readfirstlane((unsigned)v), hi = __builtin_amdgcn_readfirstlane((unsigned)(v >> 32));
    return (const void*)(((unsigned long long)hi << 32) | lo);
}
__device__ __forceinline__ void glds16(const void* sbase, unsigned voff, unsigned lds) {
    sbase = uni(sbase); lds = __builtin_amdgcn_readfirstlane(lds);
    unsigned sv;
    asm volatile("s_mov_b32 %0, m0\n\ts_mov_b32 m0, %3\n\ts_nop 3\n\tglobal_load_lds_dwordx4 %1, %2\n\ts_mov_b32 m0, %0"
                 : "=&s"(sv) : "v"(voff), "s"(sbase), "s"(lds) : "memory");
}
__device__ __forceinline__ void glds16_o1k(const void* sbase, unsigned voff, unsigned lds) {
    sbase = uni(sbase); lds = __builtin_amdgcn_readfirstlane(lds);
    unsigned sv;
    asm volatile("s_mov_b32 %0, m0\n\ts_mov_b32 m0, %3\n\ts_nop 3\n\tglobal_load_lds_dwordx4 %1, %2 offset:1024\n\ts_mov_b32 m0, %0"
                 : "=&s"(sv) : "v"(voff), "s"(sbase), "s"(lds) : "memory");
}
__device__ __forceinline__ void glds16_q(const void* sbase, unsigned voff, unsigned lds) {
    sbase = uni(sbase); lds = __builtin_amdgcn_readfirstlane(lds);
    unsigned sv; unsigned long long ex;
    asm volatile("s_mov_b32 %0, m0\n\ts_mov_b64 %1, exec\n\ts_mov_b32 m0, %4\n\ts_mov_b64 exec, 0xffff\n\ts_nop 1\n\tglobal_load_lds_dwordx4 %2, %3\n\ts_mov_b64 exec, %1\n\ts_mov_b32 m0, %0"
                 : "=&s"(sv), "=&s"(ex) : "v"(voff), "s"(sbase), "s"(lds) : "memory");
}
__device__ __forceinline__ void glds16_m(const void* sbase, unsigned voff, unsigned lds) {
    sbase = uni(sbase); lds = __builtin_amdgcn_readfirstlane(lds);
    unsigned sv; unsigned long long ex;
    asm volatile("s_mov_b32 %0, m0\n\ts_mov_b64 %1, exec\n\ts_mov_b32 m0, %4\n\ts_mov_b32 exec_lo, -1\n\ts_mov_b32 exec_hi, 0xff\n\ts_nop 1\n\tglobal_load_lds_dwordx4 %2, %3\n\ts_mov_b64 exec, %1\n\ts_mov_b32 m0, %0"
                 : "=&s"(sv), "=&s"(ex) : "v"(voff), "s"(sbase), "s"(lds) : "memory");
}
#define SCHED() __builtin_amdgcn_sched_barrier(0)
#define STAMP(kid, i) do {} while (0)
#define WSTAMP(i) do {} while (0)
__device__ __forceinline__ s16x4 tr_read(LAS char* p) {
    return __builtin_bit_cast(s16x4, __builtin_amdgcn_ds_read_tr16_b64_v4i16((LAS s16x4*)p));
}
__device__ __forceinline__ bf16x8 tr_frag(LAS char* p, int off_t1) {
    s16x4 lo = tr_read(p), hi = tr_read(p + off_t1);
    return __builtin_shufflevector(lo, hi, 0, 1, 2, 3, 4, 5, 6, 7);
}
__device__ __forceinline__ bf16x8 row_frag(LAS char* p) { return *(LAS bf16x8*)p; }
#define MFMA16(a, b, c) __builtin_amdgcn_mfma_f32_16x16x32_bf16((a), (b), (c), 0, 0, 0)
__device__ __forceinline__ void mfma_ip(f32x4& c, bf16x8 a, bf16x8 b) { asm volatile("v_mfma_f32_16x16x32_bf16 %0, %1, %2, %0" : "+v"(c) : "v"(a), "v"(b)); }
#define WAIT_V(n) asm volatile("s_waitcnt vmcnt(" #n ")" ::: "memory")
#define BAR() do { asm volatile("" ::: "memory"); __builtin_amdgcn_s_barrier(); asm volatile("" ::: "memory"); } while (0)

__device__ __forceinline__ int blk_off(int row, int chunk) { return row * 64 + ((chunk ^ (((row >> 3) & 1) << 1)) << 4); }

__global__ __launch_bounds__(128) void k0_ln(const u16* __restrict__ x, u16* __restrict__ xnb,
                                            const u16* __restrict__ wqkv, u16* __restrict__ wqh) {
    if (threadIdx.x >= 64) {
        const int l = threadIdx.x - 64;
#pragma unroll 4
        for (int i = 0; i < 36; ++i) {
            const int id = l + 64 * i, r = blockIdx.x * 72 + (id >> 5), cp = id & 31;
            const int head = r / 576, k = r - head * 576;
            unsigned v = 0u;
            if (cp < 27) {
                const int part = cp / 9, d2 = cp - part * 9;
                const int src = (part == 2 ? 0 : part + 1) * DM + 18 * head + 2 * d2;
                v = *(const unsigned*)(wqkv + (size_t)k * (3 * DM) + src);
            }
            *(unsigned*)(wqh + (size_t)r * 64 + 2 * cp) = v;
        }
        return;
    }
    const int lane = threadIdx.x, i = lane & 7, row = blockIdx.x * 8 + (lane >> 3);
    u32x4 xv[9];
#pragma unroll
    for (int cc = 0; cc < 9; ++cc) xv[cc] = *(const u32x4*)(x + (size_t)row * DM + (i + 8 * cc) * 8);
    float s = 0.f;
#pragma unroll
    for (int cc = 0; cc < 9; ++cc)
#pragma unroll
        for (int e = 0; e < 4; ++e) s += bf_lo(xv[cc][e]) + bf_hi(xv[cc][e]);
    s += __shfl_xor(s, 1); s += __shfl_xor(s, 2); s += __shfl_xor(s, 4);
    const float mean = s * (1.0f / 576.0f);
    float q = 0.f;
#pragma unroll
    for (int cc = 0; cc < 9; ++cc)
#pragma unroll
        for (int e = 0; e < 4; ++e) { const float a = bf_lo(xv[cc][e]) - mean, c2 = bf_hi(xv[cc][e]) - mean; q += a * a + c2 * c2; }
    q += __shfl_xor(q, 1); q += __shfl_xor(q, 2); q += __shfl_xor(q, 4);
    const float mb = rbf(mean), vb = rbf(q * (1.0f / 576.0f));
    const float sb = rbf(sqrtf(rbf(vb + 1e-5f)));
    const float rs = 1.0f / sb;
#pragma unroll
    for (int cc = 0; cc < 9; ++cc) {
        u32x4 o;
#pragma unroll
        for (int e = 0; e < 4; ++e) {
            const unsigned d = pk_bf16(bf_lo(xv[cc][e]) - mb, bf_hi(xv[cc][e]) - mb);
            o[e] = pk_bf16(bf_lo(d) * rs, bf_hi(d) * rs);
        }
        const int blk = 2 * cc + (i >> 2);
        *(u32x4*)((char*)xnb + (size_t)blk * (BLK_ELEMS * 2) + blk_off(row, i & 3)) = o;
    }
}

constexpr int RA = 0, RB = 65536, RE = 131072, RQ = 139264, RH = 155648;
constexpr int T_DUMMY = 159744;
constexpr int K1_LDS = T_DUMMY + 1024;
constexpr int QS = 144;
#define HST_AUX 16
#define PF 2
constexpr int NB = PF + 1;
template <int N> __device__ __forceinline__ void wait_vm() { asm volatile("s_waitcnt vmcnt(%0)" :: "i"(N) : "memory"); }
#define GLDS_MASKED(NAME, LO, HI) \
__device__ __forceinline__ void NAME(const void* sbase, unsigned voff, unsigned lds) { \
    sbase = uni(sbase); lds = __builtin_amdgcn_readfirstlane(lds); \
    unsigned sv; unsigned long long ex; \
    asm volatile("s_mov_b32 %0, m0\n\ts_mov_b64 %1, exec\n\ts_mov_b32 m0, %4\n\ts_mov_b32 exec_lo, " #LO "\n\ts_mov_b32 exec_hi, " #HI "\n\tglobal_load_lds_dwordx4 %2, %3\n\ts_mov_b64 exec, %1\n\ts_mov_b32 m0, %0" \
                 : "=&s"(sv), "=&s"(ex) : "v"(voff), "s"(sbase), "s"(lds) : "memory"); }
GLDS_MASKED(glds16_32, -1, 0)
GLDS_MASKED(glds16_8, 0xff, 0)

__device__ __forceinline__ void k1_epilogue(f32x4 (&acc)[11][4], f32x4& ach, LAS char* lds, unsigned lds0, int w, int xcd, int idx,
                                            int j, int mt, const u16* __restrict__ Wd, u16* __restrict__ hb) {
    int tid = threadIdx.x; asm volatile("" : "+v"(tid));
    const int lane = tid & 63, fr = lane & 15, g = lane >> 4;
    const int wm = w >> 1, wn = w & 1;
    {
        const int split = xcd >> 1;
        const unsigned base = (unsigned)(split * 1152 * DM * 2 + (idx * 8 + w) * 6144);
#pragma unroll
        for (int i = 0; i < 6; ++i) {
            unsigned o = base + i * 1024; o = o + 1024 <= (unsigned)(FF * DM * 2) ? o : 0u;
            glds16((const char*)Wd + o, (unsigned)lane * 16u, lds0 + T_DUMMY);
        }
    }
    LAS char* qt = lds;
    {
#pragma unroll
        for (int u = 0; u < 2; ++u) {
            const int nn = 16 * (2 * wn + (u ^ (wm & 1))) + 4 * g;
            const int h0 = nn / 18, h1 = (nn + 2) / 18;
            const int o0 = h0 * 48 + (nn - h0 * 18) * 2, o1 = h1 * 48 + (nn + 2 - h1 * 18) * 2;
#pragma unroll
            for (int m = 0; m < 4; ++m) {
                LAS char* rp = qt + (16 + 64 * wm + 16 * m + fr) * QS;
                if (h0 < 3) *(LAS unsigned*)(rp + o0) = pk_bf16(acc[9 + u][m][0], acc[9 + u][m][1]);
                if (h1 < 3) *(LAS unsigned*)(rp + o1) = pk_bf16(acc[9 + u][m][2], acc[9 + u][m][3]);
            }
            if (u == 0 && (w == 0 || w == 2 || w == 1)) {
                LAS char* rp = qt + fr * QS;
                if (h0 < 3) *(LAS unsigned*)(rp + o0) = pk_bf16(ach[0], ach[1]);
                if (h1 < 3) *(LAS unsigned*)(rp + o1) = pk_bf16(ach[2], ach[3]);
            }
        }
    }
    asm volatile("s_waitcnt lgkmcnt(0)" ::: "memory");
    BAR();
    STAMP(1, 3);
    const __amdgpu_buffer_rsrc_t rh = __builtin_amdgcn_make_buffer_rsrc((void*)hb, 0, (int)(NKB_H * BLK_ELEMS * 2), 0x00020000);
    u32x4 hp[4][2]; u32x2 hm[4];
#pragma unroll
    for (int m = 0; m < 4; ++m) {
#pragma unroll
        for (int pp = 0; pp < 2; ++pp) {
            unsigned v[2][2];
#pragma unroll
            for (int hh = 0; hh < 2; ++hh) {
                const int gt = 2 * pp + hh;
                float hv[4];
#pragma unroll
                for (int e = 0; e < 4; ++e) hv[e] = fmaxf(acc[2 * gt][m][e], 0.f) * acc[2 * gt + 1][m][e];
                v[hh][0] = pk_bf16(hv[0], hv[1]); v[hh][1] = pk_bf16(hv[2], hv[3]);
            }
            auto r0 = __builtin_amdgcn_permlane16_swap(v[0][0], v[1][0], false, false);
            auto r1 = __builtin_amdgcn_permlane16_swap(v[0][1], v[1][1], false, false);
            hp[m][pp][0] = r0[0]; hp[m][pp][1] = r1[0]; hp[m][pp][2] = r0[1]; hp[m][pp][3] = r1[1];
        }
        {
            float hv[4];
#pragma unroll
            for (int e = 0; e < 4; ++e) {
                const unsigned mine = __float_as_uint(acc[8][m][e]);
                auto r = __builtin_amdgcn_permlane32_swap(mine, mine, false, false);
                hv[e] = fmaxf(__uint_as_float(r[0]), 0.f) * __uint_as_float(r[1]);
            }
            hm[m][0] = pk_bf16(hv[0], hv[1]); hm[m][1] = pk_bf16(hv[2], hv[3]);
        }
        asm volatile("" : "+v"(hp[m][0]), "+v"(hp[m][1]), "+v"(hm[m]));
    }
    SCHED();
    auto hstore = [&](int m) {
        const int row = 256 * mt + 64 * wm + 16 * m + fr;
#pragma unroll
        for (int pp = 0; pp < 2; ++pp) {
            const int blk = 4 * j + 2 * wn + pp, chunk = ((g & 1) << 1) | (g >> 1);
            __builtin_amdgcn_raw_buffer_store_b128(hp[m][pp], rh, (unsigned)(blk * (int)(BLK_ELEMS * 2) + blk_off(row, chunk)), 0, HST_AUX);
        }
        const int blk = 128 + (j >> 1), chunk = (j & 1) * 2 + wn;
        const unsigned off = g < 2 ? (unsigned)(blk * (int)(BLK_ELEMS * 2) + blk_off(row, chunk) + g * 8) : 0xfffffff0u;
        __builtin_amdgcn_raw_buffer_store_b64(hm[m], rh, off, 0, HST_AUX);
    };
    {
        const int s = tid >> 1, half = tid & 1;
        const int lrow = 16 + s, grow = 256 * mt + s;
        auto ld18 = [&](LAS const char* p, unsigned (&v)[9]) {
            const u32x4 a = *(LAS const u32x4*)p, c = *(LAS const u32x4*)(p + 16);
            v[0] = a[0]; v[1] = a[1]; v[2] = a[2]; v[3] = a[3]; v[4] = c[0]; v[5] = c[1]; v[6] = c[2]; v[7] = c[3];
            v[8] = *(LAS const unsigned*)(p + 32);
        };
        hstore(0);
        SCHED();
        unsigned qv[9];
        ld18(qt + lrow * QS + 96, qv);
        float lg[4];
#pragma unroll
        for (int ww = 0; ww < 4; ++ww) {
            const int wi = 4 * half + ww;
            unsigned kv[9];
            ld18(qt + (lrow - 2 * wi) * QS, kv);
            float a = 0.f;
#pragma unroll
            for (int i = 0; i < 9; ++i) a = __builtin_amdgcn_fdot2_f32_bf16(__builtin_bit_cast(bf16x2_t, qv[i]), __builtin_bit_cast(bf16x2_t, kv[i]), a, false);
            a = rbf(rbf(a) * 0.23570226039551584f);
            lg[ww] = (grow - 2 * wi >= 0) ? a : -1e9f;
        }
        SCHED();
        hstore(1);
        SCHED();
        float mx = fmaxf(fmaxf(lg[0], lg[1]), fmaxf(lg[2], lg[3]));
        mx = fmaxf(mx, __shfl_xor(mx, 1));
        float ex[4], sum = 0.f;
#pragma unroll
        for (int ww = 0; ww < 4; ++ww) { ex[ww] = __expf(lg[ww] - mx); sum += ex[ww]; }
        sum += __shfl_xor(sum, 1);
        const float inv = 1.0f / sum;
        float o[18];
#pragma unroll
        for (int i = 0; i < 18; ++i) o[i] = 0.f;
#pragma unroll
        for (int ww = 0; ww < 4; ++ww) {
            const int wi = 4 * half + ww;
            const float pw = (grow - 2 * wi >= 0) ? rbf(ex[ww] * inv) : 0.f;
            unsigned vv[9];
            ld18(qt + (lrow - 2 * wi) * QS + 48, vv);
#pragma unroll
            for (int i = 0; i < 9; ++i) { o[2 * i] += pw * bf_lo(vv[i]); o[2 * i + 1] += pw * bf_hi(vv[i]); }
        }
        SCHED();
        hstore(2);
        SCHED();
#pragma unroll
        for (int ii = 0; ii < 5; ++ii) {
            const float s0 = half ? o[2 * ii] : o[(10 + 2 * ii) % 18], s1 = half ? o[2 * ii + 1] : o[(11 + 2 * ii) % 18];
            const float r0 = __shfl_xor(s0, 1), r1 = __shfl_xor(s1, 1);
            const float m0 = half ? o[(10 + 2 * ii) % 18] : o[2 * ii], m1 = half ? o[(11 + 2 * ii) % 18] : o[2 * ii + 1];
            const int i = half * 5 + ii;
            if (i < 9) {
                const int col = FF + 18 * j + 2 * i;
                *(unsigned*)((char*)hb + (size_t)(col >> 5) * (BLK_ELEMS * 2) + blk_off(grow, (col >> 3) & 3) + (col & 7) * 2) = pk_bf16(m0 + r0, m1 + r1);
            }
        }
        SCHED();
        hstore(3);
    }
    STAMP(1, 15);
}

__global__ __launch_bounds__(512, 2) void k1_gateup(const u16* __restrict__ xnb, const u16* __restrict__ Wg,
                                                    const u16* __restrict__ Wu, const u16* __restrict__ wqh,
                                                    const u16* __restrict__ Wd, u16* __restrict__ hb) {
    extern __shared__ __attribute__((aligned(16))) char smem[];
    LAS char* lds = (LAS char*)smem;
    const int tid = threadIdx.x, lane = tid & 63;
    const int w = __builtin_amdgcn_readfirstlane(tid >> 6);
    const int b = blockIdx.x, xcd = b & 7, idx = b >> 3;
    const int j = 4 * xcd + (idx & 3), mt = idx >> 2;
    const int wm = w >> 1, wn = w & 1;
    const unsigned lds0 = (unsigned)(size_t)lds;
    const int fr = lane & 15, g = lane >> 4, q4 = (lane >> 2) & 3, p4 = lane & 3;

    f32x4 acc[11][4], ach;
    {
        const unsigned srcA = (unsigned)((256 * mt + 32 * w) * 32 + lane * 8) * 2u;
        const int koct = w >> 1, mat = w & 1;
        const u16* W = mat ? Wu : Wg;
        unsigned srcB, srcE, srcQ, srcH;
        {
            const int r = lane >> 3, swz = ((r >> 1) & 1) | ((koct & 1) << 1), c = (lane & 7) ^ (swz << 1);
            srcB = (unsigned)((8 * koct + r) * FF + 128 * j + 8 * c) * 2u;
            const int re = ((lane >> 1) & 7) ^ ((koct & 1) << 2), ce = (lane & 1) ^ mat;
            srcE = (unsigned)((8 * koct + re) * FF + 4096 + 16 * j + 8 * ce) * 2u;
            const int rq = 4 * mat + ((lane >> 3) & 3), swq = ((rq >> 1) & 1) | ((koct & 1) << 1), cq = (lane & 7) ^ (swq << 1);
            srcQ = (unsigned)((j * 576 + 8 * koct + rq) * 64 + 8 * cq) * 2u;
            int hr = 256 * mt - 16 + 2 * w + ((lane >> 2) & 1); hr = hr < 0 ? 0 : hr;
            srcH = (unsigned)(hr * 64 + (lane & 3) * 16);
        }
        const unsigned dA = lds0 + RA + (2 * w) * 1024, dB = lds0 + RB + (2 * w) * 1024, dE = lds0 + RE + (mat * 4 + koct) * 256;
        const unsigned dQ = lds0 + RQ + koct * 1024 + mat * 512, dH = lds0 + RH + w * 128;
        auto issue1 = [&](int t, int i) {
            const int sl = t & 3;
            const u16* ba = xnb + (size_t)t * BLK_ELEMS;
            const u16* bw = W + (size_t)t * 32 * FF;
            if (i == 0) glds16(bw, srcB, dB + sl * 16384);
            if (i == 1) glds16(bw + 64, srcB, dB + sl * 16384 + 1024);
            if (i == 2) glds16_q(bw, srcE, dE + sl * 2048);
            if (i == 3) glds16(ba, srcA, dA + sl * 16384);
            if (i == 4) glds16_o1k(ba, srcA, dA + sl * 16384);
            if (i == 5) glds16_32(wqh + (size_t)t * 32 * 64, srcQ, dQ + sl * 4096);
            if (i == 6) glds16_8(ba, srcH, dH + sl * 1024);
        };
        STAMP(1, 0);
#pragma unroll
        for (int t = 0; t < 3; ++t)
#pragma unroll
            for (int i = 0; i < 7; ++i) issue1(t, i);

        const unsigned pa = lds0 + RA + blk_off(64 * wm + fr, g);
        const int swz = ((q4 >> 1) & 1) | ((g & 1) << 1);
        const unsigned pb0 = lds0 + RB + (g * 4 + wn) * 1024 + q4 * 128 + (swz << 5) + p4 * 8;
        const int emat = p4 >> 1;
        const unsigned pe0 = lds0 + RE + (emat * 4 + g) * 256 + ((q4 ^ ((g & 1) << 2)) * 32) + ((wn ^ emat) * 16) + (p4 & 1) * 8;
        const unsigned pq0 = lds0 + RQ + g * 1024 + q4 * 128 + (((2 * wn + (wm & 1)) ^ swz) << 5) + p4 * 8;
        const int hdelta = RH - RA - 4096 * wm;
        auto xr = [&](unsigned base, int imm) -> LAS char* {
            if (imm == 0) return (LAS char*)(size_t)base;
            unsigned r; asm volatile("v_xor_b32 %0, %1, %2" : "=v"(r) : "i"(imm), "v"(base)); return (LAS char*)(size_t)r;
        };
        auto rdB = [&](int sl, int nf, int t1) -> s16x4 {
            if (nf < 8) return tr_read(xr(pb0, (nf >> 1) << 5) + sl * 16384 + (nf & 1) * 2048 + t1 * 512);
            if (nf == 8) return tr_read(xr(pe0, t1 * 128) + sl * 2048);
            return tr_read(xr(pq0, (nf - 9) << 5) + sl * 4096 + t1 * 512);
        };
        auto rdA = [&](int sl, int m) -> bf16x8 { return row_frag((LAS char*)(size_t)pa + sl * 16384 + m * 1024); };
        auto rdH = [&](int sl) -> bf16x8 {
            unsigned r; asm volatile("v_add_u32 %0, %1, %2" : "=v"(r) : "s"(hdelta), "v"(pa)); return row_frag((LAS char*)(size_t)r + sl * 1024);
        };
#pragma unroll
        for (int n = 0; n < 11; ++n)
#pragma unroll
            for (int m = 0; m < 4; ++m) { acc[n][m] = (f32x4){0.f, 0.f, 0.f, 0.f}; asm volatile("" : "+v"(acc[n][m])); }
        ach = (f32x4){0.f, 0.f, 0.f, 0.f}; asm volatile("" : "+v"(ach));

        wait_vm<14>(); BAR();
        STAMP(1, 1);
        bf16x8 fa[2][2], fc[2], fh, fb[NB];
#pragma unroll
        for (int m = 0; m < 2; ++m) { fa[0][m] = rdA(0, m); fc[m] = rdA(0, 2 + m); }
#pragma unroll
        for (int i = 0; i < PF; ++i) { s16x4 lo = rdB(0, i, 0), hi = rdB(0, i, 1); fb[i] = __builtin_shufflevector(lo, hi, 0, 1, 2, 3, 4, 5, 6, 7); }
#define MM(C, A, B) mfma_ip(C, A, B)
#pragma unroll
        for (int t = 0; t < NKB_X; ++t) {
            const int sl = t & 3, sn = (t + 1) & 3, cur = t & 1;
#pragma unroll
            for (int nf = 0; nf < 11; ++nf) {
                const int r = 11 * t + nf;
                const int psl = (nf + PF < 11) ? sl : sn, nn = (nf + PF < 11) ? nf + PF : nf + PF - 11;
                const bool pf = (nf + PF < 11) || (t + 1 < NKB_X);
                const bool lastg = (nf == 10);
                s16x4 lo, hi;
                if (!lastg) MM(acc[nf][0], fb[r % NB], fa[cur][0]); else MM(acc[nf][2], fb[r % NB], fc[0]);
                SCHED();
                if (nf == 0) {
                    if (t + 2 < NKB_X) wait_vm<7>(); else wait_vm<0>();
                    BAR();
                }
                if (pf) lo = rdB(psl, nn, 0);
                SCHED();
                if (!lastg) MM(acc[nf][1], fb[r % NB], fa[cur][1]); else MM(acc[nf][3], fb[r % NB], fc[1]);
                SCHED();
                if (pf) hi = rdB(psl, nn, 1);
                if (lastg && t + 1 < NKB_X) fc[0] = rdA(sn, 2);
                SCHED();
                if (!lastg) MM(acc[nf][2], fb[r % NB], fc[0]); else MM(acc[nf][0], fb[r % NB], fa[cur][0]);
                SCHED();
                if (lastg && t + 1 < NKB_X) fc[1] = rdA(sn, 3);
                if (nf >= 4 && nf < 6 && t + 1 < NKB_X) fa[cur ^ 1][nf - 4] = rdA(sn, nf - 4);
                if (nf == 7) fh = rdH(sl);
                if ((nf < 4 || (nf >= 6 && nf < 9)) && t + 3 < NKB_X) issue1(t + 3, nf < 4 ? nf : nf - 2);
                SCHED();
                if (!lastg) MM(acc[nf][3], fb[r % NB], fc[1]); else MM(acc[nf][1], fb[r % NB], fa[cur][1]);
                SCHED();
                if (nf == 9) { MM(ach, fb[r % NB], fh); SCHED(); }
                if (pf) fb[(r + PF) % NB] = __builtin_shufflevector(lo, hi, 0, 1, 2, 3, 4, 5, 6, 7);
            }
        }
    }
    asm volatile("s_nop 7\n\ts_nop 7" ::: "memory");
    BAR();
    STAMP(1, 2);
    k1_epilogue(acc, ach, lds, lds0, w, xcd, idx, j, mt, Wd, hb);
}

constexpr int K2_NT = 21, K2_LA = 5;
constexpr int K2_RB = 0, K2_RE = 131072, K2_LDS = 131072 + 16384;
constexpr int NSPLIT = 4;
__device__ __forceinline__ void gload2(bf16x8& d0, bf16x8& d1, const void* sbase, unsigned voff) {
    sbase = uni(sbase);
    asm volatile("s_nop 4\n\tglobal_load_dwordx4 %0, %2, %3\n\tglobal_load_dwordx4 %1, %2, %3 offset:1024"
                 : "=&v"(d0), "=&v"(d1) : "v"(voff), "s"(sbase) : "memory");
}
__global__ __launch_bounds__(512, 2) void k2_down(const u16* __restrict__ hb, const u16* __restrict__ Wd,
                                                  const u16* __restrict__ wo, u16* __restrict__ part) {
    extern __shared__ __attribute__((aligned(16))) char smem[];
    LAS char* lds = (LAS char*)smem;
    const int tid = threadIdx.x, lane = tid & 63;
    const int w = __builtin_amdgcn_readfirstlane(tid >> 6);
    const int b = blockIdx.x, xcd = b & 7, idx = b >> 3;
    const int split = xcd >> 1, mt = (xcd & 1) * 8 + (idx >> 2), nt = idx & 3;
    const int wm = w >> 1, kh = w & 1;
    const int T0 = split == 0 ? 0 : 21 + 20 * (split - 1);

    const unsigned srcA = (unsigned)(kh * (int)BLK_ELEMS * 2 + blk_off(128 * mt + 32 * wm + (lane & 15), lane >> 4));
    const int koct = w >> 1, sub = w & 1;
    unsigned offB, offE;
    {
        const int r = lane >> 3, swz = ((r >> 1) & 1) | ((koct & 1) << 1), c = (lane & 7) ^ (swz << 1);
        offB = (unsigned)((32 * sub + 8 * koct + r) * DM + 128 * nt + 8 * c) * 2u;
        const int re = ((lane >> 1) & 7) ^ ((koct & 1) << 2), ce = (lane & 1);
        offE = (unsigned)((32 * sub + 8 * koct + re) * DM + 512 + 16 * nt + 8 * ce) * 2u;
    }
    const unsigned offB1 = offB + 128;
    const unsigned lds0 = (unsigned)(size_t)lds;
    const unsigned dB = lds0 + K2_RB + (2 * w) * 1024, dE = lds0 + K2_RE + (sub * 4 + koct) * 256;
    bf16x8 fa[8][2];
    auto issue1 = [&](int t, int i) {
        const int sl = t & 7;
        int T = T0 + t; if (t == K2_NT - 1 && split != 0) T -= 1;
        const u16* Wt = T < 72 ? Wd + (size_t)T * 64 * DM : wo + (size_t)(T - 72) * 64 * DM;
        const u16* ba = hb + (size_t)T * 2 * BLK_ELEMS;
        if (i == 0) glds16(Wt, offB, dB + sl * 16384);
        if (i == 1) glds16(Wt, offB1, dB + sl * 16384 + 1024);
        if (i == 2) glds16_q(Wt, offE, dE + sl * 2048);
        if (i == 3) gload2(fa[t & 7][0], fa[t & 7][1], ba, srcA);
    };
    STAMP(2, 0);
#pragma unroll
    for (int t = 0; t < K2_LA; ++t)
#pragma unroll
        for (int i = 0; i < 4; ++i) issue1(t, i);

    const int fr = lane & 15, g = lane >> 4, q4 = (lane >> 2) & 3, p4 = lane & 3;
    const int swz = ((q4 >> 1) & 1) | ((g & 1) << 1);
    LAS char* pb[2][4];
#pragma unroll
    for (int u = 0; u < 4; ++u) { pb[0][u] = lds + K2_RB + (g * 4 + kh * 2) * 1024 + q4 * 128 + ((u ^ swz) << 5) + p4 * 8; pb[1][u] = pb[0][u] + 65536; }
    LAS char* pe0 = lds + K2_RE + (kh * 4 + g) * 256 + ((q4 ^ ((g & 1) << 2)) * 32) + p4 * 8;
    LAS char* pe1 = lds + K2_RE + (kh * 4 + g) * 256 + (((4 + q4) ^ ((g & 1) << 2)) * 32) + p4 * 8;
    auto rdB = [&](int sl, int nf, int t1) -> s16x4 {
        if (nf < 8) return tr_read(pb[sl >> 2][nf & 3] + (sl & 3) * 16384 + (nf >> 2) * 1024 + t1 * 512);
        return tr_read((t1 ? pe1 : pe0) + sl * 2048);
    };

    f32x4 acc[9][2];
#pragma unroll
    for (int n = 0; n < 9; ++n)
#pragma unroll
        for (int m = 0; m < 2; ++m) acc[n][m] = (f32x4){0.f, 0.f, 0.f, 0.f};

    WAIT_V(20); BAR();
    STAMP(2, 1);
    constexpr int PF2 = 4;
    bf16x8 fb[9];
#pragma unroll
    for (int i = 0; i < PF2; ++i) { s16x4 lo = rdB(0, i, 0), hi = rdB(0, i, 1); fb[i] = __builtin_shufflevector(lo, hi, 0, 1, 2, 3, 4, 5, 6, 7); }
#pragma unroll
    for (int t = 0; t < K2_NT; ++t) {
        const int sl = t & 7, sn = (t + 1) & 7;
        asm volatile("" : "+v"(fa[sl][0]), "+v"(fa[sl][1]));
        if (t == K2_NT - 1 && split != 0) {
#pragma unroll
            for (int m = 0; m < 2; ++m) fa[sl][m] = (bf16x8){0, 0, 0, 0, 0, 0, 0, 0};
        }
#pragma unroll
        for (int nf = 0; nf < 9; ++nf) {
            const int psl = (nf + PF2 < 9) ? sl : sn, nn = (nf + PF2 < 9) ? nf + PF2 : nf + PF2 - 9;
            const bool pf = (nf + PF2 < 9) || (t + 1 < K2_NT);
            s16x4 lo, hi;
            acc[nf][0] = MFMA16(fb[nf], fa[sl][0], acc[nf][0]);
            SCHED();
            if (nf == 0 && (t & 1) == 0) {
                const int last = (t + K2_LA - 1 < K2_NT - 1) ? t + K2_LA - 1 : K2_NT - 1;
                const int n = last - (t + 2) > 0 ? (last - (t + 2)) * 5 : 0;
                if (n == 10) { WAIT_V(10); } else if (n == 5) { WAIT_V(5); } else { WAIT_V(0); }
                BAR();
            }
            if (pf) lo = rdB(psl, nn, 0);
            SCHED();
            acc[nf][1] = MFMA16(fb[nf], fa[sl][1], acc[nf][1]);
            SCHED();
            if (pf) hi = rdB(psl, nn, 1);
            if (nf >= 1 && nf < 5 && t + K2_LA < K2_NT) issue1(t + K2_LA, nf - 1);
            SCHED();
            if (pf) fb[nn] = __builtin_shufflevector(lo, hi, 0, 1, 2, 3, 4, 5, 6, 7);
        }
    }
    BAR();
    STAMP(2, 2);
    {
        LAS char* xw = lds + w * 10240;
        LAS char* xr = lds + (w ^ 1) * 10240;
#pragma unroll
        for (int n = 0; n < 9; ++n) {
            const bool give = kh ? (n < 5) : (n >= 5);
            if (give) {
#pragma unroll
                for (int m = 0; m < 2; ++m) *(LAS f32x4*)(xw + ((kh ? n : n - 5) * 2 + m) * 1024 + lane * 16) = acc[n][m];
            }
        }
        asm volatile("s_waitcnt lgkmcnt(0)" ::: "memory");
        BAR();
        u16* pp = part + (size_t)split * SEQ * DM;
#pragma unroll
        for (int n = 0; n < 9; ++n) {
            const bool keep = kh ? (n >= 5) : (n < 5);
            if (keep) {
#pragma unroll
                for (int m = 0; m < 2; ++m) {
                    const f32x4 o = *(LAS f32x4*)(xr + ((kh ? n - 5 : n) * 2 + m) * 1024 + lane * 16) + acc[n][m];
                    const int row = 128 * mt + 32 * wm + 16 * m + fr;
                    const int col = (n < 8 ? 128 * nt + 16 * n : 512 + 16 * nt) + 4 * g;
                    u32x2 v; v[0] = pk_bf16(o[0], o[1]); v[1] = pk_bf16(o[2], o[3]);
                    *(u32x2*)(pp + (size_t)row * DM + col) = v;
                }
            }
        }
    }
    STAMP(2, 15);
}

__global__ __launch_bounds__(256) void k3_reduce(const u16* __restrict__ x, const u16* __restrict__ part, u16* __restrict__ out) {
    const int i = blockIdx.x * 256 + threadIdx.x;
    const size_t e = (size_t)i * 8;
    constexpr size_t PS = (size_t)SEQ * DM;
    const u32x4 xv = *(const u32x4*)(x + e);
    u32x4 pv[NSPLIT];
#pragma unroll
    for (int s = 0; s < NSPLIT; ++s) pv[s] = *(const u32x4*)(part + s * PS + e);
    u32x4 o;
#pragma unroll
    for (int k = 0; k < 4; ++k) {
        float lo = 0.f, hi = 0.f;
#pragma unroll
        for (int s = 0; s < NSPLIT; ++s) { lo += bf_lo(pv[s][k]); hi += bf_hi(pv[s][k]); }
        o[k] = pk_bf16(lo + bf_lo(xv[k]), hi + bf_hi(xv[k]));
    }
    *(u32x4*)(out + e) = o;
}

extern "C" void kernel_launch(void* const* d_in, const int* in_sizes, int n_in, void* d_out, int out_size, void* d_ws,
                              size_t ws_size, hipStream_t stream) {
    const u16* x = (const u16*)d_in[0];
    const u16* wqkv = (const u16*)d_in[1];
    const u16* wo = (const u16*)d_in[2];
    const u16* Wg = (const u16*)d_in[4];
    const u16* Wu = (const u16*)d_in[5];
    const u16* Wd = (const u16*)d_in[6];
    char* ws = (char*)d_ws;
    u16* xnb = (u16*)ws;
    u16* hb = (u16*)(ws + (4u << 20));
    u16* part = (u16*)(ws + (32u << 20));
    u16* wqh = (u16*)(ws + (48u << 20));
    (void)hipFuncSetAttribute((const void*)k1_gateup, hipFuncAttributeMaxDynamicSharedMemorySize, K1_LDS);
    (void)hipFuncSetAttribute((const void*)k2_down, hipFuncAttributeMaxDynamicSharedMemorySize, K2_LDS);
    k0_ln<<<256, 128, 0, stream>>>(x, xnb, wqkv, wqh);
    k1_gateup<<<256, 512, K1_LDS, stream>>>(xnb, Wg, Wu, wqh, Wd, hb);
    k2_down<<<256, 512, K2_LDS, stream>>>(hb, Wd, wo, part);
    k3_reduce<<<SEQ * DM / 8 / 256, 256, 0, stream>>>(x, part, (u16*)d_out);
}
```

```cpp
#include <hip/hip_runtime.h>
#include <hip/hip_bf16.h>
#include <stdint.h>

typedef unsigned short u16;
typedef short bf16x8 __attribute__((ext_vector_type(8)));
typedef short s16x4 __attribute__((ext_vector_type(4)));
typedef float f32x4 __attribute__((ext_vector_type(4)));
typedef float f32x2 __attribute__((ext_vector_type(2)));
typedef __bf16 bf16x2_t __attribute__((ext_vector_type(2)));
typedef unsigned u32x4 __attribute__((ext_vector_type(4)));
typedef unsigned u32x2 __attribute__((ext_vector_type(2)));
#define LAS __attribute__((address_space(3)))
#define GAS __attribute__((address_space(1)))

constexpr int SEQ = 2048, DM = 576, FF = 4608;
constexpr int NKB_X = 18;
constexpr int NKB_H = 162;
constexpr size_t BLK_ELEMS = (size_t)SEQ * 32;

__device__ __forceinline__ unsigned pk_bf16(float lo, float hi) {
    f32x2 v = {lo, hi};
    bf16x2_t b = __builtin_convertvector(v, bf16x2_t);
    return __builtin_bit_cast(unsigned, b);
}
__device__ __forceinline__ float bf_lo(unsigned u) { return __uint_as_float(u << 16); }
__device__ __forceinline__ float bf_hi(unsigned u) { return __uint_as_float(u & 0xffff0000u); }
__device__ __forceinline__ float rbf(float x) { return bf_lo(pk_bf16(x, x)); }

__device__ __forceinline__ const void* uni(const void* p) {
    const unsigned long long v = (unsigned long long)p;
    const unsigned lo = __builtin_amdgcn_readfirstlane((unsigned)v), hi = __builtin_amdgcn_readfirstlane((unsigned)(v >> 32));
    return (const void*)(((unsigned long long)hi << 32) | lo);
}
__device__ __forceinline__ void glds16(const void* sbase, unsigned voff, unsigned lds) {
    sbase = uni(sbase); lds = __builtin_amdgcn_readfirstlane(lds);
    unsigned sv;
    asm volatile("s_mov_b32 %0, m0\n\ts_mov_b32 m0, %3\n\ts_nop 3\n\tglobal_load_lds_dwordx4 %1, %2\n\ts_mov_b32 m0, %0"
                 : "=&s"(sv) : "v"(voff), "s"(sbase), "s"(lds) : "memory");
}
__device__ __forceinline__ void glds16_o1k(const void* sbase, unsigned voff, unsigned lds) {
    sbase = uni(sbase); lds = __builtin_amdgcn_readfirstlane(lds);
    unsigned sv;
    asm volatile("s_mov_b32 %0, m0\n\ts_mov_b32 m0, %3\n\ts_nop 3\n\tglobal_load_lds_dwordx4 %1, %2 offset:1024\n\ts_mov_b32 m0, %0"
                 : "=&s"(sv) : "v"(voff), "s"(sbase), "s"(lds) : "memory");
}
__device__ __forceinline__ void glds16_q(const void* sbase, unsigned voff, unsigned lds) {
    sbase = uni(sbase); lds = __builtin_amdgcn_readfirstlane(lds);
    unsigned sv; unsigned long long ex;
    asm volatile("s_mov_b32 %0, m0\n\ts_mov_b64 %1, exec\n\ts_mov_b32 m0, %4\n\ts_mov_b64 exec, 0xffff\n\ts_nop 1\n\tglobal_load_lds_dwordx4 %2, %3\n\ts_mov_b64 exec, %1\n\ts_mov_b32 m0, %0"
                 : "=&s"(sv), "=&s"(ex) : "v"(voff), "s"(sbase), "s"(lds) : "memory");
}
__device__ __forceinline__ void glds16_m(const void* sbase, unsigned voff, unsigned lds) {
    sbase = uni(sbase); lds = __builtin_amdgcn_readfirstlane(lds);
    unsigned sv; unsigned long long ex;
    asm volatile("s_mov_b32 %0, m0\n\ts_mov_b64 %1, exec\n\ts_mov_b32 m0, %4\n\ts_mov_b32 exec_lo, -1\n\ts_mov_b32 exec_hi, 0xff\n\ts_nop 1\n\tglobal_load_lds_dwordx4 %2, %3\n\ts_mov_b64 exec, %1\n\ts_mov_b32 m0, %0"
                 : "=&s"(sv), "=&s"(ex) : "v"(voff), "s"(sbase), "s"(lds) : "memory");
}
#define SCHED() __builtin_amdgcn_sched_barrier(0)
#define STAMP(kid, i) do {} while (0)
#define WSTAMP(i) do {} while (0)
__device__ __forceinline__ s16x4 tr_read(LAS char* p) {
    return __builtin_bit_cast(s16x4, __builtin_amdgcn_ds_read_tr16_b64_v4i16((LAS s16x4*)p));
}
__device__ __forceinline__ bf16x8 tr_frag(LAS char* p, int off_t1) {
    s16x4 lo = tr_read(p), hi = tr_read(p + off_t1);
    return __builtin_shufflevector(lo, hi, 0, 1, 2, 3, 4, 5, 6, 7);
}
__device__ __forceinline__ bf16x8 row_frag(LAS char* p) { return *(LAS bf16x8*)p; }
#define MFMA16(a, b, c) __builtin_amdgcn_mfma_f32_16x16x32_bf16((a), (b), (c), 0, 0, 0)
__device__ __forceinline__ void mfma_ip(f32x4& c, bf16x8 a, bf16x8 b) { asm volatile("v_mfma_f32_16x16x32_bf16 %0, %1, %2, %0" : "+v"(c) : "v"(a), "v"(b)); }
#define WAIT_V(n) asm volatile("s_waitcnt vmcnt(" #n ")" ::: "memory")
#define BAR() do { asm volatile("" ::: "memory"); __builtin_amdgcn_s_barrier(); asm volatile("" ::: "memory"); } while (0)

__device__ __forceinline__ int blk_off(int row, int chunk) { return row * 64 + ((chunk ^ (((row >> 3) & 1) << 1)) << 4); }

__global__ __launch_bounds__(256) void k0_ln(const u16* __restrict__ x, u16* __restrict__ xnb,
                                            const u16* __restrict__ wqkv, u16* __restrict__ wqh) {
    if (threadIdx.x >= 64) {
        const int l = threadIdx.x - 64, head = blockIdx.x >> 3, k0 = (blockIdx.x & 7) * 72;
        unsigned v[12];
#pragma unroll
        for (int i = 0; i < 12; ++i) {
            const int id = l + 192 * i, k = k0 + (id >> 5), cp = id & 31;
            const int part = cp >= 18 ? 2 : (cp >= 9 ? 1 : 0), d2 = cp - part * 9;
            const int src = (part == 2 ? 0 : part + 1) * DM + 18 * head + 2 * d2;
            v[i] = cp < 27 ? *(const unsigned*)(wqkv + (size_t)k * (3 * DM) + src) : 0u;
        }
#pragma unroll
        for (int i = 0; i < 12; ++i) {
            const int id = l + 192 * i;
            *(unsigned*)(wqh + ((size_t)head * 576 + k0) * 64 + 2 * id) = v[i];
        }
        return;
    }
    const int lane = threadIdx.x, i = lane & 7, row = blockIdx.x * 8 + (lane >> 3);
    u32x4 xv[9];
#pragma unroll
    for (int cc = 0; cc < 9; ++cc) xv[cc] = *(const u32x4*)(x + (size_t)row * DM + (i + 8 * cc) * 8);
    float s = 0.f;
#pragma unroll
    for (int cc = 0; cc < 9; ++cc)
#pragma unroll
        for (int e = 0; e < 4; ++e) s += bf_lo(xv[cc][e]) + bf_hi(xv[cc][e]);
    s += __shfl_xor(s, 1); s += __shfl_xor(s, 2); s += __shfl_xor(s, 4);
    const float mean = s * (1.0f / 576.0f);
    float q = 0.f;
#pragma unroll
    for (int cc = 0; cc < 9; ++cc)
#pragma unroll
        for (int e = 0; e < 4; ++e) { const float a = bf_lo(xv[cc][e]) - mean, c2 = bf_hi(xv[cc][e]) - mean; q += a * a + c2 * c2; }
    q += __shfl_xor(q, 1); q += __shfl_xor(q, 2); q += __shfl_xor(q, 4);
    const float mb = rbf(mean), vb = rbf(q * (1.0f / 576.0f));
    const float sb = rbf(sqrtf(rbf(vb + 1e-5f)));
    const float rs = 1.0f / sb;
#pragma unroll
    for (int cc = 0; cc < 9; ++cc) {
        u32x4 o;
#pragma unroll
        for (int e = 0; e < 4; ++e) {
            const unsigned d = pk_bf16(bf_lo(xv[cc][e]) - mb, bf_hi(xv[cc][e]) - mb);
            o[e] = pk_bf16(bf_lo(d) * rs, bf_hi(d) * rs);
        }
        const int blk = 2 * cc + (i >> 2);
        *(u32x4*)((char*)xnb + (size_t)blk * (BLK_ELEMS * 2) + blk_off(row, i & 3)) = o;
    }
}

constexpr int RA = 0, RB = 65536, RE = 131072, RQ = 139264, RH = 155648;
constexpr int T_DUMMY = 159744;
constexpr int K1_LDS = T_DUMMY + 1024;
constexpr int QS = 144;
#define HST_AUX 16
#define PF 2
constexpr int NB = PF + 1;
template <int N> __device__ __forceinline__ void wait_vm() { asm volatile("s_waitcnt vmcnt(%0)" :: "i"(N) : "memory"); }
#define GLDS_MASKED(NAME, LO, HI) \
__device__ __forceinline__ void NAME(const void* sbase, unsigned voff, unsigned lds) { \
    sbase = uni(sbase); lds = __builtin_amdgcn_readfirstlane(lds); \
    unsigned sv; unsigned long long ex; \
    asm volatile("s_mov_b32 %0, m0\n\ts_mov_b64 %1, exec\n\ts_mov_b32 m0, %4\n\ts_mov_b32 exec_lo, " #LO "\n\ts_mov_b32 exec_hi, " #HI "\n\tglobal_load_lds_dwordx4 %2, %3\n\ts_mov_b64 exec, %1\n\ts_mov_b32 m0, %0" \
                 : "=&s"(sv), "=&s"(ex) : "v"(voff), "s"(sbase), "s"(lds) : "memory"); }
GLDS_MASKED(glds16_32, -1, 0)
GLDS_MASKED(glds16_8, 0xff, 0)

__device__ __forceinline__ void k1_epilogue(f32x4 (&acc)[11][4], f32x4& ach, LAS char* lds, unsigned lds0, int w, int xcd, int idx,
                                            int j, int mt, const u16* __restrict__ Wd, u16* __restrict__ hb) {
    int tid = threadIdx.x; asm volatile("" : "+v"(tid));
    const int lane = tid & 63, fr = lane & 15, g = lane >> 4;
    const int wm = w >> 1, wn = w & 1;
    {
        const int split = xcd >> 1;
        const unsigned base = (unsigned)(split * 1152 * DM * 2 + (idx * 8 + w) * 6144);
#pragma unroll
        for (int i = 0; i < 6; ++i) {
            unsigned o = base + i * 1024; o = o + 1024 <= (unsigned)(FF * DM * 2) ? o : 0u;
            glds16((const char*)Wd + o, (unsigned)lane * 16u, lds0 + T_DUMMY);
        }
    }
    LAS char* qt = lds;
    {
#pragma unroll
        for (int u = 0; u < 2; ++u) {
            const int nn = 16 * (2 * wn + (u ^ (wm & 1))) + 4 * g;
            const int h0 = nn / 18, h1 = (nn + 2) / 18;
            const int o0 = h0 * 48 + (nn - h0 * 18) * 2, o1 = h1 * 48 + (nn + 2 - h1 * 18) * 2;
#pragma unroll
            for (int m = 0; m < 4; ++m) {
                LAS char* rp = qt + (16 + 64 * wm + 16 * m + fr) * QS;
                if (h0 < 3) *(LAS unsigned*)(rp + o0) = pk_bf16(acc[9 + u][m][0], acc[9 + u][m][1]);
                if (h1 < 3) *(LAS unsigned*)(rp + o1) = pk_bf16(acc[9 + u][m][2], acc[9 + u][m][3]);
            }
            if (u == 0 && (w == 0 || w == 2 || w == 1)) {
                LAS char* rp = qt + fr * QS;
                if (h0 < 3) *(LAS unsigned*)(rp + o0) = pk_bf16(ach[0], ach[1]);
                if (h1 < 3) *(LAS unsigned*)(rp + o1) = pk_bf16(ach[2], ach[3]);
            }
        }
    }
    asm volatile("s_waitcnt lgkmcnt(0)" ::: "memory");
    BAR();
    STAMP(1, 3);
    const __amdgpu_buffer_rsrc_t rh = __builtin_amdgcn_make_buffer_rsrc((void*)hb, 0, (int)(NKB_H * BLK_ELEMS * 2), 0x00020000);
    u32x4 hp[4][2]; u32x2 hm[4];
#pragma unroll
    for (int m = 0; m < 4; ++m) {
#pragma unroll
        for (int pp = 0; pp < 2; ++pp) {
            unsigned v[2][2];
#pragma unroll
            for (int hh = 0; hh < 2; ++hh) {
                const int gt = 2 * pp + hh;
                float hv[4];
#pragma unroll
                for (int e = 0; e < 4; ++e) hv[e] = fmaxf(acc[2 * gt][m][e], 0.f) * acc[2 * gt + 1][m][e];
                v[hh][0] = pk_bf16(hv[0], hv[1]); v[hh][1] = pk_bf16(hv[2], hv[3]);
            }
            auto r0 = __builtin_amdgcn_permlane16_swap(v[0][0], v[1][0], false, false);
            auto r1 = __builtin_amdgcn_permlane16_swap(v[0][1], v[1][1], false, false);
            hp[m][pp][0] = r0[0]; hp[m][pp][1] = r1[0]; hp[m][pp][2] = r0[1]; hp[m][pp][3] = r1[1];
        }
        {
            float hv[4];
#pragma unroll
            for (int e = 0; e < 4; ++e) {
                const unsigned mine = __float_as_uint(acc[8][m][e]);
                auto r = __builtin_amdgcn_permlane32_swap(mine, mine, false, false);
                hv[e] = fmaxf(__uint_as_float(r[0]), 0.f) * __uint_as_float(r[1]);
            }
            hm[m][0] = pk_bf16(hv[0], hv[1]); hm[m][1] = pk_bf16(hv[2], hv[3]);
        }
        asm volatile("" : "+v"(hp[m][0]), "+v"(hp[m][1]), "+v"(hm[m]));
    }
    SCHED();
    auto hstore = [&](int m) {
        const int row = 256 * mt + 64 * wm + 16 * m + fr;
#pragma unroll
        for (int pp = 0; pp < 2; ++pp) {
            const int blk = 4 * j + 2 * wn + pp, chunk = ((g & 1) << 1) | (g >> 1);
            __builtin_amdgcn_raw_buffer_store_b128(hp[m][pp], rh, (unsigned)(blk * (int)(BLK_ELEMS * 2) + blk_off(row, chunk)), 0, HST_AUX);
        }
        const int blk = 128 + (j >> 1), chunk = (j & 1) * 2 + wn;
        const unsigned off = g < 2 ? (unsigned)(blk * (int)(BLK_ELEMS * 2) + blk_off(row, chunk) + g * 8) : 0xfffffff0u;
        __builtin_amdgcn_raw_buffer_store_b64(hm[m], rh, off, 0, HST_AUX);
    };
    {
        const int s = tid >> 1, half = tid & 1;
        const int lrow = 16 + s, grow = 256 * mt + s;
        auto ld18 = [&](LAS const char* p, unsigned (&v)[9]) {
            const u32x4 a = *(LAS const u32x4*)p, c = *(LAS const u32x4*)(p + 16);
            v[0] = a[0]; v[1] = a[1]; v[2] = a[2]; v[3] = a[3]; v[4] = c[0]; v[5] = c[1]; v[6] = c[2]; v[7] = c[3];
            v[8] = *(LAS const unsigned*)(p + 32);
        };
        hstore(0);
        SCHED();
        unsigned qv[9];
        ld18(qt + lrow * QS + 96, qv);
        float lg[4];
#pragma unroll
        for (int ww = 0; ww < 4; ++ww) {
            const int wi = 4 * half + ww;
            unsigned kv[9];
            ld18(qt + (lrow - 2 * wi) * QS, kv);
            float a = 0.f;
#pragma unroll
            for (int i = 0; i < 9; ++i) a = __builtin_amdgcn_fdot2_f32_bf16(__builtin_bit_cast(bf16x2_t, qv[i]), __builtin_bit_cast(bf16x2_t, kv[i]), a, false);
            a = rbf(rbf(a) * 0.23570226039551584f);
            lg[ww] = (grow - 2 * wi >= 0) ? a : -1e9f;
        }
        SCHED();
        hstore(1);
        SCHED();
        float mx = fmaxf(fmaxf(lg[0], lg[1]), fmaxf(lg[2], lg[3]));
        mx = fmaxf(mx, __shfl_xor(mx, 1));
        float ex[4], sum = 0.f;
#pragma unroll
        for (int ww = 0; ww < 4; ++ww) { ex[ww] = __expf(lg[ww] - mx); sum += ex[ww]; }
        sum += __shfl_xor(sum, 1);
        const float inv = 1.0f / sum;
        float o[18];
#pragma unroll
        for (int i = 0; i < 18; ++i) o[i] = 0.f;
#pragma unroll
        for (int ww = 0; ww < 4; ++ww) {
            const int wi = 4 * half + ww;
            const float pw = (grow - 2 * wi >= 0) ? rbf(ex[ww] * inv) : 0.f;
            unsigned vv[9];
            ld18(qt + (lrow - 2 * wi) * QS + 48, vv);
#pragma unroll
            for (int i = 0; i < 9; ++i) { o[2 * i] += pw * bf_lo(vv[i]); o[2 * i + 1] += pw * bf_hi(vv[i]); }
        }
        SCHED();
        hstore(2);
        SCHED();
#pragma unroll
        for (int ii = 0; ii < 5; ++ii) {
            const float s0 = half ? o[2 * ii] : o[(10 + 2 * ii) % 18], s1 = half ? o[2 * ii + 1] : o[(11 + 2 * ii) % 18];
            const float r0 = __shfl_xor(s0, 1), r1 = __shfl_xor(s1, 1);
            const float m0 = half ? o[(10 + 2 * ii) % 18] : o[2 * ii], m1 = half ? o[(11 + 2 * ii) % 18] : o[2 * ii + 1];
            const int i = half * 5 + ii;
            if (i < 9) {
                const int col = FF + 18 * j + 2 * i;
                *(unsigned*)((char*)hb + (size_t)(col >> 5) * (BLK_ELEMS * 2) + blk_off(grow, (col >> 3) & 3) + (col & 7) * 2) = pk_bf16(m0 + r0, m1 + r1);
            }
        }
        SCHED();
        hstore(3);
    }
    STAMP(1, 15);
}

__global__ __launch_bounds__(512, 2) void k1_gateup(const u16* __restrict__ xnb, const u16* __restrict__ Wg,
                                                    const u16* __restrict__ Wu, const u16* __restrict__ wqh,
                                                    const u16* __restrict__ Wd, u16* __restrict__ hb) {
    extern __shared__ __attribute__((aligned(16))) char smem[];
    LAS char* lds = (LAS char*)smem;
    const int tid = threadIdx.x, lane = tid & 63;
    const int w = __builtin_amdgcn_readfirstlane(tid >> 6);
    const int b = blockIdx.x, xcd = b & 7, idx = b >> 3;
    const int j = 4 * xcd + (idx & 3), mt = idx >> 2;
    const int wm = w >> 1, wn = w & 1;
    const unsigned lds0 = (unsigned)(size_t)lds;
    const int fr = lane & 15, g = lane >> 4, q4 = (lane >> 2) & 3, p4 = lane & 3;

    f32x4 acc[11][4], ach;
    {
        const unsigned srcA = (unsigned)((256 * mt + 32 * w) * 32 + lane * 8) * 2u;
        const int koct = w >> 1, mat = w & 1;
        const u16* W = mat ? Wu : Wg;
        unsigned srcB, srcE, srcQ, srcH;
        {
            const int r = lane >> 3, swz = ((r >> 1) & 1) | ((koct & 1) << 1), c = (lane & 7) ^ (swz << 1);
            srcB = (unsigned)((8 * koct + r) * FF + 128 * j + 8 * c) * 2u;
            const int re = ((lane >> 1) & 7) ^ ((koct & 1) << 2), ce = (lane & 1) ^ mat;
            srcE = (unsigned)((8 * koct + re) * FF + 4096 + 16 * j + 8 * ce) * 2u;
            const int rq = 4 * mat + ((lane >> 3) & 3), swq = ((rq >> 1) & 1) | ((koct & 1) << 1), cq = (lane & 7) ^ (swq << 1);
            srcQ = (unsigned)((j * 576 + 8 * koct + rq) * 64 + 8 * cq) * 2u;
            int hr = 256 * mt - 16 + 2 * w + ((lane >> 2) & 1); hr = hr < 0 ? 0 : hr;
            srcH = (unsigned)(hr * 64 + (lane & 3) * 16);
        }
        const unsigned dA = lds0 + RA + (2 * w) * 1024, dB = lds0 + RB + (2 * w) * 1024, dE = lds0 + RE + (mat * 4 + koct) * 256;
        const unsigned dQ = lds0 + RQ + koct * 1024 + mat * 512, dH = lds0 + RH + w * 128;
        auto issue1 = [&](int t, int i) {
            const int sl = t & 3;
            const u16* ba = xnb + (size_t)t * BLK_ELEMS;
            const u16* bw = W + (size_t)t * 32 * FF;
            if (i == 0) glds16(bw, srcB, dB + sl * 16384);
            if (i == 1) glds16(bw + 64, srcB, dB + sl * 16384 + 1024);
            if (i == 2) glds16_q(bw, srcE, dE + sl * 2048);
            if (i == 3) glds16(ba, srcA, dA + sl * 16384);
            if (i == 4) glds16_o1k(ba, srcA, dA + sl * 16384);
            if (i == 5) glds16_32(wqh + (size_t)t * 32 * 64, srcQ, dQ + sl * 4096);
            if (i == 6) glds16_8(ba, srcH, dH + sl * 1024);
        };
        STAMP(1, 0);
#pragma unroll
        for (int t = 0; t < 3; ++t)
#pragma unroll
            for (int i = 0; i < 7; ++i) issue1(t, i);

        const unsigned pa = lds0 + RA + blk_off(64 * wm + fr, g);
        const int swz = ((q4 >> 1) & 1) | ((g & 1) << 1);
        const unsigned pb0 = lds0 + RB + (g * 4 + wn) * 1024 + q4 * 128 + (swz << 5) + p4 * 8;
        const int emat = p4 >> 1;
        const unsigned pe0 = lds0 + RE + (emat * 4 + g) * 256 + ((q4 ^ ((g & 1) << 2)) * 32) + ((wn ^ emat) * 16) + (p4 & 1) * 8;
        const unsigned pq0 = lds0 + RQ + g * 1024 + q4 * 128 + (((2 * wn + (wm & 1)) ^ swz) << 5) + p4 * 8;
        const int hdelta = RH - RA - 4096 * wm;
        auto xr = [&](unsigned base, int imm) -> LAS char* {
            if (imm == 0) return (LAS char*)(size_t)base;
            unsigned r; asm volatile("v_xor_b32 %0, %1, %2" : "=v"(r) : "i"(imm), "v"(base)); return (LAS char*)(size_t)r;
        };
        auto rdB = [&](int sl, int nf, int t1) -> s16x4 {
            if (nf < 8) return tr_read(xr(pb0, (nf >> 1) << 5) + sl * 16384 + (nf & 1) * 2048 + t1 * 512);
            if (nf == 8) return tr_read(xr(pe0, t1 * 128) + sl * 2048);
            return tr_read(xr(pq0, (nf - 9) << 5) + sl * 4096 + t1 * 512);
        };
        auto rdA = [&](int sl, int m) -> bf16x8 { return row_frag((LAS char*)(size_t)pa + sl * 16384 + m * 1024); };
        auto rdH = [&](int sl) -> bf16x8 {
            unsigned r; asm volatile("v_add_u32 %0, %1, %2" : "=v"(r) : "s"(hdelta), "v"(pa)); return row_frag((LAS char*)(size_t)r + sl * 1024);
        };
#pragma unroll
        for (int n = 0; n < 11; ++n)
#pragma unroll
            for (int m = 0; m < 4; ++m) { acc[n][m] = (f32x4){0.f, 0.f, 0.f, 0.f}; asm volatile("" : "+v"(acc[n][m])); }
        ach = (f32x4){0.f, 0.f, 0.f, 0.f}; asm volatile("" : "+v"(ach));

        wait_vm<14>(); BAR();
        STAMP(1, 1);
        bf16x8 fa[2][2], fc[2], fh, fb[NB];
#pragma unroll
        for (int m = 0; m < 2; ++m) { fa[0][m] = rdA(0, m); fc[m] = rdA(0, 2 + m); }
#pragma unroll
        for (int i = 0; i < PF; ++i) { s16x4 lo = rdB(0, i, 0), hi = rdB(0, i, 1); fb[i] = __builtin_shufflevector(lo, hi, 0, 1, 2, 3, 4, 5, 6, 7); }
#define MM(C, A, B) mfma_ip(C, A, B)
#pragma unroll
        for (int t = 0; t < NKB_X; ++t) {
            const int sl = t & 3, sn = (t + 1) & 3, cur = t & 1;
#pragma unroll
            for (int nf = 0; nf < 11; ++nf) {
                const int r = 11 * t + nf;
                const int psl = (nf + PF < 11) ? sl : sn, nn = (nf + PF < 11) ? nf + PF : nf + PF - 11;
                const bool pf = (nf + PF < 11) || (t + 1 < NKB_X);
                const bool lastg = (nf == 10);
                s16x4 lo, hi;
                if (!lastg) MM(acc[nf][0], fb[r % NB], fa[cur][0]); else MM(acc[nf][2], fb[r % NB], fc[0]);
                SCHED();
                if (nf == 0) {
                    if (t + 2 < NKB_X) wait_vm<7>(); else wait_vm<0>();
                    BAR();
                }
                if (pf) lo = rdB(psl, nn, 0);
                SCHED();
                if (!lastg) MM(acc[nf][1], fb[r % NB], fa[cur][1]); else MM(acc[nf][3], fb[r % NB], fc[1]);
                SCHED();
                if (pf) hi = rdB(psl, nn, 1);
                if (lastg && t + 1 < NKB_X) fc[0] = rdA(sn, 2);
                SCHED();
                if (!lastg) MM(acc[nf][2], fb[r % NB], fc[0]); else MM(acc[nf][0], fb[r % NB], fa[cur][0]);
                SCHED();
                if (lastg && t + 1 < NKB_X) fc[1] = rdA(sn, 3);
                if (nf >= 4 && nf < 6 && t + 1 < NKB_X) fa[cur ^ 1][nf - 4] = rdA(sn, nf - 4);
                if (nf == 7) fh = rdH(sl);
                if ((nf < 4 || (nf >= 6 && nf < 9)) && t + 3 < NKB_X) issue1(t + 3, nf < 4 ? nf : nf - 2);
                SCHED();
                if (!lastg) MM(acc[nf][3], fb[r % NB], fc[1]); else MM(acc[nf][1], fb[r % NB], fa[cur][1]);
                SCHED();
                if (nf == 9) { MM(ach, fb[r % NB], fh); SCHED(); }
                if (pf) fb[(r + PF) % NB] = __builtin_shufflevector(lo, hi, 0, 1, 2, 3, 4, 5, 6, 7);
            }
        }
    }
    asm volatile("s_nop 7\n\ts_nop 7" ::: "memory");
    BAR();
    STAMP(1, 2);
    k1_epilogue(acc, ach, lds, lds0, w, xcd, idx, j, mt, Wd, hb);
}

constexpr int K2_NT = 21, K2_LA = 5;
constexpr int K2_RB = 0, K2_RE = 131072, K2_LDS = 131072 + 16384;
constexpr int NSPLIT = 4;
__device__ __forceinline__ void gload2(bf16x8& d0, bf16x8& d1, const void* sbase, unsigned voff) {
    sbase = uni(sbase);
    asm volatile("s_nop 4\n\tglobal_load_dwordx4 %0, %2, %3\n\tglobal_load_dwordx4 %1, %2, %3 offset:1024"
                 : "=&v"(d0), "=&v"(d1) : "v"(voff), "s"(sbase) : "memory");
}
__global__ __launch_bounds__(512, 2) void k2_down(const u16* __restrict__ hb, const u16* __restrict__ Wd,
                                                  const u16* __restrict__ wo, u16* __restrict__ part) {
    extern __shared__ __attribute__((aligned(16))) char smem[];
    LAS char* lds = (LAS char*)smem;
    const int tid = threadIdx.x, lane = tid & 63;
    const int w = __builtin_amdgcn_readfirstlane(tid >> 6);
    const int b = blockIdx.x, xcd = b & 7, idx = b >> 3;
    const int split = xcd >> 1, mt = (xcd & 1) * 8 + (idx >> 2), nt = idx & 3;
    const int wm = w >> 1, kh = w & 1;
    const int T0 = split == 0 ? 0 : 21 + 20 * (split - 1);

    const unsigned srcA = (unsigned)(kh * (int)BLK_ELEMS * 2 + blk_off(128 * mt + 32 * wm + (lane & 15), lane >> 4));
    const int koct = w >> 1, sub = w & 1;
    unsigned offB, offE;
    {
        const int r = lane >> 3, swz = ((r >> 1) & 1) | ((koct & 1) << 1), c = (lane & 7) ^ (swz << 1);
        offB = (unsigned)((32 * sub + 8 * koct + r) * DM + 128 * nt + 8 * c) * 2u;
        const int re = ((lane >> 1) & 7) ^ ((koct & 1) << 2), ce = (lane & 1);
        offE = (unsigned)((32 * sub + 8 * koct + re) * DM + 512 + 16 * nt + 8 * ce) * 2u;
    }
    const unsigned offB1 = offB + 128;
    const unsigned lds0 = (unsigned)(size_t)lds;
    const unsigned dB = lds0 + K2_RB + (2 * w) * 1024, dE = lds0 + K2_RE + (sub * 4 + koct) * 256;
    bf16x8 fa[8][2];
    auto issue1 = [&](int t, int i) {
        const int sl = t & 7;
        int T = T0 + t; if (t == K2_NT - 1 && split != 0) T -= 1;
        const u16* Wt = T < 72 ? Wd + (size_t)T * 64 * DM : wo + (size_t)(T - 72) * 64 * DM;
        const u16* ba = hb + (size_t)T * 2 * BLK_ELEMS;
        if (i == 0) glds16(Wt, offB, dB + sl * 16384);
        if (i == 1) glds16(Wt, offB1, dB + sl * 16384 + 1024);
        if (i == 2) glds16_q(Wt, offE, dE + sl * 2048);
        if (i == 3) gload2(fa[t & 7][0], fa[t & 7][1], ba, srcA);
    };
    STAMP(2, 0);
#pragma unroll
    for (int t = 0; t < K2_LA; ++t)
#pragma unroll
        for (int i = 0; i < 4; ++i) issue1(t, i);

    const int fr = lane & 15, g = lane >> 4, q4 = (lane >> 2) & 3, p4 = lane & 3;
    const int swz = ((q4 >> 1) & 1) | ((g & 1) << 1);
    LAS char* pb[2][4];
#pragma unroll
    for (int u = 0; u < 4; ++u) { pb[0][u] = lds + K2_RB + (g * 4 + kh * 2) * 1024 + q4 * 128 + ((u ^ swz) << 5) + p4 * 8; pb[1][u] = pb[0][u] + 65536; }
    LAS char* pe0 = lds + K2_RE + (kh * 4 + g) * 256 + ((q4 ^ ((g & 1) << 2)) * 32) + p4 * 8;
    LAS char* pe1 = lds + K2_RE + (kh * 4 + g) * 256 + (((4 + q4) ^ ((g & 1) << 2)) * 32) + p4 * 8;
    auto rdB = [&](int sl, int nf, int t1) -> s16x4 {
        if (nf < 8) return tr_read(pb[sl >> 2][nf & 3] + (sl & 3) * 16384 + (nf >> 2) * 1024 + t1 * 512);
        return tr_read((t1 ? pe1 : pe0) + sl * 2048);
    };

    f32x4 acc[9][2];
#pragma unroll
    for (int n = 0; n < 9; ++n)
#pragma unroll
        for (int m = 0; m < 2; ++m) acc[n][m] = (f32x4){0.f, 0.f, 0.f, 0.f};

    WAIT_V(20); BAR();
    STAMP(2, 1);
    constexpr int PF2 = 4;
    bf16x8 fb[9];
#pragma unroll
    for (int i = 0; i < PF2; ++i) { s16x4 lo = rdB(0, i, 0), hi = rdB(0, i, 1); fb[i] = __builtin_shufflevector(lo, hi, 0, 1, 2, 3, 4, 5, 6, 7); }
#pragma unroll
    for (int t = 0; t < K2_NT; ++t) {
        const int sl = t & 7, sn = (t + 1) & 7;
        asm volatile("" : "+v"(fa[sl][0]), "+v"(fa[sl][1]));
        if (t == K2_NT - 1 && split != 0) {
#pragma unroll
            for (int m = 0; m < 2; ++m) fa[sl][m] = (bf16x8){0, 0, 0, 0, 0, 0, 0, 0};
        }
#pragma unroll
        for (int nf = 0; nf < 9; ++nf) {
            const int psl = (nf + PF2 < 9) ? sl : sn, nn = (nf + PF2 < 9) ? nf + PF2 : nf + PF2 - 9;
            const bool pf = (nf + PF2 < 9) || (t + 1 < K2_NT);
            s16x4 lo, hi;
            acc[nf][0] = MFMA16(fb[nf], fa[sl][0], acc[nf][0]);
            SCHED();
            if (nf == 0 && (t & 1) == 0) {
                const int last = (t + K2_LA - 1 < K2_NT - 1) ? t + K2_LA - 1 : K2_NT - 1;
                const int n = last - (t + 2) > 0 ? (last - (t + 2)) * 5 : 0;
                if (n == 10) { WAIT_V(10); } else if (n == 5) { WAIT_V(5); } else { WAIT_V(0); }
                BAR();
            }
            if (pf) lo = rdB(psl, nn, 0);
            SCHED();
            acc[nf][1] = MFMA16(fb[nf], fa[sl][1], acc[nf][1]);
            SCHED();
            if (pf) hi = rdB(psl, nn, 1);
            if (nf >= 1 && nf < 5 && t + K2_LA < K2_NT) issue1(t + K2_LA, nf - 1);
            SCHED();
            if (pf) fb[nn] = __builtin_shufflevector(lo, hi, 0, 1, 2, 3, 4, 5, 6, 7);
        }
    }
    BAR();
    STAMP(2, 2);
    {
        LAS char* xw = lds + w * 10240;
        LAS char* xr = lds + (w ^ 1) * 10240;
#pragma unroll
        for (int n = 0; n < 9; ++n) {
            const bool give = kh ? (n < 5) : (n >= 5);
            if (give) {
#pragma unroll
                for (int m = 0; m < 2; ++m) *(LAS f32x4*)(xw + ((kh ? n : n - 5) * 2 + m) * 1024 + lane * 16) = acc[n][m];
            }
        }
        asm volatile("s_waitcnt lgkmcnt(0)" ::: "memory");
        BAR();
        u16* pp = part + (size_t)split * SEQ * DM;
#pragma unroll
        for (int n = 0; n < 9; ++n) {
            const bool keep = kh ? (n >= 5) : (n < 5);
            if (keep) {
#pragma unroll
                for (int m = 0; m < 2; ++m) {
                    const f32x4 o = *(LAS f32x4*)(xr + ((kh ? n - 5 : n) * 2 + m) * 1024 + lane * 16) + acc[n][m];
                    const int row = 128 * mt + 32 * wm + 16 * m + fr;
                    const int col = (n < 8 ? 128 * nt + 16 * n : 512 + 16 * nt) + 4 * g;
                    u32x2 v; v[0] = pk_bf16(o[0], o[1]); v[1] = pk_bf16(o[2], o[3]);
                    *(u32x2*)(pp + (size_t)row * DM + col) = v;
                }
            }
        }
    }
    STAMP(2, 15);
}

__global__ __launch_bounds__(256) void k3_reduce(const u16* __restrict__ x, const u16* __restrict__ part, u16* __restrict__ out) {
    const int i = blockIdx.x * 256 + threadIdx.x;
    const size_t e = (size_t)i * 8;
    constexpr size_t PS = (size_t)SEQ * DM;
    const u32x4 xv = *(const u32x4*)(x + e);
    u32x4 pv[NSPLIT];
#pragma unroll
    for (int s = 0; s < NSPLIT; ++s) pv[s] = *(const u32x4*)(part + s * PS + e);
    u32x4 o;
#pragma unroll
    for (int k = 0; k < 4; ++k) {
        float lo = 0.f, hi = 0.f;
#pragma unroll
        for (int s = 0; s < NSPLIT; ++s) { lo += bf_lo(pv[s][k]); hi += bf_hi(pv[s][k]); }
        o[k] = pk_bf16(lo + bf_lo(xv[k]), hi + bf_hi(xv[k]));
    }
    *(u32x4*)(out + e) = o;
}

extern "C" void kernel_launch(void* const* d_in, const int* in_sizes, int n_in, void* d_out, int out_size, void* d_ws,
                              size_t ws_size, hipStream_t stream) {
    const u16* x = (const u16*)d_in[0];
    const u16* wqkv = (const u16*)d_in[1];
    const u16* wo = (const u16*)d_in[2];
    const u16* Wg = (const u16*)d_in[4];
    const u16* Wu = (const u16*)d_in[5];
    const u16* Wd = (const u16*)d_in[6];
    char* ws = (char*)d_ws;
    u16* xnb = (u16*)ws;
    u16* hb = (u16*)(ws + (4u << 20));
    u16* part = (u16*)(ws + (32u << 20));
    u16* wqh = (u16*)(ws + (48u << 20));
    (void)hipFuncSetAttribute((const void*)k1_gateup, hipFuncAttributeMaxDynamicSharedMemorySize, K1_LDS);
    (void)hipFuncSetAttribute((const void*)k2_down, hipFuncAttributeMaxDynamicSharedMemorySize, K2_LDS);
    k0_ln<<<256, 256, 0, stream>>>(x, xnb, wqkv, wqh);
    k1_gateup<<<256, 512, K1_LDS, stream>>>(xnb, Wg, Wu, wqh, Wd, hb);
    k2_down<<<256, 512, K2_LDS, stream>>>(hb, Wd, wo, part);
    k3_reduce<<<SEQ * DM / 8 / 256, 256, 0, stream>>>(x, part, (u16*)d_out);
}
```

```cpp
#include <hip/hip_runtime.h>
#include <hip/hip_bf16.h>
#include <stdint.h>
#include <type_traits>

typedef unsigned short u16;
typedef short bf16x8 __attribute__((ext_vector_type(8)));
typedef short s16x4 __attribute__((ext_vector_type(4)));
typedef float f32x4 __attribute__((ext_vector_type(4)));
typedef float f32x2 __attribute__((ext_vector_type(2)));
typedef __bf16 bf16x2_t __attribute__((ext_vector_type(2)));
typedef unsigned u32x4 __attribute__((ext_vector_type(4)));
typedef unsigned u32x2 __attribute__((ext_vector_type(2)));
#define LAS __attribute__((address_space(3)))
#define GAS __attribute__((address_space(1)))

constexpr int SEQ = 2048, DM = 576, FF = 4608;
constexpr int NKB_X = 18;
constexpr int NKB_H = 162;
constexpr size_t BLK_ELEMS = (size_t)SEQ * 32;

__device__ __forceinline__ unsigned pk_bf16(float lo, float hi) {
    f32x2 v = {lo, hi};
    bf16x2_t b = __builtin_convertvector(v, bf16x2_t);
    return __builtin_bit_cast(unsigned, b);
}
__device__ __forceinline__ float bf_lo(unsigned u) { return __uint_as_float(u << 16); }
__device__ __forceinline__ float bf_hi(unsigned u) { return __uint_as_float(u & 0xffff0000u); }
__device__ __forceinline__ float rbf(float x) { return bf_lo(pk_bf16(x, x)); }

__device__ __forceinline__ const void* uni(const void* p) {
    const unsigned long long v = (unsigned long long)p;
    const unsigned lo = __builtin_amdgcn_readfirstlane((unsigned)v), hi = __builtin_amdgcn_readfirstlane((unsigned)(v >> 32));
    return (const void*)(((unsigned long long)hi << 32) | lo);
}
__device__ __forceinline__ void glds16(const void* sbase, unsigned voff, unsigned lds) {
    sbase = uni(sbase); lds = __builtin_amdgcn_readfirstlane(lds);
    unsigned sv;
    asm volatile("s_mov_b32 %0, m0\n\ts_mov_b32 m0, %3\n\ts_nop 3\n\tglobal_load_lds_dwordx4 %1, %2\n\ts_mov_b32 m0, %0"
                 : "=&s"(sv) : "v"(voff), "s"(sbase), "s"(lds) : "memory");
}
__device__ __forceinline__ void glds16_o1k(const void* sbase, unsigned voff, unsigned lds) {
    sbase = uni(sbase); lds = __builtin_amdgcn_readfirstlane(lds);
    unsigned sv;
    asm volatile("s_mov_b32 %0, m0\n\ts_mov_b32 m0, %3\n\ts_nop 3\n\tglobal_load_lds_dwordx4 %1, %2 offset:1024\n\ts_mov_b32 m0, %0"
                 : "=&s"(sv) : "v"(voff), "s"(sbase), "s"(lds) : "memory");
}
__device__ __forceinline__ void glds16_q(const void* sbase, unsigned voff, unsigned lds) {
    sbase = uni(sbase); lds = __builtin_amdgcn_readfirstlane(lds);
    unsigned sv; unsigned long long ex;
    asm volatile("s_mov_b32 %0, m0\n\ts_mov_b64 %1, exec\n\ts_mov_b32 m0, %4\n\ts_mov_b64 exec, 0xffff\n\ts_nop 1\n\tglobal_load_lds_dwordx4 %2, %3\n\ts_mov_b64 exec, %1\n\ts_mov_b32 m0, %0"
                 : "=&s"(sv), "=&s"(ex) : "v"(voff), "s"(sbase), "s"(lds) : "memory");
}
__device__ __forceinline__ void glds16_m(const void* sbase, unsigned voff, unsigned lds) {
    sbase = uni(sbase); lds = __builtin_amdgcn_readfirstlane(lds);
    unsigned sv; unsigned long long ex;
    asm volatile("s_mov_b32 %0, m0\n\ts_mov_b64 %1, exec\n\ts_mov_b32 m0, %4\n\ts_mov_b32 exec_lo, -1\n\ts_mov_b32 exec_hi, 0xff\n\ts_nop 1\n\tglobal_load_lds_dwordx4 %2, %3\n\ts_mov_b64 exec, %1\n\ts_mov_b32 m0, %0"
                 : "=&s"(sv), "=&s"(ex) : "v"(voff), "s"(sbase), "s"(lds) : "memory");
}
#define SCHED() __builtin_amdgcn_sched_barrier(0)
#define STAMP(kid, i) do {} while (0)
#define WSTAMP(i) do {} while (0)
__device__ __forceinline__ s16x4 tr_read(LAS char* p) {
    return __builtin_bit_cast(s16x4, __builtin_amdgcn_ds_read_tr16_b64_v4i16((LAS s16x4*)p));
}
__device__ __forceinline__ bf16x8 tr_frag(LAS char* p, int off_t1) {
    s16x4 lo = tr_read(p), hi = tr_read(p + off_t1);
    return __builtin_shufflevector(lo, hi, 0, 1, 2, 3, 4, 5, 6, 7);
}
__device__ __forceinline__ bf16x8 row_frag(LAS char* p) { return *(LAS bf16x8*)p; }
#define MFMA16(a, b, c) __builtin_amdgcn_mfma_f32_16x16x32_bf16((a), (b), (c), 0, 0, 0)
__device__ __forceinline__ void mfma_ip(f32x4& c, bf16x8 a, bf16x8 b) { asm volatile("v_mfma_f32_16x16x32_bf16 %0, %1, %2, %0" : "+v"(c) : "v"(a), "v"(b)); }
#define WAIT_V(n) asm volatile("s_waitcnt vmcnt(" #n ")" ::: "memory")
#define BAR() do { asm volatile("" ::: "memory"); __builtin_amdgcn_s_barrier(); asm volatile("" ::: "memory"); } while (0)

__device__ __forceinline__ int blk_off(int row, int chunk) { return row * 64 + ((chunk ^ (((row >> 3) & 1) << 1)) << 4); }

__global__ __launch_bounds__(256) void k0_ln(const u16* __restrict__ x, u16* __restrict__ xnb,
                                            const u16* __restrict__ wqkv, u16* __restrict__ wqh) {
    if (threadIdx.x >= 64) {
        const int l = threadIdx.x - 64, head = blockIdx.x >> 3, k0 = (blockIdx.x & 7) * 72;
        unsigned v[12];
#pragma unroll
        for (int i = 0; i < 12; ++i) {
            const int id = l + 192 * i, k = k0 + (id >> 5), cp = id & 31;
            const int part = cp >= 18 ? 2 : (cp >= 9 ? 1 : 0), d2 = cp - part * 9;
            const int src = (part == 2 ? 0 : part + 1) * DM + 18 * head + 2 * d2;
            v[i] = cp < 27 ? *(const unsigned*)(wqkv + (size_t)k * (3 * DM) + src) : 0u;
        }
#pragma unroll
        for (int i = 0; i < 12; ++i) {
            const int id = l + 192 * i;
            *(unsigned*)(wqh + ((size_t)head * 576 + k0) * 64 + 2 * id) = v[i];
        }
        return;
    }
    const int lane = threadIdx.x, i = lane & 7, row = blockIdx.x * 8 + (lane >> 3);
    u32x4 xv[9];
#pragma unroll
    for (int cc = 0; cc < 9; ++cc) xv[cc] = *(const u32x4*)(x + (size_t)row * DM + (i + 8 * cc) * 8);
    float s = 0.f;
#pragma unroll
    for (int cc = 0; cc < 9; ++cc)
#pragma unroll
        for (int e = 0; e < 4; ++e) s += bf_lo(xv[cc][e]) + bf_hi(xv[cc][e]);
    s += __shfl_xor(s, 1); s += __shfl_xor(s, 2); s += __shfl_xor(s, 4);
    const float mean = s * (1.0f / 576.0f);
    float q = 0.f;
#pragma unroll
    for (int cc = 0; cc < 9; ++cc)
#pragma unroll
        for (int e = 0; e < 4; ++e) { const float a = bf_lo(xv[cc][e]) - mean, c2 = bf_hi(xv[cc][e]) - mean; q += a * a + c2 * c2; }
    q += __shfl_xor(q, 1); q += __shfl_xor(q, 2); q += __shfl_xor(q, 4);
    const float mb = rbf(mean), vb = rbf(q * (1.0f / 576.0f));
    const float sb = rbf(sqrtf(rbf(vb + 1e-5f)));
    const float rs = 1.0f / sb;
#pragma unroll
    for (int cc = 0; cc < 9; ++cc) {
        u32x4 o;
#pragma unroll
        for (int e = 0; e < 4; ++e) {
            const unsigned d = pk_bf16(bf_lo(xv[cc][e]) - mb, bf_hi(xv[cc][e]) - mb);
            o[e] = pk_bf16(bf_lo(d) * rs, bf_hi(d) * rs);
        }
        const int blk = 2 * cc + (i >> 2);
        *(u32x4*)((char*)xnb + (size_t)blk * (BLK_ELEMS * 2) + blk_off(row, i & 3)) = o;
    }
}

constexpr int RA = 0, RB = 65536, RE = 131072, RQ = 139264, RH = 155648;
constexpr int T_DUMMY = 159744;
constexpr int K1_LDS = T_DUMMY + 1024;
constexpr int QS = 144;
#define HST_AUX 16
#define PF 3
constexpr int NB = PF + 1;
template <int N> __device__ __forceinline__ void wait_vm() { asm volatile("s_waitcnt vmcnt(%0)" :: "i"(N) : "memory"); }
#define GLDS_MASKED(NAME, LO, HI) \
__device__ __forceinline__ void NAME(const void* sbase, unsigned voff, unsigned lds) { \
    sbase = uni(sbase); lds = __builtin_amdgcn_readfirstlane(lds); \
    unsigned sv; unsigned long long ex; \
    asm volatile("s_mov_b32 %0, m0\n\ts_mov_b64 %1, exec\n\ts_mov_b32 m0, %4\n\ts_mov_b32 exec_lo, " #LO "\n\ts_mov_b32 exec_hi, " #HI "\n\tglobal_load_lds_dwordx4 %2, %3\n\ts_mov_b64 exec, %1\n\ts_mov_b32 m0, %0" \
                 : "=&s"(sv), "=&s"(ex) : "v"(voff), "s"(sbase), "s"(lds) : "memory"); }
GLDS_MASKED(glds16_32, -1, 0)
GLDS_MASKED(glds16_8, 0xff, 0)

__device__ __forceinline__ void k1_epilogue(f32x4 (&acc)[11][4], f32x4& ach, LAS char* lds, unsigned lds0, int w, int xcd, int idx,
                                            int j, int mt, const u16* __restrict__ Wd, u16* __restrict__ hb) {
    int tid = threadIdx.x; asm volatile("" : "+v"(tid));
    const int lane = tid & 63, fr = lane & 15, g = lane >> 4;
    const int wm = w >> 1, wn = w & 1;
    {
        const int split = xcd >> 1;
        const unsigned base = (unsigned)(split * 1152 * DM * 2 + (idx * 8 + w) * 6144);
#pragma unroll
        for (int i = 0; i < 6; ++i) {
            unsigned o = base + i * 1024; o = o + 1024 <= (unsigned)(FF * DM * 2) ? o : 0u;
            glds16((const char*)Wd + o, (unsigned)lane * 16u, lds0 + T_DUMMY);
        }
    }
    LAS char* qt = lds;
    {
#pragma unroll
        for (int u = 0; u < 2; ++u) {
            const int nn = 16 * (2 * wn + (u ^ (wm & 1))) + 4 * g;
            const int h0 = nn / 18, h1 = (nn + 2) / 18;
            const int o0 = h0 * 48 + (nn - h0 * 18) * 2, o1 = h1 * 48 + (nn + 2 - h1 * 18) * 2;
#pragma unroll
            for (int m = 0; m < 4; ++m) {
                LAS char* rp = qt + (16 + 64 * wm + 16 * m + fr) * QS;
                if (h0 < 3) *(LAS unsigned*)(rp + o0) = pk_bf16(acc[9 + u][m][0], acc[9 + u][m][1]);
                if (h1 < 3) *(LAS unsigned*)(rp + o1) = pk_bf16(acc[9 + u][m][2], acc[9 + u][m][3]);
            }
            if (u == 0 && (w == 0 || w == 2 || w == 1)) {
                LAS char* rp = qt + fr * QS;
                if (h0 < 3) *(LAS unsigned*)(rp + o0) = pk_bf16(ach[0], ach[1]);
                if (h1 < 3) *(LAS unsigned*)(rp + o1) = pk_bf16(ach[2], ach[3]);
            }
        }
    }
    asm volatile("s_waitcnt lgkmcnt(0)" ::: "memory");
    BAR();
    STAMP(1, 3);
    const __amdgpu_buffer_rsrc_t rh = __builtin_amdgcn_make_buffer_rsrc((void*)hb, 0, (int)(NKB_H * BLK_ELEMS * 2), 0x00020000);
    u32x4 hp[4][2]; u32x2 hm[4];
#pragma unroll
    for (int m = 0; m < 4; ++m) {
#pragma unroll
        for (int pp = 0; pp < 2; ++pp) {
            unsigned v[2][2];
#pragma unroll
            for (int hh = 0; hh < 2; ++hh) {
                const int gt = 2 * pp + hh;
                float hv[4];
#pragma unroll
                for (int e = 0; e < 4; ++e) hv[e] = fmaxf(acc[2 * gt][m][e], 0.f) * acc[2 * gt + 1][m][e];
                v[hh][0] = pk_bf16(hv[0], hv[1]); v[hh][1] = pk_bf16(hv[2], hv[3]);
            }
            auto r0 = __builtin_amdgcn_permlane16_swap(v[0][0], v[1][0], false, false);
            auto r1 = __builtin_amdgcn_permlane16_swap(v[0][1], v[1][1], false, false);
            hp[m][pp][0] = r0[0]; hp[m][pp][1] = r1[0]; hp[m][pp][2] = r0[1]; hp[m][pp][3] = r1[1];
        }
        {
            float hv[4];
#pragma unroll
            for (int e = 0; e < 4; ++e) {
                const unsigned mine = __float_as_uint(acc[8][m][e]);
                auto r = __builtin_amdgcn_permlane32_swap(mine, mine, false, false);
                hv[e] = fmaxf(__uint_as_float(r[0]), 0.f) * __uint_as_float(r[1]);
            }
            hm[m][0] = pk_bf16(hv[0], hv[1]); hm[m][1] = pk_bf16(hv[2], hv[3]);
        }
        asm volatile("" : "+v"(hp[m][0]), "+v"(hp[m][1]), "+v"(hm[m]));
    }
    SCHED();
    auto hstore = [&](int m) {
        const int row = 256 * mt + 64 * wm + 16 * m + fr;
#pragma unroll
        for (int pp = 0; pp < 2; ++pp) {
            const int blk = 4 * j + 2 * wn + pp, chunk = ((g & 1) << 1) | (g >> 1);
            __builtin_amdgcn_raw_buffer_store_b128(hp[m][pp], rh, (unsigned)(blk * (int)(BLK_ELEMS * 2) + blk_off(row, chunk)), 0, HST_AUX);
        }
        const int blk = 128 + (j >> 1), chunk = (j & 1) * 2 + wn;
        const unsigned off = g < 2 ? (unsigned)(blk * (int)(BLK_ELEMS * 2) + blk_off(row, chunk) + g * 8) : 0xfffffff0u;
        __builtin_amdgcn_raw_buffer_store_b64(hm[m], rh, off, 0, HST_AUX);
    };
    {
        const int s = tid >> 1, half = tid & 1;
        const int lrow = 16 + s, grow = 256 * mt + s;
        auto ld18 = [&](LAS const char* p, unsigned (&v)[9]) {
            const u32x4 a = *(LAS const u32x4*)p, c = *(LAS const u32x4*)(p + 16);
            v[0] = a[0]; v[1] = a[1]; v[2] = a[2]; v[3] = a[3]; v[4] = c[0]; v[5] = c[1]; v[6] = c[2]; v[7] = c[3];
            v[8] = *(LAS const unsigned*)(p + 32);
        };
        hstore(0);
        SCHED();
        unsigned qv[9];
        ld18(qt + lrow * QS + 96, qv);
        float lg[4];
#pragma unroll
        for (int ww = 0; ww < 4; ++ww) {
            const int wi = 4 * half + ww;
            unsigned kv[9];
            ld18(qt + (lrow - 2 * wi) * QS, kv);
            float a = 0.f;
#pragma unroll
            for (int i = 0; i < 9; ++i) a = __builtin_amdgcn_fdot2_f32_bf16(__builtin_bit_cast(bf16x2_t, qv[i]), __builtin_bit_cast(bf16x2_t, kv[i]), a, false);
            a = rbf(rbf(a) * 0.23570226039551584f);
            lg[ww] = (grow - 2 * wi >= 0) ? a : -1e9f;
        }
        SCHED();
        hstore(1);
        SCHED();
        float mx = fmaxf(fmaxf(lg[0], lg[1]), fmaxf(lg[2], lg[3]));
        mx = fmaxf(mx, __shfl_xor(mx, 1));
        float ex[4], sum = 0.f;
#pragma unroll
        for (int ww = 0; ww < 4; ++ww) { ex[ww] = __expf(lg[ww] - mx); sum += ex[ww]; }
        sum += __shfl_xor(sum, 1);
        const float inv = 1.0f / sum;
        float o[18];
#pragma unroll
        for (int i = 0; i < 18; ++i) o[i] = 0.f;
#pragma unroll
        for (int ww = 0; ww < 4; ++ww) {
            const int wi = 4 * half + ww;
            const float pw = (grow - 2 * wi >= 0) ? rbf(ex[ww] * inv) : 0.f;
            unsigned vv[9];
            ld18(qt + (lrow - 2 * wi) * QS + 48, vv);
#pragma unroll
            for (int i = 0; i < 9; ++i) { o[2 * i] += pw * bf_lo(vv[i]); o[2 * i + 1] += pw * bf_hi(vv[i]); }
        }
        SCHED();
        hstore(2);
        SCHED();
#pragma unroll
        for (int ii = 0; ii < 5; ++ii) {
            const float s0 = half ? o[2 * ii] : o[(10 + 2 * ii) % 18], s1 = half ? o[2 * ii + 1] : o[(11 + 2 * ii) % 18];
            const float r0 = __shfl_xor(s0, 1), r1 = __shfl_xor(s1, 1);
            const float m0 = half ? o[(10 + 2 * ii) % 18] : o[2 * ii], m1 = half ? o[(11 + 2 * ii) % 18] : o[2 * ii + 1];
            const int i = half * 5 + ii;
            if (i < 9) {
                const int col = FF + 18 * j + 2 * i;
                *(unsigned*)((char*)hb + (size_t)(col >> 5) * (BLK_ELEMS * 2) + blk_off(grow, (col >> 3) & 3) + (col & 7) * 2) = pk_bf16(m0 + r0, m1 + r1);
            }
        }
        SCHED();
        hstore(3);
    }
    STAMP(1, 15);
}

__global__ __launch_bounds__(512, 2) void k1_gateup(const u16* __restrict__ xnb, const u16* __restrict__ Wg,
                                                    const u16* __restrict__ Wu, const u16* __restrict__ wqh,
                                                    const u16* __restrict__ Wd, u16* __restrict__ hb) {
    extern __shared__ __attribute__((aligned(16))) char smem[];
    LAS char* lds = (LAS char*)smem;
    const int tid = threadIdx.x, lane = tid & 63;
    const int w = __builtin_amdgcn_readfirstlane(tid >> 6);
    const int b = blockIdx.x, xcd = b & 7, idx = b >> 3;
    const int j = 4 * xcd + (idx & 3), mt = idx >> 2;
    const int wm = w >> 1, wn = w & 1;
    const unsigned lds0 = (unsigned)(size_t)lds;
    const int fr = lane & 15, g = lane >> 4, q4 = (lane >> 2) & 3, p4 = lane & 3;

    f32x4 acc[11][4], ach;
    {
        const unsigned srcA = (unsigned)((256 * mt + 32 * w) * 32 + lane * 8) * 2u;
        const int koct = w >> 1, mat = w & 1;
        const u16* W = mat ? Wu : Wg;
        unsigned srcB, srcE, srcQ, srcH;
        {
            const int r = lane >> 3, swz = ((r >> 1) & 1) | ((koct & 1) << 1), c = (lane & 7) ^ (swz << 1);
            srcB = (unsigned)((8 * koct + r) * FF + 128 * j + 8 * c) * 2u;
            const int re = ((lane >> 1) & 7) ^ ((koct & 1) << 2), ce = (lane & 1) ^ mat;
            srcE = (unsigned)((8 * koct + re) * FF + 4096 + 16 * j + 8 * ce) * 2u;
            const int rq = 4 * mat + ((lane >> 3) & 3), swq = ((rq >> 1) & 1) | ((koct & 1) << 1), cq = (lane & 7) ^ (swq << 1);
            srcQ = (unsigned)((j * 576 + 8 * koct + rq) * 64 + 8 * cq) * 2u;
            int hr = 256 * mt - 16 + 2 * w + ((lane >> 2) & 1); hr = hr < 0 ? 0 : hr;
            srcH = (unsigned)(hr * 64 + (lane & 3) * 16);
        }
        const unsigned dA = lds0 + RA + (2 * w) * 1024, dB = lds0 + RB + (2 * w) * 1024, dE = lds0 + RE + (mat * 4 + koct) * 256;
        const unsigned dQ = lds0 + RQ + koct * 1024 + mat * 512, dH = lds0 + RH + w * 128;
        auto issue1 = [&](int t, int sl, int i) {
            const u16* ba = xnb + (size_t)t * BLK_ELEMS;
            const u16* bw = W + (size_t)t * 32 * FF;
            if (i == 0) glds16(bw, srcB, dB + sl * 16384);
            if (i == 1) glds16(bw + 64, srcB, dB + sl * 16384 + 1024);
            if (i == 2) glds16_q(bw, srcE, dE + sl * 2048);
            if (i == 3) glds16(ba, srcA, dA + sl * 16384);
            if (i == 4) glds16_o1k(ba, srcA, dA + sl * 16384);
            if (i == 5) glds16_32(wqh + (size_t)t * 32 * 64, srcQ, dQ + sl * 4096);
            if (i == 6) glds16_8(ba, srcH, dH + sl * 1024);
        };
        STAMP(1, 0);
#pragma unroll
        for (int t = 0; t < 3; ++t)
#pragma unroll
            for (int i = 0; i < 7; ++i) issue1(t, t, i);

        const unsigned pa = lds0 + RA + blk_off(64 * wm + fr, g);
        const int swz = ((q4 >> 1) & 1) | ((g & 1) << 1);
        const unsigned pb0 = lds0 + RB + (g * 4 + wn) * 1024 + q4 * 128 + (swz << 5) + p4 * 8;
        const int emat = p4 >> 1;
        const unsigned pe0 = lds0 + RE + (emat * 4 + g) * 256 + ((q4 ^ ((g & 1) << 2)) * 32) + ((wn ^ emat) * 16) + (p4 & 1) * 8;
        const unsigned pq0 = lds0 + RQ + g * 1024 + q4 * 128 + (((2 * wn + (wm & 1)) ^ swz) << 5) + p4 * 8;
        const int hdelta = RH - RA - 4096 * wm;
        auto xr = [&](unsigned base, int imm) -> LAS char* {
            if (imm == 0) return (LAS char*)(size_t)base;
            unsigned r; asm volatile("v_xor_b32 %0, %1, %2" : "=v"(r) : "i"(imm), "v"(base)); return (LAS char*)(size_t)r;
        };
        auto rdB = [&](int sl, int nf, int t1) -> s16x4 {
            if (nf < 8) return tr_read(xr(pb0, (nf >> 1) << 5) + sl * 16384 + (nf & 1) * 2048 + t1 * 512);
            if (nf == 8) return tr_read(xr(pe0, t1 * 128) + sl * 2048);
            return tr_read(xr(pq0, (nf - 9) << 5) + sl * 4096 + t1 * 512);
        };
        auto rdA = [&](int sl, int m) -> bf16x8 { return row_frag((LAS char*)(size_t)pa + sl * 16384 + m * 1024); };
        auto rdH = [&](int sl) -> bf16x8 {
            unsigned r; asm volatile("v_add_u32 %0, %1, %2" : "=v"(r) : "s"(hdelta), "v"(pa)); return row_frag((LAS char*)(size_t)r + sl * 1024);
        };
#pragma unroll
        for (int n = 0; n < 11; ++n)
#pragma unroll
            for (int m = 0; m < 4; ++m) { acc[n][m] = (f32x4){0.f, 0.f, 0.f, 0.f}; asm volatile("" : "+v"(acc[n][m])); }
        ach = (f32x4){0.f, 0.f, 0.f, 0.f}; asm volatile("" : "+v"(ach));

        wait_vm<14>(); BAR();
        STAMP(1, 1);
        bf16x8 fa[2][2], fc[2], fh, fb[NB];
#pragma unroll
        for (int m = 0; m < 2; ++m) { fa[0][m] = rdA(0, m); fc[m] = rdA(0, 2 + m); }
#pragma unroll
        for (int i = 0; i < PF; ++i) { s16x4 lo = rdB(0, i, 0), hi = rdB(0, i, 1); fb[i] = __builtin_shufflevector(lo, hi, 0, 1, 2, 3, 4, 5, 6, 7); }
#define MM(C, A, B) mfma_ip(C, A, B)
        auto step = [&](auto S_, int t, bool do_issue, auto LAST1_, auto WAIT0_) {
            constexpr int S = decltype(S_)::value;
            constexpr bool has_next = !decltype(LAST1_)::value;
            constexpr bool wait0 = decltype(WAIT0_)::value;
            constexpr int sl = S, sn = (S + 1) & 3, cur = S & 1;
#pragma unroll
            for (int nf = 0; nf < 11; ++nf) {
                constexpr int dummy = 0; (void)dummy;
                const int r = 11 * S + nf;
                const int psl = (nf + PF < 11) ? sl : sn, nn = (nf + PF < 11) ? nf + PF : nf + PF - 11;
                const bool pf = (nf + PF < 11) || has_next;
                const bool lastg = (nf == 10);
                s16x4 lo, hi;
                if (!lastg) MM(acc[nf][0], fb[r % NB], fa[cur][0]); else MM(acc[nf][2], fb[r % NB], fc[0]);
                SCHED();
                if (nf == 0) {
                    if (!wait0) wait_vm<7>(); else wait_vm<0>();
                    BAR();
                }
                if (pf) lo = rdB(psl, nn, 0);
                SCHED();
                if (!lastg) MM(acc[nf][1], fb[r % NB], fa[cur][1]); else MM(acc[nf][3], fb[r % NB], fc[1]);
                SCHED();
                if (pf) hi = rdB(psl, nn, 1);
                if (lastg && has_next) fc[0] = rdA(sn, 2);
                SCHED();
                if (!lastg) MM(acc[nf][2], fb[r % NB], fc[0]); else MM(acc[nf][0], fb[r % NB], fa[cur][0]);
                SCHED();
                if (lastg && has_next) fc[1] = rdA(sn, 3);
                if (nf >= 4 && nf < 6 && has_next) fa[cur ^ 1][nf - 4] = rdA(sn, nf - 4);
                if (nf == 7) fh = rdH(sl);
                if ((nf < 4 || (nf >= 6 && nf < 9)) && do_issue) issue1(t + 3, (S + 3) & 3, nf < 4 ? nf : nf - 2);
                SCHED();
                if (!lastg) MM(acc[nf][3], fb[r % NB], fc[1]); else MM(acc[nf][1], fb[r % NB], fa[cur][1]);
                SCHED();
                if (nf == 9) { MM(ach, fb[r % NB], fh); SCHED(); }
                if (pf) fb[(r + PF) % NB] = __builtin_shufflevector(lo, hi, 0, 1, 2, 3, 4, 5, 6, 7);
            }
        };
        using I0 = std::integral_constant<int, 0>; using I1 = std::integral_constant<int, 1>;
        using I2 = std::integral_constant<int, 2>; using I3 = std::integral_constant<int, 3>;
        using F = std::false_type; using T = std::true_type;
#pragma unroll 1
        for (int t0 = 0; t0 < 16; t0 += 4) {
            step(I0{}, t0, true, F{}, F{});
            step(I1{}, t0 + 1, true, F{}, F{});
            step(I2{}, t0 + 2, true, F{}, F{});
            step(I3{}, t0 + 3, t0 + 3 < 15, F{}, F{});
        }
        step(I0{}, 16, false, F{}, T{});
        step(I1{}, 17, false, T{}, T{});
    }
    asm volatile("s_nop 7\n\ts_nop 7" ::: "memory");
    BAR();
    STAMP(1, 2);
    k1_epilogue(acc, ach, lds, lds0, w, xcd, idx, j, mt, Wd, hb);
}

constexpr int K2_NT = 21;
constexpr int K2_RB = 0, K2_RE = 65536, K2_LDS = 81920;
constexpr int NSPLIT = 4;
__device__ __forceinline__ void gload2(bf16x8& d0, bf16x8& d1, const void* sbase, unsigned voff) {
    sbase = uni(sbase);
    asm volatile("s_nop 4\n\tglobal_load_dwordx4 %0, %2, %3\n\tglobal_load_dwordx4 %1, %2, %3 offset:1024"
                 : "=&v"(d0), "=&v"(d1) : "v"(voff), "s"(sbase) : "memory");
}
__global__ __launch_bounds__(512, 2) void k2_down(const u16* __restrict__ hb, const u16* __restrict__ Wd,
                                                  const u16* __restrict__ wo, u16* __restrict__ part) {
    extern __shared__ __attribute__((aligned(16))) char smem[];
    LAS char* lds = (LAS char*)smem;
    const int tid = threadIdx.x, lane = tid & 63;
    const int w = __builtin_amdgcn_readfirstlane(tid >> 6);
    const int b = blockIdx.x, xcd = b & 7, idx = b >> 3;
    const int split = xcd >> 1, mt = (xcd & 1) * 8 + (idx >> 2), nt = idx & 3;
    const int wm = w >> 1, kh = w & 1;
    const int T0 = split == 0 ? 0 : 21 + 20 * (split - 1);

    const unsigned srcA = (unsigned)(kh * (int)BLK_ELEMS * 2 + blk_off(128 * mt + 32 * wm + (lane & 15), lane >> 4));
    const int koct = w >> 1, sub = w & 1;
    unsigned offB, offE;
    {
        const int r = lane >> 3, swz = ((r >> 1) & 1) | ((koct & 1) << 1), c = (lane & 7) ^ (swz << 1);
        offB = (unsigned)((32 * sub + 8 * koct + r) * DM + 128 * nt + 8 * c) * 2u;
        const int re = ((lane >> 1) & 7) ^ ((koct & 1) << 2), ce = (lane & 1);
        offE = (unsigned)((32 * sub + 8 * koct + re) * DM + 512 + 16 * nt + 8 * ce) * 2u;
    }
    const unsigned lds0 = (unsigned)(size_t)lds;
    const unsigned dB = lds0 + K2_RB + (2 * w) * 1024, dE = lds0 + K2_RE + (sub * 4 + koct) * 256;
    bf16x8 fa[4][2];
    auto issue1 = [&](int t, int sl, int i, bf16x8& a0, bf16x8& a1) {
        int T = T0 + t; if (t == K2_NT - 1 && split != 0) T -= 1;
        const u16* Wt = T < 72 ? Wd + (size_t)T * 64 * DM : wo + (size_t)(T - 72) * 64 * DM;
        const u16* ba = hb + (size_t)T * 2 * BLK_ELEMS;
        if (i == 0) glds16(Wt, offB, dB + sl * 16384);
        if (i == 1) glds16(Wt + 64, offB, dB + sl * 16384 + 1024);
        if (i == 2) glds16_q(Wt, offE, dE + sl * 2048);
        if (i == 3) gload2(a0, a1, ba, srcA);
    };
    STAMP(2, 0);
#pragma unroll
    for (int t = 0; t < 3; ++t)
#pragma unroll
        for (int i = 0; i < 4; ++i) issue1(t, t, i, fa[t][0], fa[t][1]);

    const int fr = lane & 15, g = lane >> 4, q4 = (lane >> 2) & 3, p4 = lane & 3;
    const int swz = ((q4 >> 1) & 1) | ((g & 1) << 1);
    LAS char* pb[4];
#pragma unroll
    for (int u = 0; u < 4; ++u) pb[u] = lds + K2_RB + (g * 4 + kh * 2) * 1024 + q4 * 128 + ((u ^ swz) << 5) + p4 * 8;
    LAS char* pe0 = lds + K2_RE + (kh * 4 + g) * 256 + ((q4 ^ ((g & 1) << 2)) * 32) + p4 * 8;
    LAS char* pe1 = lds + K2_RE + (kh * 4 + g) * 256 + (((4 + q4) ^ ((g & 1) << 2)) * 32) + p4 * 8;
    auto rdB = [&](int sl, int nf, int t1) -> s16x4 {
        if (nf < 8) return tr_read(pb[nf & 3] + sl * 16384 + (nf >> 2) * 1024 + t1 * 512);
        return tr_read((t1 ? pe1 : pe0) + sl * 2048);
    };

    f32x4 acc[9][2];
#pragma unroll
    for (int n = 0; n < 9; ++n)
#pragma unroll
        for (int m = 0; m < 2; ++m) { acc[n][m] = (f32x4){0.f, 0.f, 0.f, 0.f}; asm volatile("" : "+v"(acc[n][m])); }

    wait_vm<10>(); BAR();
    STAMP(2, 1);
    constexpr int PF2 = 4;
    bf16x8 fb[9];
#pragma unroll
    for (int i = 0; i < PF2; ++i) { s16x4 lo = rdB(0, i, 0), hi = rdB(0, i, 1); fb[i] = __builtin_shufflevector(lo, hi, 0, 1, 2, 3, 4, 5, 6, 7); }
    auto step = [&](auto S_, int t, bool do_issue, bool wait0, auto LAST_) {
        constexpr int S = decltype(S_)::value;
        constexpr bool has_next = !decltype(LAST_)::value;
        constexpr int sl = S, sn = (S + 1) & 3;
        asm volatile("" : "+v"(fa[sl][0]), "+v"(fa[sl][1]));
        if (!has_next && split != 0) {
#pragma unroll
            for (int m = 0; m < 2; ++m) fa[sl][m] = (bf16x8){0, 0, 0, 0, 0, 0, 0, 0};
            asm volatile("s_nop 1" : "+v"(fa[sl][0]), "+v"(fa[sl][1]));
        }
#pragma unroll
        for (int nf = 0; nf < 9; ++nf) {
            const int psl = (nf + PF2 < 9) ? sl : sn, nn = (nf + PF2 < 9) ? nf + PF2 : nf + PF2 - 9;
            const bool pf = (nf + PF2 < 9) || has_next;
            s16x4 lo, hi;
            mfma_ip(acc[nf][0], fb[nf], fa[sl][0]);
            SCHED();
            if (nf == 0) {
                if (!wait0) wait_vm<5>(); else wait_vm<0>();
                BAR();
            }
            if (pf) lo = rdB(psl, nn, 0);
            SCHED();
            mfma_ip(acc[nf][1], fb[nf], fa[sl][1]);
            SCHED();
            if (pf) hi = rdB(psl, nn, 1);
            if (nf >= 1 && nf < 5 && do_issue) issue1(t + 3, (S + 3) & 3, nf - 1, fa[(S + 3) & 3][0], fa[(S + 3) & 3][1]);
            SCHED();
            if (pf) fb[nn] = __builtin_shufflevector(lo, hi, 0, 1, 2, 3, 4, 5, 6, 7);
        }
    };
    using I0 = std::integral_constant<int, 0>; using I1 = std::integral_constant<int, 1>;
    using I2 = std::integral_constant<int, 2>; using I3 = std::integral_constant<int, 3>;
#pragma unroll 1
    for (int t0 = 0; t0 < 20; t0 += 4) {
        step(I0{}, t0, true, false, std::false_type{});
        step(I1{}, t0 + 1, true, false, std::false_type{});
        step(I2{}, t0 + 2, t0 + 2 < 18, false, std::false_type{});
        step(I3{}, t0 + 3, t0 + 3 < 18, t0 + 3 == 19, std::false_type{});
    }
    step(I0{}, 20, false, true, std::true_type{});
    asm volatile("s_nop 7\n\ts_nop 7" ::: "memory");
    BAR();
    STAMP(2, 2);
    {
        LAS char* xw = lds + w * 10240;
        LAS char* xr = lds + (w ^ 1) * 10240;
#pragma unroll
        for (int n = 0; n < 9; ++n) {
            const bool give = kh ? (n < 5) : (n >= 5);
            if (give) {
#pragma unroll
                for (int m = 0; m < 2; ++m) *(LAS f32x4*)(xw + ((kh ? n : n - 5) * 2 + m) * 1024 + lane * 16) = acc[n][m];
            }
        }
        asm volatile("s_waitcnt lgkmcnt(0)" ::: "memory");
        BAR();
        u16* pp = part + (size_t)split * SEQ * DM;
#pragma unroll
        for (int n = 0; n < 9; ++n) {
            const bool keep = kh ? (n >= 5) : (n < 5);
            if (keep) {
#pragma unroll
                for (int m = 0; m < 2; ++m) {
                    const f32x4 o = *(LAS f32x4*)(xr + ((kh ? n - 5 : n) * 2 + m) * 1024 + lane * 16) + acc[n][m];
                    const int row = 128 * mt + 32 * wm + 16 * m + fr;
                    const int col = (n < 8 ? 128 * nt + 16 * n : 512 + 16 * nt) + 4 * g;
                    u32x2 v; v[0] = pk_bf16(o[0], o[1]); v[1] = pk_bf16(o[2], o[3]);
                    *(u32x2*)(pp + (size_t)row * DM + col) = v;
                }
            }
        }
    }
    STAMP(2, 15);
}

__global__ __launch_bounds__(256) void k3_reduce(const u16* __restrict__ x, const u16* __restrict__ part, u16* __restrict__ out) {
    const int i = blockIdx.x * 256 + threadIdx.x;
    const size_t e = (size_t)i * 8;
    constexpr size_t PS = (size_t)SEQ * DM;
    const u32x4 xv = *(const u32x4*)(x + e);
    u32x4 pv[NSPLIT];
#pragma unroll
    for (int s = 0; s < NSPLIT; ++s) pv[s] = *(const u32x4*)(part + s * PS + e);
    u32x4 o;
#pragma unroll
    for (int k = 0; k < 4; ++k) {
        float lo = 0.f, hi = 0.f;
#pragma unroll
        for (int s = 0; s < NSPLIT; ++s) { lo += bf_lo(pv[s][k]); hi += bf_hi(pv[s][k]); }
        o[k] = pk_bf16(lo + bf_lo(xv[k]), hi + bf_hi(xv[k]));
    }
    *(u32x4*)(out + e) = o;
}

extern "C" void kernel_launch(void* const* d_in, const int* in_sizes, int n_in, void* d_out, int out_size, void* d_ws,
                              size_t ws_size, hipStream_t stream) {
    const u16* x = (const u16*)d_in[0];
    const u16* wqkv = (const u16*)d_in[1];
    const u16* wo = (const u16*)d_in[2];
    const u16* Wg = (const u16*)d_in[4];
    const u16* Wu = (const u16*)d_in[5];
    const u16* Wd = (const u16*)d_in[6];
    char* ws = (char*)d_ws;
    u16* xnb = (u16*)ws;
    u16* hb = (u16*)(ws + (4u << 20));
    u16* part = (u16*)(ws + (32u << 20));
    u16* wqh = (u16*)(ws + (48u << 20));
    (void)hipFuncSetAttribute((const void*)k1_gateup, hipFuncAttributeMaxDynamicSharedMemorySize, K1_LDS);
    (void)hipFuncSetAttribute((const void*)k2_down, hipFuncAttributeMaxDynamicSharedMemorySize, K2_LDS);
    k0_ln<<<256, 256, 0, stream>>>(x, xnb, wqkv, wqh);
    k1_gateup<<<256, 512, K1_LDS, stream>>>(xnb, Wg, Wu, wqh, Wd, hb);
    k2_down<<<256, 512, K2_LDS, stream>>>(hb, Wd, wo, part);
    k3_reduce<<<SEQ * DM / 8 / 256, 256, 0, stream>>>(x, part, (u16*)d_out);
}
```

```cpp
#include <hip/hip_runtime.h>
#include <hip/hip_bf16.h>
#include <stdint.h>
#include <type_traits>

typedef unsigned short u16;
typedef short bf16x8 __attribute__((ext_vector_type(8)));
typedef short s16x4 __attribute__((ext_vector_type(4)));
typedef float f32x4 __attribute__((ext_vector_type(4)));
typedef float f32x2 __attribute__((ext_vector_type(2)));
typedef __bf16 bf16x2_t __attribute__((ext_vector_type(2)));
typedef unsigned u32x4 __attribute__((ext_vector_type(4)));
typedef unsigned u32x2 __attribute__((ext_vector_type(2)));
#define LAS __attribute__((address_space(3)))
#define GAS __attribute__((address_space(1)))

constexpr int SEQ = 2048, DM = 576, FF = 4608;
constexpr int NKB_X = 18;
constexpr int NKB_H = 162;
constexpr size_t BLK_ELEMS = (size_t)SEQ * 32;

__device__ __forceinline__ unsigned pk_bf16(float lo, float hi) {
    f32x2 v = {lo, hi};
    bf16x2_t b = __builtin_convertvector(v, bf16x2_t);
    return __builtin_bit_cast(unsigned, b);
}
__device__ __forceinline__ float bf_lo(unsigned u) { return __uint_as_float(u << 16); }
__device__ __forceinline__ float bf_hi(unsigned u) { return __uint_as_float(u & 0xffff0000u); }
__device__ __forceinline__ float rbf(float x) { return bf_lo(pk_bf16(x, x)); }

__device__ __forceinline__ const void* uni(const void* p) {
    const unsigned long long v = (unsigned long long)p;
    const unsigned lo = __builtin_amdgcn_readfirstlane((unsigned)v), hi = __builtin_amdgcn_readfirstlane((unsigned)(v >> 32));
    return (const void*)(((unsigned long long)hi << 32) | lo);
}
__device__ __forceinline__ void glds16(const void* sbase, unsigned voff, unsigned lds) {
    sbase = uni(sbase); lds = __builtin_amdgcn_readfirstlane(lds);
    unsigned sv;
    asm volatile("s_mov_b32 %0, m0\n\ts_mov_b32 m0, %3\n\ts_nop 3\n\tglobal_load_lds_dwordx4 %1, %2\n\ts_mov_b32 m0, %0"
                 : "=&s"(sv) : "v"(voff), "s"(sbase), "s"(lds) : "memory");
}
__device__ __forceinline__ void glds16_o1k(const void* sbase, unsigned voff, unsigned lds) {
    sbase = uni(sbase); lds = __builtin_amdgcn_readfirstlane(lds);
    unsigned sv;
    asm volatile("s_mov_b32 %0, m0\n\ts_mov_b32 m0, %3\n\ts_nop 3\n\tglobal_load_lds_dwordx4 %1, %2 offset:1024\n\ts_mov_b32 m0, %0"
                 : "=&s"(sv) : "v"(voff), "s"(sbase), "s"(lds) : "memory");
}
__device__ __forceinline__ void glds16_q(const void* sbase, unsigned voff, unsigned lds) {
    sbase = uni(sbase); lds = __builtin_amdgcn_readfirstlane(lds);
    unsigned sv; unsigned long long ex;
    asm volatile("s_mov_b32 %0, m0\n\ts_mov_b64 %1, exec\n\ts_mov_b32 m0, %4\n\ts_mov_b64 exec, 0xffff\n\ts_nop 1\n\tglobal_load_lds_dwordx4 %2, %3\n\ts_mov_b64 exec, %1\n\ts_mov_b32 m0, %0"
                 : "=&s"(sv), "=&s"(ex) : "v"(voff), "s"(sbase), "s"(lds) : "memory");
}
__device__ __forceinline__ void glds16_m(const void* sbase, unsigned voff, unsigned lds) {
    sbase = uni(sbase); lds = __builtin_amdgcn_readfirstlane(lds);
    unsigned sv; unsigned long long ex;
    asm volatile("s_mov_b32 %0, m0\n\ts_mov_b64 %1, exec\n\ts_mov_b32 m0, %4\n\ts_mov_b32 exec_lo, -1\n\ts_mov_b32 exec_hi, 0xff\n\ts_nop 1\n\tglobal_load_lds_dwordx4 %2, %3\n\ts_mov_b64 exec, %1\n\ts_mov_b32 m0, %0"
                 : "=&s"(sv), "=&s"(ex) : "v"(voff), "s"(sbase), "s"(lds) : "memory");
}
#define SCHED() __builtin_amdgcn_sched_barrier(0)
#define STAMP(kid, i) do {} while (0)
#define WSTAMP(i) do {} while (0)
__device__ __forceinline__ s16x4 tr_read(LAS char* p) {
    return __builtin_bit_cast(s16x4, __builtin_amdgcn_ds_read_tr16_b64_v4i16((LAS s16x4*)p));
}
__device__ __forceinline__ bf16x8 tr_frag(LAS char* p, int off_t1) {
    s16x4 lo = tr_read(p), hi = tr_read(p + off_t1);
    return __builtin_shufflevector(lo, hi, 0, 1, 2, 3, 4, 5, 6, 7);
}
__device__ __forceinline__ bf16x8 row_frag(LAS char* p) { return *(LAS bf16x8*)p; }
#define MFMA16(a, b, c) __builtin_amdgcn_mfma_f32_16x16x32_bf16((a), (b), (c), 0, 0, 0)
__device__ __forceinline__ void mfma_ip(f32x4& c, bf16x8 a, bf16x8 b) { asm volatile("v_mfma_f32_16x16x32_bf16 %0, %1, %2, %0" : "+v"(c) : "v"(a), "v"(b)); }
#define WAIT_V(n) asm volatile("s_waitcnt vmcnt(" #n ")" ::: "memory")
#define BAR() do { asm volatile("" ::: "memory"); __builtin_amdgcn_s_barrier(); asm volatile("" ::: "memory"); } while (0)

__device__ __forceinline__ int blk_off(int row, int chunk) { return row * 64 + ((chunk ^ (((row >> 3) & 1) << 1)) << 4); }

__global__ __launch_bounds__(256) void k0_ln(const u16* __restrict__ x, u16* __restrict__ xnb,
                                            const u16* __restrict__ wqkv, u16* __restrict__ wqh) {
    if (threadIdx.x >= 64) {
        const int l = threadIdx.x - 64, head = blockIdx.x >> 3, k0 = (blockIdx.x & 7) * 72;
        unsigned v[12];
#pragma unroll
        for (int i = 0; i < 12; ++i) {
            const int id = l + 192 * i, k = k0 + (id >> 5), cp = id & 31;
            const int part = cp >= 18 ? 2 : (cp >= 9 ? 1 : 0), d2 = cp - part * 9;
            const int src = (part == 2 ? 0 : part + 1) * DM + 18 * head + 2 * d2;
            v[i] = cp < 27 ? *(const unsigned*)(wqkv + (size_t)k * (3 * DM) + src) : 0u;
        }
#pragma unroll
        for (int i = 0; i < 12; ++i) {
            const int id = l + 192 * i;
            *(unsigned*)(wqh + ((size_t)head * 576 + k0) * 64 + 2 * id) = v[i];
        }
        return;
    }
    const int lane = threadIdx.x, i = lane & 7, row = blockIdx.x * 8 + (lane >> 3);
    u32x4 xv[9];
#pragma unroll
    for (int cc = 0; cc < 9; ++cc) xv[cc] = *(const u32x4*)(x + (size_t)row * DM + (i + 8 * cc) * 8);
    float s = 0.f;
#pragma unroll
    for (int cc = 0; cc < 9; ++cc)
#pragma unroll
        for (int e = 0; e < 4; ++e) s += bf_lo(xv[cc][e]) + bf_hi(xv[cc][e]);
    s += __shfl_xor(s, 1); s += __shfl_xor(s, 2); s += __shfl_xor(s, 4);
    const float mean = s * (1.0f / 576.0f);
    float q = 0.f;
#pragma unroll
    for (int cc = 0; cc < 9; ++cc)
#pragma unroll
        for (int e = 0; e < 4; ++e) { const float a = bf_lo(xv[cc][e]) - mean, c2 = bf_hi(xv[cc][e]) - mean; q += a * a + c2 * c2; }
    q += __shfl_xor(q, 1); q += __shfl_xor(q, 2); q += __shfl_xor(q, 4);
    const float mb = rbf(mean), vb = rbf(q * (1.0f / 576.0f));
    const float sb = rbf(sqrtf(rbf(vb + 1e-5f)));
    const float rs = 1.0f / sb;
#pragma unroll
    for (int cc = 0; cc < 9; ++cc) {
        u32x4 o;
#pragma unroll
        for (int e = 0; e < 4; ++e) {
            const unsigned d = pk_bf16(bf_lo(xv[cc][e]) - mb, bf_hi(xv[cc][e]) - mb);
            o[e] = pk_bf16(bf_lo(d) * rs, bf_hi(d) * rs);
        }
        const int blk = 2 * cc + (i >> 2);
        *(u32x4*)((char*)xnb + (size_t)blk * (BLK_ELEMS * 2) + blk_off(row, i & 3)) = o;
    }
}

constexpr int RA = 0, RB = 65536, RE = 131072, RQ = 139264, RH = 155648;
constexpr int T_DUMMY = 159744;
constexpr int K1_LDS = T_DUMMY + 1024;
constexpr int QS = 144;
#define HST_AUX 16
#define PF 3
constexpr int NB = PF + 1;
template <int N> __device__ __forceinline__ void wait_vm() { asm volatile("s_waitcnt vmcnt(%0)" :: "i"(N) : "memory"); }
#define GLDS_MASKED(NAME, LO, HI) \
__device__ __forceinline__ void NAME(const void* sbase, unsigned voff, unsigned lds) { \
    sbase = uni(sbase); lds = __builtin_amdgcn_readfirstlane(lds); \
    unsigned sv; unsigned long long ex; \
    asm volatile("s_mov_b32 %0, m0\n\ts_mov_b64 %1, exec\n\ts_mov_b32 m0, %4\n\ts_mov_b32 exec_lo, " #LO "\n\ts_mov_b32 exec_hi, " #HI "\n\tglobal_load_lds_dwordx4 %2, %3\n\ts_mov_b64 exec, %1\n\ts_mov_b32 m0, %0" \
                 : "=&s"(sv), "=&s"(ex) : "v"(voff), "s"(sbase), "s"(lds) : "memory"); }
GLDS_MASKED(glds16_32, -1, 0)
GLDS_MASKED(glds16_8, 0xff, 0)

__device__ __forceinline__ void k1_epilogue(f32x4 (&acc)[11][4], f32x4& ach, LAS char* lds, unsigned lds0, int w, int xcd, int idx,
                                            int j, int mt, const u16* __restrict__ Wd, u16* __restrict__ hb) {
    int tid = threadIdx.x; asm volatile("" : "+v"(tid));
    const int lane = tid & 63, fr = lane & 15, g = lane >> 4;
    const int wm = w >> 1, wn = w & 1;
    {
        const int split = xcd >> 1;
        const unsigned base = (unsigned)(split * 1152 * DM * 2 + (idx * 8 + w) * 6144);
#pragma unroll
        for (int i = 0; i < 6; ++i) {
            unsigned o = base + i * 1024; o = o + 1024 <= (unsigned)(FF * DM * 2) ? o : 0u;
            glds16((const char*)Wd + o, (unsigned)lane * 16u, lds0 + T_DUMMY);
        }
    }
    LAS char* qt = lds;
    {
#pragma unroll
        for (int u = 0; u < 2; ++u) {
            const int nn = 16 * (2 * wn + (u ^ (wm & 1))) + 4 * g;
            const int h0 = nn / 18, h1 = (nn + 2) / 18;
            const int o0 = h0 * 48 + (nn - h0 * 18) * 2, o1 = h1 * 48 + (nn + 2 - h1 * 18) * 2;
#pragma unroll
            for (int m = 0; m < 4; ++m) {
                LAS char* rp = qt + (16 + 64 * wm + 16 * m + fr) * QS;
                if (h0 < 3) *(LAS unsigned*)(rp + o0) = pk_bf16(acc[9 + u][m][0], acc[9 + u][m][1]);
                if (h1 < 3) *(LAS unsigned*)(rp + o1) = pk_bf16(acc[9 + u][m][2], acc[9 + u][m][3]);
            }
            if (u == 0 && (w == 0 || w == 2 || w == 1)) {
                LAS char* rp = qt + fr * QS;
                if (h0 < 3) *(LAS unsigned*)(rp + o0) = pk_bf16(ach[0], ach[1]);
                if (h1 < 3) *(LAS unsigned*)(rp + o1) = pk_bf16(ach[2], ach[3]);
            }
        }
    }
    asm volatile("s_waitcnt lgkmcnt(0)" ::: "memory");
    BAR();
    STAMP(1, 3);
    const __amdgpu_buffer_rsrc_t rh = __builtin_amdgcn_make_buffer_rsrc((void*)hb, 0, (int)(NKB_H * BLK_ELEMS * 2), 0x00020000);
    u32x4 hp[4][2]; u32x2 hm[4];
#pragma unroll
    for (int m = 0; m < 4; ++m) {
#pragma unroll
        for (int pp = 0; pp < 2; ++pp) {
            unsigned v[2][2];
#pragma unroll
            for (int hh = 0; hh < 2; ++hh) {
                const int gt = 2 * pp + hh;
                float hv[4];
#pragma unroll
                for (int e = 0; e < 4; ++e) hv[e] = fmaxf(acc[2 * gt][m][e], 0.f) * acc[2 * gt + 1][m][e];
                v[hh][0] = pk_bf16(hv[0], hv[1]); v[hh][1] = pk_bf16(hv[2], hv[3]);
            }
            auto r0 = __builtin_amdgcn_permlane16_swap(v[0][0], v[1][0], false, false);
            auto r1 = __builtin_amdgcn_permlane16_swap(v[0][1], v[1][1], false, false);
            hp[m][pp][0] = r0[0]; hp[m][pp][1] = r1[0]; hp[m][pp][2] = r0[1]; hp[m][pp][3] = r1[1];
        }
        {
            float hv[4];
#pragma unroll
            for (int e = 0; e < 4; ++e) {
                const unsigned mine = __float_as_uint(acc[8][m][e]);
                auto r = __builtin_amdgcn_permlane32_swap(mine, mine, false, false);
                hv[e] = fmaxf(__uint_as_float(r[0]), 0.f) * __uint_as_float(r[1]);
            }
            hm[m][0] = pk_bf16(hv[0], hv[1]); hm[m][1] = pk_bf16(hv[2], hv[3]);
        }
        asm volatile("" : "+v"(hp[m][0]), "+v"(hp[m][1]), "+v"(hm[m]));
    }
    SCHED();
    auto hstore = [&](int m) {
        const int row = 256 * mt + 64 * wm + 16 * m + fr;
#pragma unroll
        for (int pp = 0; pp < 2; ++pp) {
            const int blk = 4 * j + 2 * wn + pp, chunk = ((g & 1) << 1) | (g >> 1);
            __builtin_amdgcn_raw_buffer_store_b128(hp[m][pp], rh, (unsigned)(blk * (int)(BLK_ELEMS * 2) + blk_off(row, chunk)), 0, HST_AUX);
        }
        const int blk = 128 + (j >> 1), chunk = (j & 1) * 2 + wn;
        const unsigned off = g < 2 ? (unsigned)(blk * (int)(BLK_ELEMS * 2) + blk_off(row, chunk) + g * 8) : 0xfffffff0u;
        __builtin_amdgcn_raw_buffer_store_b64(hm[m], rh, off, 0, HST_AUX);
    };
    {
        const int s = tid >> 1, half = tid & 1;
        const int lrow = 16 + s, grow = 256 * mt + s;
        auto ld18 = [&](LAS const char* p, unsigned (&v)[9]) {
            const u32x4 a = *(LAS const u32x4*)p, c = *(LAS const u32x4*)(p + 16);
            v[0] = a[0]; v[1] = a[1]; v[2] = a[2]; v[3] = a[3]; v[4] = c[0]; v[5] = c[1]; v[6] = c[2]; v[7] = c[3];
            v[8] = *(LAS const unsigned*)(p + 32);
        };
        hstore(0);
        SCHED();
        unsigned qv[9];
        ld18(qt + lrow * QS + 96, qv);
        float lg[4];
#pragma unroll
        for (int ww = 0; ww < 4; ++ww) {
            const int wi = 4 * half + ww;
            unsigned kv[9];
            ld18(qt + (lrow - 2 * wi) * QS, kv);
            float a = 0.f;
#pragma unroll
            for (int i = 0; i < 9; ++i) a = __builtin_amdgcn_fdot2_f32_bf16(__builtin_bit_cast(bf16x2_t, qv[i]), __builtin_bit_cast(bf16x2_t, kv[i]), a, false);
            a = rbf(rbf(a) * 0.23570226039551584f);
            lg[ww] = (grow - 2 * wi >= 0) ? a : -1e9f;
        }
        SCHED();
        hstore(1);
        SCHED();
        float mx = fmaxf(fmaxf(lg[0], lg[1]), fmaxf(lg[2], lg[3]));
        mx = fmaxf(mx, __shfl_xor(mx, 1));
        float ex[4], sum = 0.f;
#pragma unroll
        for (int ww = 0; ww < 4; ++ww) { ex[ww] = __expf(lg[ww] - mx); sum += ex[ww]; }
        sum += __shfl_xor(sum, 1);
        const float inv = 1.0f / sum;
        float o[18];
#pragma unroll
        for (int i = 0; i < 18; ++i) o[i] = 0.f;
#pragma unroll
        for (int ww = 0; ww < 4; ++ww) {
            const int wi = 4 * half + ww;
            const float pw = (grow - 2 * wi >= 0) ? rbf(ex[ww] * inv) : 0.f;
            unsigned vv[9];
            ld18(qt + (lrow - 2 * wi) * QS + 48, vv);
#pragma unroll
            for (int i = 0; i < 9; ++i) { o[2 * i] += pw * bf_lo(vv[i]); o[2 * i + 1] += pw * bf_hi(vv[i]); }
        }
        SCHED();
        hstore(2);
        SCHED();
#pragma unroll
        for (int ii = 0; ii < 5; ++ii) {
            const float s0 = half ? o[2 * ii] : o[(10 + 2 * ii) % 18], s1 = half ? o[2 * ii + 1] : o[(11 + 2 * ii) % 18];
            const float r0 = __shfl_xor(s0, 1), r1 = __shfl_xor(s1, 1);
            const float m0 = half ? o[(10 + 2 * ii) % 18] : o[2 * ii], m1 = half ? o[(11 + 2 * ii) % 18] : o[2 * ii + 1];
            const int i = half * 5 + ii;
            if (i < 9) {
                const int col = FF + 18 * j + 2 * i;
                *(unsigned*)((char*)hb + (size_t)(col >> 5) * (BLK_ELEMS * 2) + blk_off(grow, (col >> 3) & 3) + (col & 7) * 2) = pk_bf16(m0 + r0, m1 + r1);
            }
        }
        SCHED();
        hstore(3);
    }
    STAMP(1, 15);
}

__global__ __launch_bounds__(512, 2) void k1_gateup(const u16* __restrict__ xnb, const u16* __restrict__ Wg,
                                                    const u16* __restrict__ Wu, const u16* __restrict__ wqh,
                                                    const u16* __restrict__ Wd, u16* __restrict__ hb, unsigned* __restrict__ cnt) {
    extern __shared__ __attribute__((aligned(16))) char smem[];
    LAS char* lds = (LAS char*)smem;
    const int tid = threadIdx.x, lane = tid & 63;
    if (blockIdx.x < 64 && tid == 0) cnt[blockIdx.x * 64] = 0u;
    const int w = __builtin_amdgcn_readfirstlane(tid >> 6);
    const int b = blockIdx.x, xcd = b & 7, idx = b >> 3;
    const int j = 4 * xcd + (idx & 3), mt = idx >> 2;
    const int wm = w >> 1, wn = w & 1;
    const unsigned lds0 = (unsigned)(size_t)lds;
    const int fr = lane & 15, g = lane >> 4, q4 = (lane >> 2) & 3, p4 = lane & 3;

    f32x4 acc[11][4], ach;
    {
        const unsigned srcA = (unsigned)((256 * mt + 32 * w) * 32 + lane * 8) * 2u;
        const int koct = w >> 1, mat = w & 1;
        const u16* W = mat ? Wu : Wg;
        unsigned srcB, srcE, srcQ, srcH;
        {
            const int r = lane >> 3, swz = ((r >> 1) & 1) | ((koct & 1) << 1), c = (lane & 7) ^ (swz << 1);
            srcB = (unsigned)((8 * koct + r) * FF + 128 * j + 8 * c) * 2u;
            const int re = ((lane >> 1) & 7) ^ ((koct & 1) << 2), ce = (lane & 1) ^ mat;
            srcE = (unsigned)((8 * koct + re) * FF + 4096 + 16 * j + 8 * ce) * 2u;
            const int rq = 4 * mat + ((lane >> 3) & 3), swq = ((rq >> 1) & 1) | ((koct & 1) << 1), cq = (lane & 7) ^ (swq << 1);
            srcQ = (unsigned)((j * 576 + 8 * koct + rq) * 64 + 8 * cq) * 2u;
            int hr = 256 * mt - 16 + 2 * w + ((lane >> 2) & 1); hr = hr < 0 ? 0 : hr;
            srcH = (unsigned)(hr * 64 + (lane & 3) * 16);
        }
        const unsigned dA = lds0 + RA + (2 * w) * 1024, dB = lds0 + RB + (2 * w) * 1024, dE = lds0 + RE + (mat * 4 + koct) * 256;
        const unsigned dQ = lds0 + RQ + koct * 1024 + mat * 512, dH = lds0 + RH + w * 128;
        auto issue1 = [&](int t, int sl, int i) {
            const u16* ba = xnb + (size_t)t * BLK_ELEMS;
            const u16* bw = W + (size_t)t * 32 * FF;
            if (i == 0) glds16(bw, srcB, dB + sl * 16384);
            if (i == 1) glds16(bw + 64, srcB, dB + sl * 16384 + 1024);
            if (i == 2) glds16_q(bw, srcE, dE + sl * 2048);
            if (i == 3) glds16(ba, srcA, dA + sl * 16384);
            if (i == 4) glds16_o1k(ba, srcA, dA + sl * 16384);
            if (i == 5) glds16_32(wqh + (size_t)t * 32 * 64, srcQ, dQ + sl * 4096);
            if (i == 6) glds16_8(ba, srcH, dH + sl * 1024);
        };
        STAMP(1, 0);
#pragma unroll
        for (int t = 0; t < 3; ++t)
#pragma unroll
            for (int i = 0; i < 7; ++i) issue1(t, t, i);

        const unsigned pa = lds0 + RA + blk_off(64 * wm + fr, g);
        const int swz = ((q4 >> 1) & 1) | ((g & 1) << 1);
        const unsigned pb0 = lds0 + RB + (g * 4 + wn) * 1024 + q4 * 128 + (swz << 5) + p4 * 8;
        const int emat = p4 >> 1;
        const unsigned pe0 = lds0 + RE + (emat * 4 + g) * 256 + ((q4 ^ ((g & 1) << 2)) * 32) + ((wn ^ emat) * 16) + (p4 & 1) * 8;
        const unsigned pq0 = lds0 + RQ + g * 1024 + q4 * 128 + (((2 * wn + (wm & 1)) ^ swz) << 5) + p4 * 8;
        const int hdelta = RH - RA - 4096 * wm;
        auto xr = [&](unsigned base, int imm) -> LAS char* {
            if (imm == 0) return (LAS char*)(size_t)base;
            unsigned r; asm volatile("v_xor_b32 %0, %1, %2" : "=v"(r) : "i"(imm), "v"(base)); return (LAS char*)(size_t)r;
        };
        auto rdB = [&](int sl, int nf, int t1) -> s16x4 {
            if (nf < 8) return tr_read(xr(pb0, (nf >> 1) << 5) + sl * 16384 + (nf & 1) * 2048 + t1 * 512);
            if (nf == 8) return tr_read(xr(pe0, t1 * 128) + sl * 2048);
            return tr_read(xr(pq0, (nf - 9) << 5) + sl * 4096 + t1 * 512);
        };
        auto rdA = [&](int sl, int m) -> bf16x8 { return row_frag((LAS char*)(size_t)pa + sl * 16384 + m * 1024); };
        auto rdH = [&](int sl) -> bf16x8 {
            unsigned r; asm volatile("v_add_u32 %0, %1, %2" : "=v"(r) : "s"(hdelta), "v"(pa)); return row_frag((LAS char*)(size_t)r + sl * 1024);
        };
#pragma unroll
        for (int n = 0; n < 11; ++n)
#pragma unroll
            for (int m = 0; m < 4; ++m) { acc[n][m] = (f32x4){0.f, 0.f, 0.f, 0.f}; asm volatile("" : "+v"(acc[n][m])); }
        ach = (f32x4){0.f, 0.f, 0.f, 0.f}; asm volatile("" : "+v"(ach));

        wait_vm<14>(); BAR();
        STAMP(1, 1);
        bf16x8 fa[2][2], fc[2], fh, fb[NB];
#pragma unroll
        for (int m = 0; m < 2; ++m) { fa[0][m] = rdA(0, m); fc[m] = rdA(0, 2 + m); }
#pragma unroll
        for (int i = 0; i < PF; ++i) { s16x4 lo = rdB(0, i, 0), hi = rdB(0, i, 1); fb[i] = __builtin_shufflevector(lo, hi, 0, 1, 2, 3, 4, 5, 6, 7); }
#define MM(C, A, B) mfma_ip(C, A, B)
        auto step = [&](auto S_, int t, bool do_issue, auto LAST1_, auto WAIT0_) {
            constexpr int S = decltype(S_)::value;
            constexpr bool has_next = !decltype(LAST1_)::value;
            constexpr bool wait0 = decltype(WAIT0_)::value;
            constexpr int sl = S, sn = (S + 1) & 3, cur = S & 1;
#pragma unroll
            for (int nf = 0; nf < 11; ++nf) {
                constexpr int dummy = 0; (void)dummy;
                const int r = 11 * S + nf;
                const int psl = (nf + PF < 11) ? sl : sn, nn = (nf + PF < 11) ? nf + PF : nf + PF - 11;
                const bool pf = (nf + PF < 11) || has_next;
                const bool lastg = (nf == 10);
                s16x4 lo, hi;
                if (!lastg) MM(acc[nf][0], fb[r % NB], fa[cur][0]); else MM(acc[nf][2], fb[r % NB], fc[0]);
                SCHED();
                if (nf == 0) {
                    if (!wait0) wait_vm<7>(); else wait_vm<0>();
                    BAR();
                }
                if (pf) lo = rdB(psl, nn, 0);
                SCHED();
                if (!lastg) MM(acc[nf][1], fb[r % NB], fa[cur][1]); else MM(acc[nf][3], fb[r % NB], fc[1]);
                SCHED();
                if (pf) hi = rdB(psl, nn, 1);
                if (lastg && has_next) fc[0] = rdA(sn, 2);
                SCHED();
                if (!lastg) MM(acc[nf][2], fb[r % NB], fc[0]); else MM(acc[nf][0], fb[r % NB], fa[cur][0]);
                SCHED();
                if (lastg && has_next) fc[1] = rdA(sn, 3);
                if (nf >= 4 && nf < 6 && has_next) fa[cur ^ 1][nf - 4] = rdA(sn, nf - 4);
                if (nf == 7) fh = rdH(sl);
                if ((nf < 4 || (nf >= 6 && nf < 9)) && do_issue) issue1(t + 3, (S + 3) & 3, nf < 4 ? nf : nf - 2);
                SCHED();
                if (!lastg) MM(acc[nf][3], fb[r % NB], fc[1]); else MM(acc[nf][1], fb[r % NB], fa[cur][1]);
                SCHED();
                if (nf == 9) { MM(ach, fb[r % NB], fh); SCHED(); }
                if (pf) fb[(r + PF) % NB] = __builtin_shufflevector(lo, hi, 0, 1, 2, 3, 4, 5, 6, 7);
            }
        };
        using I0 = std::integral_constant<int, 0>; using I1 = std::integral_constant<int, 1>;
        using I2 = std::integral_constant<int, 2>; using I3 = std::integral_constant<int, 3>;
        using F = std::false_type; using T = std::true_type;
#pragma unroll 1
        for (int t0 = 0; t0 < 16; t0 += 4) {
            step(I0{}, t0, true, F{}, F{});
            step(I1{}, t0 + 1, true, F{}, F{});
            step(I2{}, t0 + 2, true, F{}, F{});
            step(I3{}, t0 + 3, t0 + 3 < 15, F{}, F{});
        }
        step(I0{}, 16, false, F{}, T{});
        step(I1{}, 17, false, T{}, T{});
    }
    asm volatile("s_nop 7\n\ts_nop 7" ::: "memory");
    BAR();
    STAMP(1, 2);
    k1_epilogue(acc, ach, lds, lds0, w, xcd, idx, j, mt, Wd, hb);
}

constexpr int K2_NT = 21;
constexpr int K2_RB = 0, K2_RE = 65536, K2_LDS = 81920;
constexpr int NSPLIT = 4;
__device__ __forceinline__ void gload2(bf16x8& d0, bf16x8& d1, const void* sbase, unsigned voff) {
    sbase = uni(sbase);
    asm volatile("s_nop 4\n\tglobal_load_dwordx4 %0, %2, %3\n\tglobal_load_dwordx4 %1, %2, %3 offset:1024"
                 : "=&v"(d0), "=&v"(d1) : "v"(voff), "s"(sbase) : "memory");
}
__global__ __launch_bounds__(512, 2) void k2_down(const u16* __restrict__ hb, const u16* __restrict__ Wd,
                                                  const u16* __restrict__ wo, u16* __restrict__ part, unsigned* __restrict__ cnt,
                                                  const u16* __restrict__ x, u16* __restrict__ out) {
    extern __shared__ __attribute__((aligned(16))) char smem[];
    LAS char* lds = (LAS char*)smem;
    const int tid = threadIdx.x, lane = tid & 63;
    const int w = __builtin_amdgcn_readfirstlane(tid >> 6);
    const int b = blockIdx.x, xcd = b & 7, idx = b >> 3;
    const int split = xcd >> 1, mt = (xcd & 1) * 8 + (idx >> 2), nt = idx & 3;
    const int wm = w >> 1, kh = w & 1;
    const int T0 = split == 0 ? 0 : 21 + 20 * (split - 1);

    const unsigned srcA = (unsigned)(kh * (int)BLK_ELEMS * 2 + blk_off(128 * mt + 32 * wm + (lane & 15), lane >> 4));
    const int koct = w >> 1, sub = w & 1;
    unsigned offB, offE;
    {
        const int r = lane >> 3, swz = ((r >> 1) & 1) | ((koct & 1) << 1), c = (lane & 7) ^ (swz << 1);
        offB = (unsigned)((32 * sub + 8 * koct + r) * DM + 128 * nt + 8 * c) * 2u;
        const int re = ((lane >> 1) & 7) ^ ((koct & 1) << 2), ce = (lane & 1);
        offE = (unsigned)((32 * sub + 8 * koct + re) * DM + 512 + 16 * nt + 8 * ce) * 2u;
    }
    const unsigned lds0 = (unsigned)(size_t)lds;
    const unsigned dB = lds0 + K2_RB + (2 * w) * 1024, dE = lds0 + K2_RE + (sub * 4 + koct) * 256;
    bf16x8 fa[4][2];
    auto issue1 = [&](int t, int sl, int i, bf16x8& a0, bf16x8& a1) {
        int T = T0 + t; if (t == K2_NT - 1 && split != 0) T -= 1;
        const u16* Wt = T < 72 ? Wd + (size_t)T * 64 * DM : wo + (size_t)(T - 72) * 64 * DM;
        const u16* ba = hb + (size_t)T * 2 * BLK_ELEMS;
        if (i == 0) glds16(Wt, offB, dB + sl * 16384);
        if (i == 1) glds16(Wt + 64, offB, dB + sl * 16384 + 1024);
        if (i == 2) glds16_q(Wt, offE, dE + sl * 2048);
        if (i == 3) gload2(a0, a1, ba, srcA);
    };
    STAMP(2, 0);
#pragma unroll
    for (int t = 0; t < 3; ++t)
#pragma unroll
        for (int i = 0; i < 4; ++i) issue1(t, t, i, fa[t][0], fa[t][1]);

    const int fr = lane & 15, g = lane >> 4, q4 = (lane >> 2) & 3, p4 = lane & 3;
    const int swz = ((q4 >> 1) & 1) | ((g & 1) << 1);
    LAS char* pb[4];
#pragma unroll
    for (int u = 0; u < 4; ++u) pb[u] = lds + K2_RB + (g * 4 + kh * 2) * 1024 + q4 * 128 + ((u ^ swz) << 5) + p4 * 8;
    LAS char* pe0 = lds + K2_RE + (kh * 4 + g) * 256 + ((q4 ^ ((g & 1) << 2)) * 32) + p4 * 8;
    LAS char* pe1 = lds + K2_RE + (kh * 4 + g) * 256 + (((4 + q4) ^ ((g & 1) << 2)) * 32) + p4 * 8;
    auto rdB = [&](int sl, int nf, int t1) -> s16x4 {
        if (nf < 8) return tr_read(pb[nf & 3] + sl * 16384 + (nf >> 2) * 1024 + t1 * 512);
        return tr_read((t1 ? pe1 : pe0) + sl * 2048);
    };

    f32x4 acc[9][2];
#pragma unroll
    for (int n = 0; n < 9; ++n)
#pragma unroll
        for (int m = 0; m < 2; ++m) { acc[n][m] = (f32x4){0.f, 0.f, 0.f, 0.f}; asm volatile("" : "+v"(acc[n][m])); }

    wait_vm<10>(); BAR();
    STAMP(2, 1);
    constexpr int PF2 = 4;
    bf16x8 fb[9];
#pragma unroll
    for (int i = 0; i < PF2; ++i) { s16x4 lo = rdB(0, i, 0), hi = rdB(0, i, 1); fb[i] = __builtin_shufflevector(lo, hi, 0, 1, 2, 3, 4, 5, 6, 7); }
    auto step = [&](auto S_, int t, bool do_issue, bool wait0, auto LAST_) {
        constexpr int S = decltype(S_)::value;
        constexpr bool has_next = !decltype(LAST_)::value;
        constexpr int sl = S, sn = (S + 1) & 3;
        asm volatile("" : "+v"(fa[sl][0]), "+v"(fa[sl][1]));
        if (!has_next && split != 0) {
#pragma unroll
            for (int m = 0; m < 2; ++m) fa[sl][m] = (bf16x8){0, 0, 0, 0, 0, 0, 0, 0};
            asm volatile("s_nop 1" : "+v"(fa[sl][0]), "+v"(fa[sl][1]));
        }
#pragma unroll
        for (int nf = 0; nf < 9; ++nf) {
            const int psl = (nf + PF2 < 9) ? sl : sn, nn = (nf + PF2 < 9) ? nf + PF2 : nf + PF2 - 9;
            const bool pf = (nf + PF2 < 9) || has_next;
            s16x4 lo, hi;
            mfma_ip(acc[nf][0], fb[nf], fa[sl][0]);
            SCHED();
            if (nf == 0) {
                if (!wait0) wait_vm<5>(); else wait_vm<0>();
                BAR();
            }
            if (pf) lo = rdB(psl, nn, 0);
            SCHED();
            mfma_ip(acc[nf][1], fb[nf], fa[sl][1]);
            SCHED();
            if (pf) hi = rdB(psl, nn, 1);
            if (nf >= 1 && nf < 5 && do_issue) issue1(t + 3, (S + 3) & 3, nf - 1, fa[(S + 3) & 3][0], fa[(S + 3) & 3][1]);
            SCHED();
            if (pf) fb[nn] = __builtin_shufflevector(lo, hi, 0, 1, 2, 3, 4, 5, 6, 7);
        }
    };
    using I0 = std::integral_constant<int, 0>; using I1 = std::integral_constant<int, 1>;
    using I2 = std::integral_constant<int, 2>; using I3 = std::integral_constant<int, 3>;
#pragma unroll 1
    for (int t0 = 0; t0 < 20; t0 += 4) {
        step(I0{}, t0, true, false, std::false_type{});
        step(I1{}, t0 + 1, true, false, std::false_type{});
        step(I2{}, t0 + 2, t0 + 2 < 18, false, std::false_type{});
        step(I3{}, t0 + 3, t0 + 3 < 18, t0 + 3 == 19, std::false_type{});
    }
    step(I0{}, 20, false, true, std::true_type{});
    asm volatile("s_nop 7\n\ts_nop 7" ::: "memory");
    BAR();
    STAMP(2, 2);
    f32x4 val[10];
    {
        LAS char* xw = lds + w * 10240;
        LAS char* xr = lds + (w ^ 1) * 10240;
#pragma unroll
        for (int n = 0; n < 9; ++n) {
            const bool give = kh ? (n < 5) : (n >= 5);
            if (give) {
#pragma unroll
                for (int m = 0; m < 2; ++m) *(LAS f32x4*)(xw + ((kh ? n : n - 5) * 2 + m) * 1024 + lane * 16) = acc[n][m];
            }
        }
        asm volatile("s_waitcnt lgkmcnt(0)" ::: "memory");
        BAR();
#pragma unroll
        for (int i = 0; i < 10; ++i) {
            const f32x4 mine = kh ? acc[(5 + (i >> 1)) < 9 ? 5 + (i >> 1) : 8][i & 1] : acc[i >> 1][i & 1];
            val[i] = mine + *(LAS f32x4*)(xr + i * 1024 + lane * 16);
        }
    }
    {
        const int nit = kh ? 8 : 10;
        const int grp = mt * 4 + nt;
        const __amdgpu_buffer_rsrc_t rp = __builtin_amdgcn_make_buffer_rsrc((void*)part, 0, NSPLIT * 64 * 8 * 10 * 512, 0x00020000);
        const unsigned slot = (unsigned)((grp * 8 + w) * 10 * 512 + lane * 8);
        constexpr unsigned SPL = 64 * 8 * 10 * 512;
#pragma unroll
        for (int i = 0; i < 10; ++i) {
            if (i < nit && (i & 3) != split) {
                u32x2 v; v[0] = pk_bf16(val[i][0], val[i][1]); v[1] = pk_bf16(val[i][2], val[i][3]);
                __builtin_amdgcn_raw_buffer_store_b64(v, rp, slot + split * SPL + i * 512, 0, 16);
            }
        }
        asm volatile("s_waitcnt vmcnt(0)" ::: "memory");
        BAR();
        unsigned* c = cnt + grp * 64;
        if (tid == 0) {
            __hip_atomic_fetch_add(c, 1u, __ATOMIC_RELAXED, __HIP_MEMORY_SCOPE_AGENT);
            unsigned spins = 0;
            while (__hip_atomic_load(c, __ATOMIC_RELAXED, __HIP_MEMORY_SCOPE_AGENT) < (unsigned)NSPLIT) {
                __builtin_amdgcn_s_sleep(1);
                if (++spins > (1u << 22)) break;
            }
        }
        BAR();
#pragma unroll
        for (int i = 0; i < 10; ++i) {
            if (i < nit && (i & 3) == split) {
                const int n = kh ? 5 + (i >> 1) : (i >> 1), m = i & 1;
                const int row = 128 * mt + 32 * wm + 16 * m + fr;
                const int col = (n < 8 ? 128 * nt + 16 * n : 512 + 16 * nt) + 4 * g;
                const u32x2 xv = *(const u32x2*)(x + (size_t)row * DM + col);
                f32x4 o = val[i];
#pragma unroll
                for (int s2 = 1; s2 < NSPLIT; ++s2) {
                    const int so = (split + s2) & 3;
                    const u32x2 pv = __builtin_amdgcn_raw_buffer_load_b64(rp, slot + so * SPL + i * 512, 0, 16);
                    o[0] += bf_lo(pv[0]); o[1] += bf_hi(pv[0]); o[2] += bf_lo(pv[1]); o[3] += bf_hi(pv[1]);
                }
                u32x2 v; v[0] = pk_bf16(o[0] + bf_lo(xv[0]), o[1] + bf_hi(xv[0])); v[1] = pk_bf16(o[2] + bf_lo(xv[1]), o[3] + bf_hi(xv[1]));
                *(u32x2*)(out + (size_t)row * DM + col) = v;
            }
        }
    }
    STAMP(2, 15);
}

extern "C" void kernel_launch(void* const* d_in, const int* in_sizes, int n_in, void* d_out, int out_size, void* d_ws,
                              size_t ws_size, hipStream_t stream) {
    const u16* x = (const u16*)d_in[0];
    const u16* wqkv = (const u16*)d_in[1];
    const u16* wo = (const u16*)d_in[2];
    const u16* Wg = (const u16*)d_in[4];
    const u16* Wu = (const u16*)d_in[5];
    const u16* Wd = (const u16*)d_in[6];
    char* ws = (char*)d_ws;
    u16* xnb = (u16*)ws;
    u16* hb = (u16*)(ws + (4u << 20));
    u16* part = (u16*)(ws + (32u << 20));
    unsigned* cnt = (unsigned*)(ws + (56u << 20));
    u16* wqh = (u16*)(ws + (48u << 20));
    (void)hipFuncSetAttribute((const void*)k1_gateup, hipFuncAttributeMaxDynamicSharedMemorySize, K1_LDS);
    (void)hipFuncSetAttribute((const void*)k2_down, hipFuncAttributeMaxDynamicSharedMemorySize, K2_LDS);
    k0_ln<<<256, 256, 0, stream>>>(x, xnb, wqkv, wqh);
    k1_gateup<<<256, 512, K1_LDS, stream>>>(xnb, Wg, Wu, wqh, Wd, hb, cnt);
    k2_down<<<256, 512, K2_LDS, stream>>>(hb, Wd, wo, part, cnt, x, (u16*)d_out);
}
```

```cpp
#include <hip/hip_runtime.h>
#include <hip/hip_bf16.h>
#include <stdint.h>
#include <type_traits>

typedef unsigned short u16;
typedef short bf16x8 __attribute__((ext_vector_type(8)));
typedef short s16x4 __attribute__((ext_vector_type(4)));
typedef float f32x4 __attribute__((ext_vector_type(4)));
typedef float f32x2 __attribute__((ext_vector_type(2)));
typedef __bf16 bf16x2_t __attribute__((ext_vector_type(2)));
typedef unsigned u32x4 __attribute__((ext_vector_type(4)));
typedef unsigned u32x2 __attribute__((ext_vector_type(2)));
#define LAS __attribute__((address_space(3)))
#define GAS __attribute__((address_space(1)))

constexpr int SEQ = 2048, DM = 576, FF = 4608;
constexpr int NKB_X = 18;
constexpr int NKB_H = 162;
constexpr size_t BLK_ELEMS = (size_t)SEQ * 32;

__device__ __forceinline__ unsigned pk_bf16(float lo, float hi) {
    f32x2 v = {lo, hi};
    bf16x2_t b = __builtin_convertvector(v, bf16x2_t);
    return __builtin_bit_cast(unsigned, b);
}
__device__ __forceinline__ float bf_lo(unsigned u) { return __uint_as_float(u << 16); }
__device__ __forceinline__ float bf_hi(unsigned u) { return __uint_as_float(u & 0xffff0000u); }
__device__ __forceinline__ float rbf(float x) { return bf_lo(pk_bf16(x, x)); }

__device__ __forceinline__ const void* uni(const void* p) {
    const unsigned long long v = (unsigned long long)p;
    const unsigned lo = __builtin_amdgcn_readfirstlane((unsigned)v), hi = __builtin_amdgcn_readfirstlane((unsigned)(v >> 32));
    return (const void*)(((unsigned long long)hi << 32) | lo);
}
__device__ __forceinline__ void glds16(const void* sbase, unsigned voff, unsigned lds) {
    sbase = uni(sbase); lds = __builtin_amdgcn_readfirstlane(lds);
    unsigned sv;
    asm volatile("s_mov_b32 %0, m0\n\ts_mov_b32 m0, %3\n\ts_nop 3\n\tglobal_load_lds_dwordx4 %1, %2\n\ts_mov_b32 m0, %0"
                 : "=&s"(sv) : "v"(voff), "s"(sbase), "s"(lds) : "memory");
}
__device__ __forceinline__ void glds16_o1k(const void* sbase, unsigned voff, unsigned lds) {
    sbase = uni(sbase); lds = __builtin_amdgcn_readfirstlane(lds);
    unsigned sv;
    asm volatile("s_mov_b32 %0, m0\n\ts_mov_b32 m0, %3\n\ts_nop 3\n\tglobal_load_lds_dwordx4 %1, %2 offset:1024\n\ts_mov_b32 m0, %0"
                 : "=&s"(sv) : "v"(voff), "s"(sbase), "s"(lds) : "memory");
}
__device__ __forceinline__ void glds16_q(const void* sbase, unsigned voff, unsigned lds) {
    sbase = uni(sbase); lds = __builtin_amdgcn_readfirstlane(lds);
    unsigned sv; unsigned long long ex;
    asm volatile("s_mov_b32 %0, m0\n\ts_mov_b64 %1, exec\n\ts_mov_b32 m0, %4\n\ts_mov_b64 exec, 0xffff\n\ts_nop 1\n\tglobal_load_lds_dwordx4 %2, %3\n\ts_mov_b64 exec, %1\n\ts_mov_b32 m0, %0"
                 : "=&s"(sv), "=&s"(ex) : "v"(voff), "s"(sbase), "s"(lds) : "memory");
}
__device__ __forceinline__ void glds16_m(const void* sbase, unsigned voff, unsigned lds) {
    sbase = uni(sbase); lds = __builtin_amdgcn_readfirstlane(lds);
    unsigned sv; unsigned long long ex;
    asm volatile("s_mov_b32 %0, m0\n\ts_mov_b64 %1, exec\n\ts_mov_b32 m0, %4\n\ts_mov_b32 exec_lo, -1\n\ts_mov_b32 exec_hi, 0xff\n\ts_nop 1\n\tglobal_load_lds_dwordx4 %2, %3\n\ts_mov_b64 exec, %1\n\ts_mov_b32 m0, %0"
                 : "=&s"(sv), "=&s"(ex) : "v"(voff), "s"(sbase), "s"(lds) : "memory");
}
#define SCHED() __builtin_amdgcn_sched_barrier(0)
#define STAMP(kid, i) do {} while (0)
#define WSTAMP(i) do {} while (0)
__device__ __forceinline__ s16x4 tr_read(LAS char* p) {
    return __builtin_bit_cast(s16x4, __builtin_amdgcn_ds_read_tr16_b64_v4i16((LAS s16x4*)p));
}
__device__ __forceinline__ bf16x8 tr_frag(LAS char* p, int off_t1) {
    s16x4 lo = tr_read(p), hi = tr_read(p + off_t1);
    return __builtin_shufflevector(lo, hi, 0, 1, 2, 3, 4, 5, 6, 7);
}
__device__ __forceinline__ bf16x8 row_frag(LAS char* p) { return *(LAS bf16x8*)p; }
#define MFMA16(a, b, c) __builtin_amdgcn_mfma_f32_16x16x32_bf16((a), (b), (c), 0, 0, 0)
__device__ __forceinline__ void mfma_ip(f32x4& c, bf16x8 a, bf16x8 b) { asm volatile("v_mfma_f32_16x16x32_bf16 %0, %1, %2, %0" : "+v"(c) : "v"(a), "v"(b)); }
#define WAIT_V(n) asm volatile("s_waitcnt vmcnt(" #n ")" ::: "memory")
#define BAR() do { asm volatile("" ::: "memory"); __builtin_amdgcn_s_barrier(); asm volatile("" ::: "memory"); } while (0)

__device__ __forceinline__ int blk_off(int row, int chunk) { return row * 64 + ((chunk ^ (((row >> 3) & 1) << 1)) << 4); }

__global__ __launch_bounds__(256) void k0_ln(const u16* __restrict__ x, u16* __restrict__ xnb,
                                            const u16* __restrict__ wqkv, u16* __restrict__ wqh) {
    if (threadIdx.x >= 64) {
        const int l = threadIdx.x - 64, head = blockIdx.x >> 3, k0 = (blockIdx.x & 7) * 72;
        unsigned v[12];
#pragma unroll
        for (int i = 0; i < 12; ++i) {
            const int id = l + 192 * i, k = k0 + (id >> 5), cp = id & 31;
            const int part = cp >= 18 ? 2 : (cp >= 9 ? 1 : 0), d2 = cp - part * 9;
            const int src = (part == 2 ? 0 : part + 1) * DM + 18 * head + 2 * d2;
            v[i] = cp < 27 ? *(const unsigned*)(wqkv + (size_t)k * (3 * DM) + src) : 0u;
        }
#pragma unroll
        for (int i = 0; i < 12; ++i) {
            const int id = l + 192 * i;
            *(unsigned*)(wqh + ((size_t)head * 576 + k0) * 64 + 2 * id) = v[i];
        }
        return;
    }
    const int lane = threadIdx.x, i = lane & 7, row = blockIdx.x * 8 + (lane >> 3);
    u32x4 xv[9];
#pragma unroll
    for (int cc = 0; cc < 9; ++cc) xv[cc] = *(const u32x4*)(x + (size_t)row * DM + (i + 8 * cc) * 8);
    float s = 0.f;
#pragma unroll
    for (int cc = 0; cc < 9; ++cc)
#pragma unroll
        for (int e = 0; e < 4; ++e) s += bf_lo(xv[cc][e]) + bf_hi(xv[cc][e]);
    s += __shfl_xor(s, 1); s += __shfl_xor(s, 2); s += __shfl_xor(s, 4);
    const float mean = s * (1.0f / 576.0f);
    float q = 0.f;
#pragma unroll
    for (int cc = 0; cc < 9; ++cc)
#pragma unroll
        for (int e = 0; e < 4; ++e) { const float a = bf_lo(xv[cc][e]) - mean, c2 = bf_hi(xv[cc][e]) - mean; q += a * a + c2 * c2; }
    q += __shfl_xor(q, 1); q += __shfl_xor(q, 2); q += __shfl_xor(q, 4);
    const float mb = rbf(mean), vb = rbf(q * (1.0f / 576.0f));
    const float sb = rbf(sqrtf(rbf(vb + 1e-5f)));
    const float rs = 1.0f / sb;
#pragma unroll
    for (int cc = 0; cc < 9; ++cc) {
        u32x4 o;
#pragma unroll
        for (int e = 0; e < 4; ++e) {
            const unsigned d = pk_bf16(bf_lo(xv[cc][e]) - mb, bf_hi(xv[cc][e]) - mb);
            o[e] = pk_bf16(bf_lo(d) * rs, bf_hi(d) * rs);
        }
        const int blk = 2 * cc + (i >> 2);
        *(u32x4*)((char*)xnb + (size_t)blk * (BLK_ELEMS * 2) + blk_off(row, i & 3)) = o;
    }
}

constexpr int RA = 0, RB = 65536, RE = 131072, RQ = 139264, RH = 155648;
constexpr int T_DUMMY = 159744;
constexpr int K1_LDS = T_DUMMY + 1024;
constexpr int QS = 144;
#define HST_AUX 16
#define PF 3
constexpr int NB = PF + 1;
template <int N> __device__ __forceinline__ void wait_vm() { asm volatile("s_waitcnt vmcnt(%0)" :: "i"(N) : "memory"); }
#define GLDS_MASKED(NAME, LO, HI) \
__device__ __forceinline__ void NAME(const void* sbase, unsigned voff, unsigned lds) { \
    sbase = uni(sbase); lds = __builtin_amdgcn_readfirstlane(lds); \
    unsigned sv; unsigned long long ex; \
    asm volatile("s_mov_b32 %0, m0\n\ts_mov_b64 %1, exec\n\ts_mov_b32 m0, %4\n\ts_mov_b32 exec_lo, " #LO "\n\ts_mov_b32 exec_hi, " #HI "\n\tglobal_load_lds_dwordx4 %2, %3\n\ts_mov_b64 exec, %1\n\ts_mov_b32 m0, %0" \
                 : "=&s"(sv), "=&s"(ex) : "v"(voff), "s"(sbase), "s"(lds) : "memory"); }
GLDS_MASKED(glds16_32, -1, 0)
GLDS_MASKED(glds16_8, 0xff, 0)

__device__ __forceinline__ void k1_epilogue(f32x4 (&acc)[11][4], f32x4& ach, LAS char* lds, unsigned lds0, int w, int xcd, int idx,
                                            int j, int mt, const u16* __restrict__ Wd, const u16* __restrict__ wo, u16* __restrict__ hb) {
    int tid = threadIdx.x; asm volatile("" : "+v"(tid));
    const int lane = tid & 63, fr = lane & 15, g = lane >> 4;
    const int wm = w >> 1, wn = w & 1;
    {
        const int split = xcd >> 1;
        const unsigned start = (unsigned)((split == 0 ? 0 : 1344 + 1280 * (split - 1)) * DM * 2);
        const unsigned len = (unsigned)((split == 0 ? 1344 : 1280) * DM * 2), lwd = (unsigned)(704 * DM * 2);
#pragma unroll
        for (int i = 0; i < 6; ++i) {
            unsigned o = (unsigned)((idx * 8 + w) * 6144 + i * 1024); o = o + 1024 <= len ? o : 0u;
            const char* src = (split == 3 && o >= lwd) ? (const char*)wo + (o - lwd) : (const char*)Wd + start + o;
            glds16(src, (unsigned)lane * 16u, lds0 + T_DUMMY);
        }
    }
    LAS char* qt = lds;
    {
#pragma unroll
        for (int u = 0; u < 2; ++u) {
            const int nn = 16 * (2 * wn + (u ^ (wm & 1))) + 4 * g;
            const int h0 = nn / 18, h1 = (nn + 2) / 18;
            const int o0 = h0 * 48 + (nn - h0 * 18) * 2, o1 = h1 * 48 + (nn + 2 - h1 * 18) * 2;
#pragma unroll
            for (int m = 0; m < 4; ++m) {
                LAS char* rp = qt + (16 + 64 * wm + 16 * m + fr) * QS;
                if (h0 < 3) *(LAS unsigned*)(rp + o0) = pk_bf16(acc[9 + u][m][0], acc[9 + u][m][1]);
                if (h1 < 3) *(LAS unsigned*)(rp + o1) = pk_bf16(acc[9 + u][m][2], acc[9 + u][m][3]);
            }
            if (u == 0 && (w == 0 || w == 2 || w == 1)) {
                LAS char* rp = qt + fr * QS;
                if (h0 < 3) *(LAS unsigned*)(rp + o0) = pk_bf16(ach[0], ach[1]);
                if (h1 < 3) *(LAS unsigned*)(rp + o1) = pk_bf16(ach[2], ach[3]);
            }
        }
    }
    asm volatile("s_waitcnt lgkmcnt(0)" ::: "memory");
    BAR();
    STAMP(1, 3);
    const __amdgpu_buffer_rsrc_t rh = __builtin_amdgcn_make_buffer_rsrc((void*)hb, 0, (int)(NKB_H * BLK_ELEMS * 2), 0x00020000);
    u32x4 hp[4][2]; u32x2 hm[4];
#pragma unroll
    for (int m = 0; m < 4; ++m) {
#pragma unroll
        for (int pp = 0; pp < 2; ++pp) {
            unsigned v[2][2];
#pragma unroll
            for (int hh = 0; hh < 2; ++hh) {
                const int gt = 2 * pp + hh;
                float hv[4];
#pragma unroll
                for (int e = 0; e < 4; ++e) hv[e] = fmaxf(acc[2 * gt][m][e], 0.f) * acc[2 * gt + 1][m][e];
                v[hh][0] = pk_bf16(hv[0], hv[1]); v[hh][1] = pk_bf16(hv[2], hv[3]);
            }
            auto r0 = __builtin_amdgcn_permlane16_swap(v[0][0], v[1][0], false, false);
            auto r1 = __builtin_amdgcn_permlane16_swap(v[0][1], v[1][1], false, false);
            hp[m][pp][0] = r0[0]; hp[m][pp][1] = r1[0]; hp[m][pp][2] = r0[1]; hp[m][pp][3] = r1[1];
        }
        {
            float hv[4];
#pragma unroll
            for (int e = 0; e < 4; ++e) {
                const unsigned mine = __float_as_uint(acc[8][m][e]);
                auto r = __builtin_amdgcn_permlane32_swap(mine, mine, false, false);
                hv[e] = fmaxf(__uint_as_float(r[0]), 0.f) * __uint_as_float(r[1]);
            }
            hm[m][0] = pk_bf16(hv[0], hv[1]); hm[m][1] = pk_bf16(hv[2], hv[3]);
        }
        asm volatile("" : "+v"(hp[m][0]), "+v"(hp[m][1]), "+v"(hm[m]));
    }
    SCHED();
    auto hstore = [&](int m) {
        const int row = 256 * mt + 64 * wm + 16 * m + fr;
#pragma unroll
        for (int pp = 0; pp < 2; ++pp) {
            const int blk = 4 * j + 2 * wn + pp, chunk = ((g & 1) << 1) | (g >> 1);
            __builtin_amdgcn_raw_buffer_store_b128(hp[m][pp], rh, (unsigned)(blk * (int)(BLK_ELEMS * 2) + blk_off(row, chunk)), 0, HST_AUX);
        }
        const int blk = 128 + (j >> 1), chunk = (j & 1) * 2 + wn;
        const unsigned off = g < 2 ? (unsigned)(blk * (int)(BLK_ELEMS * 2) + blk_off(row, chunk) + g * 8) : 0xfffffff0u;
        __builtin_amdgcn_raw_buffer_store_b64(hm[m], rh, off, 0, HST_AUX);
    };
    {
        const int s = tid >> 1, half = tid & 1;
        const int lrow = 16 + s, grow = 256 * mt + s;
        auto ld18 = [&](LAS const char* p, unsigned (&v)[9]) {
            const u32x4 a = *(LAS const u32x4*)p, c = *(LAS const u32x4*)(p + 16);
            v[0] = a[0]; v[1] = a[1]; v[2] = a[2]; v[3] = a[3]; v[4] = c[0]; v[5] = c[1]; v[6] = c[2]; v[7] = c[3];
            v[8] = *(LAS const unsigned*)(p + 32);
        };
        hstore(0);
        SCHED();
        unsigned qv[9];
        ld18(qt + lrow * QS + 96, qv);
        float lg[4];
#pragma unroll
        for (int ww = 0; ww < 4; ++ww) {
            const int wi = 4 * half + ww;
            unsigned kv[9];
            ld18(qt + (lrow - 2 * wi) * QS, kv);
            float a = 0.f;
#pragma unroll
            for (int i = 0; i < 9; ++i) a = __builtin_amdgcn_fdot2_f32_bf16(__builtin_bit_cast(bf16x2_t, qv[i]), __builtin_bit_cast(bf16x2_t, kv[i]), a, false);
            a = rbf(rbf(a) * 0.23570226039551584f);
            lg[ww] = (grow - 2 * wi >= 0) ? a : -1e9f;
        }
        SCHED();
        hstore(1);
        SCHED();
        float mx = fmaxf(fmaxf(lg[0], lg[1]), fmaxf(lg[2], lg[3]));
        mx = fmaxf(mx, __shfl_xor(mx, 1));
        float ex[4], sum = 0.f;
#pragma unroll
        for (int ww = 0; ww < 4; ++ww) { ex[ww] = __expf(lg[ww] - mx); sum += ex[ww]; }
        sum += __shfl_xor(sum, 1);
        const float inv = 1.0f / sum;
        float o[18];
#pragma unroll
        for (int i = 0; i < 18; ++i) o[i] = 0.f;
#pragma unroll
        for (int ww = 0; ww < 4; ++ww) {
            const int wi = 4 * half + ww;
            const float pw = (grow - 2 * wi >= 0) ? rbf(ex[ww] * inv) : 0.f;
            unsigned vv[9];
            ld18(qt + (lrow - 2 * wi) * QS + 48, vv);
#pragma unroll
            for (int i = 0; i < 9; ++i) { o[2 * i] += pw * bf_lo(vv[i]); o[2 * i + 1] += pw * bf_hi(vv[i]); }
        }
        SCHED();
        hstore(2);
        SCHED();
#pragma unroll
        for (int ii = 0; ii < 5; ++ii) {
            const float s0 = half ? o[2 * ii] : o[(10 + 2 * ii) % 18], s1 = half ? o[2 * ii + 1] : o[(11 + 2 * ii) % 18];
            const float r0 = __shfl_xor(s0, 1), r1 = __shfl_xor(s1, 1);
            const float m0 = half ? o[(10 + 2 * ii) % 18] : o[2 * ii], m1 = half ? o[(11 + 2 * ii) % 18] : o[2 * ii + 1];
            const int i = half * 5 + ii;
            if (i < 9) {
                const int col = FF + 18 * j + 2 * i;
                *(unsigned*)((char*)hb + (size_t)(col >> 5) * (BLK_ELEMS * 2) + blk_off(grow, (col >> 3) & 3) + (col & 7) * 2) = pk_bf16(m0 + r0, m1 + r1);
            }
        }
        SCHED();
        hstore(3);
    }
    STAMP(1, 15);
}

__global__ __launch_bounds__(512, 2) void k1_gateup(const u16* __restrict__ xnb, const u16* __restrict__ Wg,
                                                    const u16* __restrict__ Wu, const u16* __restrict__ wqh,
                                                    const u16* __restrict__ Wd, const u16* __restrict__ wo, u16* __restrict__ hb, unsigned* __restrict__ cnt) {
    extern __shared__ __attribute__((aligned(16))) char smem[];
    LAS char* lds = (LAS char*)smem;
    const int tid = threadIdx.x, lane = tid & 63;
    if (blockIdx.x < 64 && tid == 0) cnt[blockIdx.x * 64] = 0u;
    const int w = __builtin_amdgcn_readfirstlane(tid >> 6);
    const int b = blockIdx.x, xcd = b & 7, idx = b >> 3;
    const int j = 4 * xcd + (idx & 3), mt = idx >> 2;
    const int wm = w >> 1, wn = w & 1;
    const unsigned lds0 = (unsigned)(size_t)lds;
    const int fr = lane & 15, g = lane >> 4, q4 = (lane >> 2) & 3, p4 = lane & 3;

    f32x4 acc[11][4], ach;
    {
        const unsigned srcA = (unsigned)((256 * mt + 32 * w) * 32 + lane * 8) * 2u;
        const int koct = w >> 1, mat = w & 1;
        const u16* W = mat ? Wu : Wg;
        unsigned srcB, srcE, srcQ, srcH;
        {
            const int r = lane >> 3, swz = ((r >> 1) & 1) | ((koct & 1) << 1), c = (lane & 7) ^ (swz << 1);
            srcB = (unsigned)((8 * koct + r) * FF + 128 * j + 8 * c) * 2u;
            const int re = ((lane >> 1) & 7) ^ ((koct & 1) << 2), ce = (lane & 1) ^ mat;
            srcE = (unsigned)((8 * koct + re) * FF + 4096 + 16 * j + 8 * ce) * 2u;
            const int rq = 4 * mat + ((lane >> 3) & 3), swq = ((rq >> 1) & 1) | ((koct & 1) << 1), cq = (lane & 7) ^ (swq << 1);
            srcQ = (unsigned)((j * 576 + 8 * koct + rq) * 64 + 8 * cq) * 2u;
            int hr = 256 * mt - 16 + 2 * w + ((lane >> 2) & 1); hr = hr < 0 ? 0 : hr;
            srcH = (unsigned)(hr * 64 + (lane & 3) * 16);
        }
        const unsigned dA = lds0 + RA + (2 * w) * 1024, dB = lds0 + RB + (2 * w) * 1024, dE = lds0 + RE + (mat * 4 + koct) * 256;
        const unsigned dQ = lds0 + RQ + koct * 1024 + mat * 512, dH = lds0 + RH + w * 128;
        auto issue1 = [&](int t, int sl, int i) {
            const u16* ba = xnb + (size_t)t * BLK_ELEMS;
            const u16* bw = W + (size_t)t * 32 * FF;
            if (i == 0) glds16(bw, srcB, dB + sl * 16384);
            if (i == 1) glds16(bw + 64, srcB, dB + sl * 16384 + 1024);
            if (i == 2) glds16_q(bw, srcE, dE + sl * 2048);
            if (i == 3) glds16(ba, srcA, dA + sl * 16384);
            if (i == 4) glds16_o1k(ba, srcA, dA + sl * 16384);
            if (i == 5) glds16_32(wqh + (size_t)t * 32 * 64, srcQ, dQ + sl * 4096);
            if (i == 6) glds16_8(ba, srcH, dH + sl * 1024);
        };
        STAMP(1, 0);
#pragma unroll
        for (int t = 0; t < 3; ++t)
#pragma unroll
            for (int i = 0; i < 7; ++i) issue1(t, t, i);

        const unsigned pa = lds0 + RA + blk_off(64 * wm + fr, g);
        const int swz = ((q4 >> 1) & 1) | ((g & 1) << 1);
        const unsigned pb0 = lds0 + RB + (g * 4 + wn) * 1024 + q4 * 128 + (swz << 5) + p4 * 8;
        const int emat = p4 >> 1;
        const unsigned pe0 = lds0 + RE + (emat * 4 + g) * 256 + ((q4 ^ ((g & 1) << 2)) * 32) + ((wn ^ emat) * 16) + (p4 & 1) * 8;
        const unsigned pq0 = lds0 + RQ + g * 1024 + q4 * 128 + (((2 * wn + (wm & 1)) ^ swz) << 5) + p4 * 8;
        const int hdelta = RH - RA - 4096 * wm;
        auto xr = [&](unsigned base, int imm) -> LAS char* {
            if (imm == 0) return (LAS char*)(size_t)base;
            unsigned r; asm volatile("v_xor_b32 %0, %1, %2" : "=v"(r) : "i"(imm), "v"(base)); return (LAS char*)(size_t)r;
        };
        auto rdB = [&](int sl, int nf, int t1) -> s16x4 {
            if (nf < 8) return tr_read(xr(pb0, (nf >> 1) << 5) + sl * 16384 + (nf & 1) * 2048 + t1 * 512);
            if (nf == 8) return tr_read(xr(pe0, t1 * 128) + sl * 2048);
            return tr_read(xr(pq0, (nf - 9) << 5) + sl * 4096 + t1 * 512);
        };
        auto rdA = [&](int sl, int m) -> bf16x8 { return row_frag((LAS char*)(size_t)pa + sl * 16384 + m * 1024); };
        auto rdH = [&](int sl) -> bf16x8 {
            unsigned r; asm volatile("v_add_u32 %0, %1, %2" : "=v"(r) : "s"(hdelta), "v"(pa)); return row_frag((LAS char*)(size_t)r + sl * 1024);
        };
#pragma unroll
        for (int n = 0; n < 11; ++n)
#pragma unroll
            for (int m = 0; m < 4; ++m) { acc[n][m] = (f32x4){0.f, 0.f, 0.f, 0.f}; asm volatile("" : "+v"(acc[n][m])); }
        ach = (f32x4){0.f, 0.f, 0.f, 0.f}; asm volatile("" : "+v"(ach));

        wait_vm<14>(); BAR();
        STAMP(1, 1);
        bf16x8 fa[2][2], fc[2], fh, fb[NB];
#pragma unroll
        for (int m = 0; m < 2; ++m) { fa[0][m] = rdA(0, m); fc[m] = rdA(0, 2 + m); }
#pragma unroll
        for (int i = 0; i < PF; ++i) { s16x4 lo = rdB(0, i, 0), hi = rdB(0, i, 1); fb[i] = __builtin_shufflevector(lo, hi, 0, 1, 2, 3, 4, 5, 6, 7); }
#define MM(C, A, B) mfma_ip(C, A, B)
        auto step = [&](auto S_, int t, bool do_issue, auto LAST1_, auto WAIT0_) {
            constexpr int S = decltype(S_)::value;
            constexpr bool has_next = !decltype(LAST1_)::value;
            constexpr bool wait0 = decltype(WAIT0_)::value;
            constexpr int sl = S, sn = (S + 1) & 3, cur = S & 1;
#pragma unroll
            for (int nf = 0; nf < 11; ++nf) {
                constexpr int dummy = 0; (void)dummy;
                const int r = 11 * S + nf;
                const int psl = (nf + PF < 11) ? sl : sn, nn = (nf + PF < 11) ? nf + PF : nf + PF - 11;
                const bool pf = (nf + PF < 11) || has_next;
                const bool lastg = (nf == 10);
                s16x4 lo, hi;
                if (!lastg) MM(acc[nf][0], fb[r % NB], fa[cur][0]); else MM(acc[nf][2], fb[r % NB], fc[0]);
                SCHED();
                if (nf == 0) {
                    if (!wait0) wait_vm<7>(); else wait_vm<0>();
                    BAR();
                }
                if (pf) lo = rdB(psl, nn, 0);
                SCHED();
                if (!lastg) MM(acc[nf][1], fb[r % NB], fa[cur][1]); else MM(acc[nf][3], fb[r % NB], fc[1]);
                SCHED();
                if (pf) hi = rdB(psl, nn, 1);
                if (lastg && has_next) fc[0] = rdA(sn, 2);
                SCHED();
                if (!lastg) MM(acc[nf][2], fb[r % NB], fc[0]); else MM(acc[nf][0], fb[r % NB], fa[cur][0]);
                SCHED();
                if (lastg && has_next) fc[1] = rdA(sn, 3);
                if (nf >= 4 && nf < 6 && has_next) fa[cur ^ 1][nf - 4] = rdA(sn, nf - 4);
                if (nf == 7) fh = rdH(sl);
                if ((nf < 4 || (nf >= 6 && nf < 9)) && do_issue) issue1(t + 3, (S + 3) & 3, nf < 4 ? nf : nf - 2);
                SCHED();
                if (!lastg) MM(acc[nf][3], fb[r % NB], fc[1]); else MM(acc[nf][1], fb[r % NB], fa[cur][1]);
                SCHED();
                if (nf == 9) { MM(ach, fb[r % NB], fh); SCHED(); }
                if (pf) fb[(r + PF) % NB] = __builtin_shufflevector(lo, hi, 0, 1, 2, 3, 4, 5, 6, 7);
            }
        };
        using I0 = std::integral_constant<int, 0>; using I1 = std::integral_constant<int, 1>;
        using I2 = std::integral_constant<int, 2>; using I3 = std::integral_constant<int, 3>;
        using F = std::false_type; using T = std::true_type;
#pragma unroll 1
        for (int t0 = 0; t0 < 16; t0 += 4) {
            step(I0{}, t0, true, F{}, F{});
            step(I1{}, t0 + 1, true, F{}, F{});
            step(I2{}, t0 + 2, true, F{}, F{});
            step(I3{}, t0 + 3, t0 + 3 < 15, F{}, F{});
        }
        step(I0{}, 16, false, F{}, T{});
        step(I1{}, 17, false, T{}, T{});
    }
    asm volatile("s_nop 7\n\ts_nop 7" ::: "memory");
    BAR();
    STAMP(1, 2);
    k1_epilogue(acc, ach, lds, lds0, w, xcd, idx, j, mt, Wd, wo, hb);
}

constexpr int K2_NT = 21;
constexpr int K2_RB = 0, K2_RE = 65536, K2_LDS = 81920;
constexpr int NSPLIT = 4;
__device__ __forceinline__ void gload2(bf16x8& d0, bf16x8& d1, const void* sbase, unsigned voff) {
    sbase = uni(sbase);
    asm volatile("s_nop 4\n\tglobal_load_dwordx4 %0, %2, %3\n\tglobal_load_dwordx4 %1, %2, %3 offset:1024"
                 : "=&v"(d0), "=&v"(d1) : "v"(voff), "s"(sbase) : "memory");
}
__global__ __launch_bounds__(512, 2) void k2_down(const u16* __restrict__ hb, const u16* __restrict__ Wd,
                                                  const u16* __restrict__ wo, u16* __restrict__ part, unsigned* __restrict__ cnt,
                                                  const u16* __restrict__ x, u16* __restrict__ out) {
    extern __shared__ __attribute__((aligned(16))) char smem[];
    LAS char* lds = (LAS char*)smem;
    const int tid = threadIdx.x, lane = tid & 63;
    const int w = __builtin_amdgcn_readfirstlane(tid >> 6);
    const int b = blockIdx.x, xcd = b & 7, idx = b >> 3;
    const int split = xcd >> 1, mt = (xcd & 1) * 8 + (idx >> 2), nt = idx & 3;
    const int wm = w >> 1, kh = w & 1;
    const int T0 = split == 0 ? 0 : 21 + 20 * (split - 1);

    const unsigned srcA = (unsigned)(kh * (int)BLK_ELEMS * 2 + blk_off(128 * mt + 32 * wm + (lane & 15), lane >> 4));
    const int koct = w >> 1, sub = w & 1;
    unsigned offB, offE;
    {
        const int r = lane >> 3, swz = ((r >> 1) & 1) | ((koct & 1) << 1), c = (lane & 7) ^ (swz << 1);
        offB = (unsigned)((32 * sub + 8 * koct + r) * DM + 128 * nt + 8 * c) * 2u;
        const int re = ((lane >> 1) & 7) ^ ((koct & 1) << 2), ce = (lane & 1);
        offE = (unsigned)((32 * sub + 8 * koct + re) * DM + 512 + 16 * nt + 8 * ce) * 2u;
    }
    const unsigned lds0 = (unsigned)(size_t)lds;
    const unsigned dB = lds0 + K2_RB + (2 * w) * 1024, dE = lds0 + K2_RE + (sub * 4 + koct) * 256;
    bf16x8 fa[4][2];
    auto issue1 = [&](int t, int sl, int i, bf16x8& a0, bf16x8& a1) {
        int T = T0 + t; if (t == K2_NT - 1 && split != 0) T -= 1;
        const u16* Wt = T < 72 ? Wd + (size_t)T * 64 * DM : wo + (size_t)(T - 72) * 64 * DM;
        const u16* ba = hb + (size_t)T * 2 * BLK_ELEMS;
        if (i == 0) glds16(Wt, offB, dB + sl * 16384);
        if (i == 1) glds16(Wt + 64, offB, dB + sl * 16384 + 1024);
        if (i == 2) glds16_q(Wt, offE, dE + sl * 2048);
        if (i == 3) gload2(a0, a1, ba, srcA);
    };
    STAMP(2, 0);
#pragma unroll
    for (int t = 0; t < 3; ++t)
#pragma unroll
        for (int i = 0; i < 4; ++i) issue1(t, t, i, fa[t][0], fa[t][1]);

    const int fr = lane & 15, g = lane >> 4, q4 = (lane >> 2) & 3, p4 = lane & 3;
    const int swz = ((q4 >> 1) & 1) | ((g & 1) << 1);
    LAS char* pb[4];
#pragma unroll
    for (int u = 0; u < 4; ++u) pb[u] = lds + K2_RB + (g * 4 + kh * 2) * 1024 + q4 * 128 + ((u ^ swz) << 5) + p4 * 8;
    LAS char* pe0 = lds + K2_RE + (kh * 4 + g) * 256 + ((q4 ^ ((g & 1) << 2)) * 32) + p4 * 8;
    LAS char* pe1 = lds + K2_RE + (kh * 4 + g) * 256 + (((4 + q4) ^ ((g & 1) << 2)) * 32) + p4 * 8;
    auto rdB = [&](int sl, int nf, int t1) -> s16x4 {
        if (nf < 8) return tr_read(pb[nf & 3] + sl * 16384 + (nf >> 2) * 1024 + t1 * 512);
        return tr_read((t1 ? pe1 : pe0) + sl * 2048);
    };

    f32x4 acc[9][2];
#pragma unroll
    for (int n = 0; n < 9; ++n)
#pragma unroll
        for (int m = 0; m < 2; ++m) { acc[n][m] = (f32x4){0.f, 0.f, 0.f, 0.f}; asm volatile("" : "+v"(acc[n][m])); }

    wait_vm<10>(); BAR();
    STAMP(2, 1);
    constexpr int PF2 = 4;
    bf16x8 fb[9];
#pragma unroll
    for (int i = 0; i < PF2; ++i) { s16x4 lo = rdB(0, i, 0), hi = rdB(0, i, 1); fb[i] = __builtin_shufflevector(lo, hi, 0, 1, 2, 3, 4, 5, 6, 7); }
    auto step = [&](auto S_, int t, bool do_issue, bool wait0, auto LAST_) {
        constexpr int S = decltype(S_)::value;
        constexpr bool has_next = !decltype(LAST_)::value;
        constexpr int sl = S, sn = (S + 1) & 3;
        asm volatile("" : "+v"(fa[sl][0]), "+v"(fa[sl][1]));
        if (!has_next && split != 0) {
#pragma unroll
            for (int m = 0; m < 2; ++m) fa[sl][m] = (bf16x8){0, 0, 0, 0, 0, 0, 0, 0};
            asm volatile("s_nop 1" : "+v"(fa[sl][0]), "+v"(fa[sl][1]));
        }
#pragma unroll
        for (int nf = 0; nf < 9; ++nf) {
            const int psl = (nf + PF2 < 9) ? sl : sn, nn = (nf + PF2 < 9) ? nf + PF2 : nf + PF2 - 9;
            const bool pf = (nf + PF2 < 9) || has_next;
            s16x4 lo, hi;
            mfma_ip(acc[nf][0], fb[nf], fa[sl][0]);
            SCHED();
            if (nf == 0) {
                if (!wait0) wait_vm<5>(); else wait_vm<0>();
                BAR();
            }
            if (pf) lo = rdB(psl, nn, 0);
            SCHED();
            mfma_ip(acc[nf][1], fb[nf], fa[sl][1]);
            SCHED();
            if (pf) hi = rdB(psl, nn, 1);
            if (nf >= 1 && nf < 5 && do_issue) issue1(t + 3, (S + 3) & 3, nf - 1, fa[(S + 3) & 3][0], fa[(S + 3) & 3][1]);
            SCHED();
            if (pf) fb[nn] = __builtin_shufflevector(lo, hi, 0, 1, 2, 3, 4, 5, 6, 7);
        }
    };
    using I0 = std::integral_constant<int, 0>; using I1 = std::integral_constant<int, 1>;
    using I2 = std::integral_constant<int, 2>; using I3 = std::integral_constant<int, 3>;
#pragma unroll 1
    for (int t0 = 0; t0 < 20; t0 += 4) {
        step(I0{}, t0, true, false, std::false_type{});
        step(I1{}, t0 + 1, true, false, std::false_type{});
        step(I2{}, t0 + 2, t0 + 2 < 18, false, std::false_type{});
        step(I3{}, t0 + 3, t0 + 3 < 18, t0 + 3 == 19, std::false_type{});
    }
    step(I0{}, 20, false, true, std::true_type{});
    asm volatile("s_nop 7\n\ts_nop 7" ::: "memory");
    BAR();
    STAMP(2, 2);
    f32x4 val[10];
    {
        LAS char* xw = lds + w * 10240;
        LAS char* xr = lds + (w ^ 1) * 10240;
#pragma unroll
        for (int n = 0; n < 9; ++n) {
            const bool give = kh ? (n < 5) : (n >= 5);
            if (give) {
#pragma unroll
                for (int m = 0; m < 2; ++m) *(LAS f32x4*)(xw + ((kh ? n : n - 5) * 2 + m) * 1024 + lane * 16) = acc[n][m];
            }
        }
        asm volatile("s_waitcnt lgkmcnt(0)" ::: "memory");
        BAR();
#pragma unroll
        for (int i = 0; i < 10; ++i) {
            const f32x4 mine = kh ? acc[(5 + (i >> 1)) < 9 ? 5 + (i >> 1) : 8][i & 1] : acc[i >> 1][i & 1];
            val[i] = mine + *(LAS f32x4*)(xr + i * 1024 + lane * 16);
        }
    }
    {
        const int nit = kh ? 8 : 10;
        const int grp = mt * 4 + nt;
        const __amdgpu_buffer_rsrc_t rp = __builtin_amdgcn_make_buffer_rsrc((void*)part, 0, NSPLIT * 64 * 8 * 10 * 512, 0x00020000);
        const unsigned slot = (unsigned)((grp * 8 + w) * 10 * 512 + lane * 8);
        constexpr unsigned SPL = 64 * 8 * 10 * 512;
        u32x2 xv[3]; unsigned xoff[3];
#pragma unroll
        for (int q = 0; q < 3; ++q) {
            int i = split + 4 * q; i = i < nit ? i : split;
            const int n = kh ? 5 + (i >> 1) : (i >> 1), m = i & 1;
            const int row = 128 * mt + 32 * wm + 16 * m + fr;
            const int col = (n < 8 ? 128 * nt + 16 * n : 512 + 16 * nt) + 4 * g;
            xoff[q] = (unsigned)(row * DM + col);
            xv[q] = *(const u32x2*)(x + xoff[q]);
        }
#pragma unroll
        for (int i = 0; i < 10; ++i) {
            if (i < nit && (i & 3) != split) {
                u32x2 v; v[0] = pk_bf16(val[i][0], val[i][1]); v[1] = pk_bf16(val[i][2], val[i][3]);
                __builtin_amdgcn_raw_buffer_store_b64(v, rp, slot + split * SPL + i * 512, 0, 16);
            }
        }
        STAMP(2, 3);
        asm volatile("s_waitcnt vmcnt(0)" ::: "memory");
        BAR();
        STAMP(2, 4);
        unsigned* c = cnt + grp * 64;
        if (tid == 0) {
            __hip_atomic_fetch_add(c, 1u, __ATOMIC_RELAXED, __HIP_MEMORY_SCOPE_AGENT);
            unsigned spins = 0;
            while (__hip_atomic_load(c, __ATOMIC_RELAXED, __HIP_MEMORY_SCOPE_AGENT) < (unsigned)NSPLIT) {
                __builtin_amdgcn_s_sleep(1);
                if (++spins > (1u << 22)) break;
            }
        }
        BAR();
        STAMP(2, 5);
        u32x2 pv[3][3];
#pragma unroll
        for (int q = 0; q < 3; ++q) {
            const int i = split + 4 * q;
#pragma unroll
            for (int s2 = 1; s2 < NSPLIT; ++s2) {
                const int so = (split + s2) & 3;
                const unsigned off = i < nit ? slot + so * SPL + i * 512 : 0xfffffff0u;
                pv[q][s2 - 1] = __builtin_amdgcn_raw_buffer_load_b64(rp, off, 0, 16);
            }
        }
#pragma unroll
        for (int q = 0; q < 3; ++q) {
            const int i = split + 4 * q;
            if (i < nit) {
                f32x4 o = val[0];
#pragma unroll
                for (int ii = 0; ii < 10; ++ii) if (ii == i) o = val[ii];
#pragma unroll
                for (int s2 = 0; s2 < 3; ++s2) { o[0] += bf_lo(pv[q][s2][0]); o[1] += bf_hi(pv[q][s2][0]); o[2] += bf_lo(pv[q][s2][1]); o[3] += bf_hi(pv[q][s2][1]); }
                u32x2 v; v[0] = pk_bf16(o[0] + bf_lo(xv[q][0]), o[1] + bf_hi(xv[q][0])); v[1] = pk_bf16(o[2] + bf_lo(xv[q][1]), o[3] + bf_hi(xv[q][1]));
                *(u32x2*)(out + xoff[q]) = v;
            }
        }
    }
    STAMP(2, 15);
}

extern "C" void kernel_launch(void* const* d_in, const int* in_sizes, int n_in, void* d_out, int out_size, void* d_ws,
                              size_t ws_size, hipStream_t stream) {
    const u16* x = (const u16*)d_in[0];
    const u16* wqkv = (const u16*)d_in[1];
    const u16* wo = (const u16*)d_in[2];
    const u16* Wg = (const u16*)d_in[4];
    const u16* Wu = (const u16*)d_in[5];
    const u16* Wd = (const u16*)d_in[6];
    char* ws = (char*)d_ws;
    u16* xnb = (u16*)ws;
    u16* hb = (u16*)(ws + (4u << 20));
    u16* part = (u16*)(ws + (32u << 20));
    unsigned* cnt = (unsigned*)(ws + (56u << 20));
    u16* wqh = (u16*)(ws + (48u << 20));
    (void)hipFuncSetAttribute((const void*)k1_gateup, hipFuncAttributeMaxDynamicSharedMemorySize, K1_LDS);
    (void)hipFuncSetAttribute((const void*)k2_down, hipFuncAttributeMaxDynamicSharedMemorySize, K2_LDS);
    k0_ln<<<256, 256, 0, stream>>>(x, xnb, wqkv, wqh);
    k1_gateup<<<256, 512, K1_LDS, stream>>>(xnb, Wg, Wu, wqh, Wd, wo, hb, cnt);
    k2_down<<<256, 512, K2_LDS, stream>>>(hb, Wd, wo, part, cnt, x, (u16*)d_out);
}
```

```cpp
#include <hip/hip_runtime.h>
#include <hip/hip_bf16.h>
#include <stdint.h>
#include <type_traits>

typedef unsigned short u16;
typedef short bf16x8 __attribute__((ext_vector_type(8)));
typedef short s16x4 __attribute__((ext_vector_type(4)));
typedef float f32x4 __attribute__((ext_vector_type(4)));
typedef float f32x2 __attribute__((ext_vector_type(2)));
typedef __bf16 bf16x2_t __attribute__((ext_vector_type(2)));
typedef unsigned u32x4 __attribute__((ext_vector_type(4)));
typedef unsigned u32x2 __attribute__((ext_vector_type(2)));
#define LAS __attribute__((address_space(3)))
#define GAS __attribute__((address_space(1)))

constexpr int SEQ = 2048, DM = 576, FF = 4608;
constexpr int NKB_X = 18;
constexpr int NKB_H = 162;
constexpr size_t BLK_ELEMS = (size_t)SEQ * 32;

__device__ __forceinline__ unsigned pk_bf16(float lo, float hi) {
    f32x2 v = {lo, hi};
    bf16x2_t b = __builtin_convertvector(v, bf16x2_t);
    return __builtin_bit_cast(unsigned, b);
}
__device__ __forceinline__ float bf_lo(unsigned u) { return __uint_as_float(u << 16); }
__device__ __forceinline__ float bf_hi(unsigned u) { return __uint_as_float(u & 0xffff0000u); }
__device__ __forceinline__ float rbf(float x) { return bf_lo(pk_bf16(x, x)); }

__device__ __forceinline__ const void* uni(const void* p) {
    const unsigned long long v = (unsigned long long)p;
    const unsigned lo = __builtin_amdgcn_readfirstlane((unsigned)v), hi = __builtin_amdgcn_readfirstlane((unsigned)(v >> 32));
    return (const void*)(((unsigned long long)hi << 32) | lo);
}
__device__ __forceinline__ void glds16(const void* sbase, unsigned voff, unsigned lds) {
    sbase = uni(sbase); lds = __builtin_amdgcn_readfirstlane(lds);
    unsigned sv;
    asm volatile("s_mov_b32 %0, m0\n\ts_mov_b32 m0, %3\n\ts_nop 3\n\tglobal_load_lds_dwordx4 %1, %2\n\ts_mov_b32 m0, %0"
                 : "=&s"(sv) : "v"(voff), "s"(sbase), "s"(lds) : "memory");
}
__device__ __forceinline__ void glds16_o1k(const void* sbase, unsigned voff, unsigned lds) {
    sbase = uni(sbase); lds = __builtin_amdgcn_readfirstlane(lds);
    unsigned sv;
    asm volatile("s_mov_b32 %0, m0\n\ts_mov_b32 m0, %3\n\ts_nop 3\n\tglobal_load_lds_dwordx4 %1, %2 offset:1024\n\ts_mov_b32 m0, %0"
                 : "=&s"(sv) : "v"(voff), "s"(sbase), "s"(lds) : "memory");
}
__device__ __forceinline__ void glds16_q(const void* sbase, unsigned voff, unsigned lds) {
    sbase = uni(sbase); lds = __builtin_amdgcn_readfirstlane(lds);
    unsigned sv; unsigned long long ex;
    asm volatile("s_mov_b32 %0, m0\n\ts_mov_b64 %1, exec\n\ts_mov_b32 m0, %4\n\ts_mov_b64 exec, 0xffff\n\ts_nop 1\n\tglobal_load_lds_dwordx4 %2, %3\n\ts_mov_b64 exec, %1\n\ts_mov_b32 m0, %0"
                 : "=&s"(sv), "=&s"(ex) : "v"(voff), "s"(sbase), "s"(lds) : "memory");
}
__device__ __forceinline__ void glds16_m(const void* sbase, unsigned voff, unsigned lds) {
    sbase = uni(sbase); lds = __builtin_amdgcn_readfirstlane(lds);
    unsigned sv; unsigned long long ex;
    asm volatile("s_mov_b32 %0, m0\n\ts_mov_b64 %1, exec\n\ts_mov_b32 m0, %4\n\ts_mov_b32 exec_lo, -1\n\ts_mov_b32 exec_hi, 0xff\n\ts_nop 1\n\tglobal_load_lds_dwordx4 %2, %3\n\ts_mov_b64 exec, %1\n\ts_mov_b32 m0, %0"
                 : "=&s"(sv), "=&s"(ex) : "v"(voff), "s"(sbase), "s"(lds) : "memory");
}
#define SCHED() __builtin_amdgcn_sched_barrier(0)
#define STAMP(kid, i) do {} while (0)
#define WSTAMP(i) do {} while (0)
__device__ __forceinline__ s16x4 tr_read(LAS char* p) {
    return __builtin_bit_cast(s16x4, __builtin_amdgcn_ds_read_tr16_b64_v4i16((LAS s16x4*)p));
}
__device__ __forceinline__ bf16x8 tr_frag(LAS char* p, int off_t1) {
    s16x4 lo = tr_read(p), hi = tr_read(p + off_t1);
    return __builtin_shufflevector(lo, hi, 0, 1, 2, 3, 4, 5, 6, 7);
}
__device__ __forceinline__ bf16x8 row_frag(LAS char* p) { return *(LAS bf16x8*)p; }
#define MFMA16(a, b, c) __builtin_amdgcn_mfma_f32_16x16x32_bf16((a), (b), (c), 0, 0, 0)
__device__ __forceinline__ void mfma_ip(f32x4& c, bf16x8 a, bf16x8 b) { asm volatile("v_mfma_f32_16x16x32_bf16 %0, %1, %2, %0" : "+v"(c) : "v"(a), "v"(b)); }
#define WAIT_V(n) asm volatile("s_waitcnt vmcnt(" #n ")" ::: "memory")
#define BAR() do { asm volatile("" ::: "memory"); __builtin_amdgcn_s_barrier(); asm volatile("" ::: "memory"); } while (0)

__device__ __forceinline__ int blk_off(int row, int chunk) { return row * 64 + ((chunk ^ (((row >> 3) & 1) << 1)) << 4); }

__global__ __launch_bounds__(256) void k0_ln(const u16* __restrict__ x, u16* __restrict__ xnb,
                                            const u16* __restrict__ wqkv, u16* __restrict__ wqh,
                                            const u16* __restrict__ Wg, const u16* __restrict__ Wu, u16* __restrict__ gx,
                                            const u16* __restrict__ Wd, const u16* __restrict__ wo, u16* __restrict__ dx) {
    if (threadIdx.x >= 64) {
        const int l = threadIdx.x - 64, head = blockIdx.x >> 3, k0 = (blockIdx.x & 7) * 72;
        unsigned v[12];
#pragma unroll
        for (int i = 0; i < 12; ++i) {
            const int id = l + 192 * i, k = k0 + (id >> 5), cp = id & 31;
            const int part = cp >= 18 ? 2 : (cp >= 9 ? 1 : 0), d2 = cp - part * 9;
            const int src = (part == 2 ? 0 : part + 1) * DM + 18 * head + 2 * d2;
            v[i] = cp < 27 ? *(const unsigned*)(wqkv + (size_t)k * (3 * DM) + src) : 0u;
        }
        u32x4 c[3];
#pragma unroll
        for (int i = 0; i < 2; ++i) {
            const int id = l + 192 * i;
            if (id < 288) {
                const int gid = blockIdx.x * 288 + id, mat = gid / 36864, r = gid - mat * 36864, k = r >> 6, ch = r & 63;
                c[i] = *(const u32x4*)((mat ? Wu : Wg) + (size_t)k * FF + 4096 + 8 * ch);
            }
        }
        if (l < 162) {
            const int gid = blockIdx.x * 162 + l, k = gid >> 3, ch = gid & 7;
            c[2] = *(const u32x4*)((k < FF ? Wd + (size_t)k * DM : wo + (size_t)(k - FF) * DM) + 512 + 8 * ch);
        }
#pragma unroll
        for (int i = 0; i < 12; ++i) {
            const int id = l + 192 * i;
            *(unsigned*)(wqh + ((size_t)head * 576 + k0) * 64 + 2 * id) = v[i];
        }
#pragma unroll
        for (int i = 0; i < 2; ++i) {
            const int id = l + 192 * i;
            if (id < 288) {
                const int gid = blockIdx.x * 288 + id, mat = gid / 36864, r = gid - mat * 36864, k = r >> 6, ch = r & 63;
                *(u32x4*)(gx + (((size_t)mat * 32 + (ch >> 1)) * 576 + k) * 16 + 8 * (ch & 1)) = c[i];
            }
        }
        if (l < 162) {
            const int gid = blockIdx.x * 162 + l, k = gid >> 3, ch = gid & 7;
            *(u32x4*)(dx + ((size_t)(ch >> 1) * 5184 + k) * 16 + 8 * (ch & 1)) = c[2];
        }
        return;
    }
    const int lane = threadIdx.x, i = lane & 7, row = blockIdx.x * 8 + (lane >> 3);
    u32x4 xv[9];
#pragma unroll
    for (int cc = 0; cc < 9; ++cc) xv[cc] = *(const u32x4*)(x + (size_t)row * DM + (i + 8 * cc) * 8);
    float s = 0.f;
#pragma unroll
    for (int cc = 0; cc < 9; ++cc)
#pragma unroll
        for (int e = 0; e < 4; ++e) s += bf_lo(xv[cc][e]) + bf_hi(xv[cc][e]);
    s += __shfl_xor(s, 1); s += __shfl_xor(s, 2); s += __shfl_xor(s, 4);
    const float mean = s * (1.0f / 576.0f);
    float q = 0.f;
#pragma unroll
    for (int cc = 0; cc < 9; ++cc)
#pragma unroll
        for (int e = 0; e < 4; ++e) { const float a = bf_lo(xv[cc][e]) - mean, c2 = bf_hi(xv[cc][e]) - mean; q += a * a + c2 * c2; }
    q += __shfl_xor(q, 1); q += __shfl_xor(q, 2); q += __shfl_xor(q, 4);
    const float mb = rbf(mean), vb = rbf(q * (1.0f / 576.0f));
    const float sb = rbf(sqrtf(rbf(vb + 1e-5f)));
    const float rs = 1.0f / sb;
#pragma unroll
    for (int cc = 0; cc < 9; ++cc) {
        u32x4 o;
#pragma unroll
        for (int e = 0; e < 4; ++e) {
            const unsigned d = pk_bf16(bf_lo(xv[cc][e]) - mb, bf_hi(xv[cc][e]) - mb);
            o[e] = pk_bf16(bf_lo(d) * rs, bf_hi(d) * rs);
        }
        const int blk = 2 * cc + (i >> 2);
        *(u32x4*)((char*)xnb + (size_t)blk * (BLK_ELEMS * 2) + blk_off(row, i & 3)) = o;
    }
}

constexpr int RA = 0, RB = 65536, RE = 131072, RQ = 139264, RH = 155648;
constexpr int T_DUMMY = 159744;
constexpr int K1_LDS = T_DUMMY + 1024;
constexpr int QS = 144;
#define HST_AUX 16
#define PF 3
constexpr int NB = PF + 1;
template <int N> __device__ __forceinline__ void wait_vm() { asm volatile("s_waitcnt vmcnt(%0)" :: "i"(N) : "memory"); }
#define GLDS_MASKED(NAME, LO, HI) \
__device__ __forceinline__ void NAME(const void* sbase, unsigned voff, unsigned lds) { \
    sbase = uni(sbase); lds = __builtin_amdgcn_readfirstlane(lds); \
    unsigned sv; unsigned long long ex; \
    asm volatile("s_mov_b32 %0, m0\n\ts_mov_b64 %1, exec\n\ts_mov_b32 m0, %4\n\ts_mov_b32 exec_lo, " #LO "\n\ts_mov_b32 exec_hi, " #HI "\n\tglobal_load_lds_dwordx4 %2, %3\n\ts_mov_b64 exec, %1\n\ts_mov_b32 m0, %0" \
                 : "=&s"(sv), "=&s"(ex) : "v"(voff), "s"(sbase), "s"(lds) : "memory"); }
GLDS_MASKED(glds16_32, -1, 0)
GLDS_MASKED(glds16_8, 0xff, 0)

__device__ __forceinline__ void k1_epilogue(f32x4 (&acc)[11][4], f32x4& ach, LAS char* lds, unsigned lds0, int w, int xcd, int idx,
                                            int j, int mt, const u16* __restrict__ Wd, const u16* __restrict__ wo, u16* __restrict__ hb) {
    int tid = threadIdx.x; asm volatile("" : "+v"(tid));
    const int lane = tid & 63, fr = lane & 15, g = lane >> 4;
    const int wm = w >> 1, wn = w & 1;
    {
        const int split = xcd >> 1;
        const unsigned start = (unsigned)((split == 0 ? 0 : 1344 + 1280 * (split - 1)) * DM * 2);
        const unsigned len = (unsigned)((split == 0 ? 1344 : 1280) * DM * 2), lwd = (unsigned)(704 * DM * 2);
#pragma unroll
        for (int i = 0; i < 6; ++i) {
            unsigned o = (unsigned)((idx * 8 + w) * 6144 + i * 1024); o = o + 1024 <= len ? o : 0u;
            const char* src = (split == 3 && o >= lwd) ? (const char*)wo + (o - lwd) : (const char*)Wd + start + o;
            glds16(src, (unsigned)lane * 16u, lds0 + T_DUMMY);
        }
    }
    LAS char* qt = lds;
    {
#pragma unroll
        for (int u = 0; u < 2; ++u) {
            const int nn = 16 * (2 * wn + (u ^ (wm & 1))) + 4 * g;
            const int h0 = nn / 18, h1 = (nn + 2) / 18;
            const int o0 = h0 * 48 + (nn - h0 * 18) * 2, o1 = h1 * 48 + (nn + 2 - h1 * 18) * 2;
#pragma unroll
            for (int m = 0; m < 4; ++m) {
                LAS char* rp = qt + (16 + 64 * wm + 16 * m + fr) * QS;
                if (h0 < 3) *(LAS unsigned*)(rp + o0) = pk_bf16(acc[9 + u][m][0], acc[9 + u][m][1]);
                if (h1 < 3) *(LAS unsigned*)(rp + o1) = pk_bf16(acc[9 + u][m][2], acc[9 + u][m][3]);
            }
            if (u == 0 && (w == 0 || w == 2 || w == 1)) {
                LAS char* rp = qt + fr * QS;
                if (h0 < 3) *(LAS unsigned*)(rp + o0) = pk_bf16(ach[0], ach[1]);
                if (h1 < 3) *(LAS unsigned*)(rp + o1) = pk_bf16(ach[2], ach[3]);
            }
        }
    }
    asm volatile("s_waitcnt lgkmcnt(0)" ::: "memory");
    BAR();
    STAMP(1, 3);
    const __amdgpu_buffer_rsrc_t rh = __builtin_amdgcn_make_buffer_rsrc((void*)hb, 0, (int)(NKB_H * BLK_ELEMS * 2), 0x00020000);
    u32x4 hp[4][2]; u32x2 hm[4];
#pragma unroll
    for (int m = 0; m < 4; ++m) {
#pragma unroll
        for (int pp = 0; pp < 2; ++pp) {
            unsigned v[2][2];
#pragma unroll
            for (int hh = 0; hh < 2; ++hh) {
                const int gt = 2 * pp + hh;
                float hv[4];
#pragma unroll
                for (int e = 0; e < 4; ++e) hv[e] = fmaxf(acc[2 * gt][m][e], 0.f) * acc[2 * gt + 1][m][e];
                v[hh][0] = pk_bf16(hv[0], hv[1]); v[hh][1] = pk_bf16(hv[2], hv[3]);
            }
            auto r0 = __builtin_amdgcn_permlane16_swap(v[0][0], v[1][0], false, false);
            auto r1 = __builtin_amdgcn_permlane16_swap(v[0][1], v[1][1], false, false);
            hp[m][pp][0] = r0[0]; hp[m][pp][1] = r1[0]; hp[m][pp][2] = r0[1]; hp[m][pp][3] = r1[1];
        }
        {
            float hv[4];
#pragma unroll
            for (int e = 0; e < 4; ++e) {
                const unsigned mine = __float_as_uint(acc[8][m][e]);
                auto r = __builtin_amdgcn_permlane32_swap(mine, mine, false, false);
                hv[e] = fmaxf(__uint_as_float(r[0]), 0.f) * __uint_as_float(r[1]);
            }
            hm[m][0] = pk_bf16(hv[0], hv[1]); hm[m][1] = pk_bf16(hv[2], hv[3]);
        }
        asm volatile("" : "+v"(hp[m][0]), "+v"(hp[m][1]), "+v"(hm[m]));
    }
    SCHED();
    auto hstore = [&](int m) {
        const int row = 256 * mt + 64 * wm + 16 * m + fr;
#pragma unroll
        for (int pp = 0; pp < 2; ++pp) {
            const int blk = 4 * j + 2 * wn + pp, chunk = ((g & 1) << 1) | (g >> 1);
            __builtin_amdgcn_raw_buffer_store_b128(hp[m][pp], rh, (unsigned)(blk * (int)(BLK_ELEMS * 2) + blk_off(row, chunk)), 0, HST_AUX);
        }
        const int blk = 128 + (j >> 1), chunk = (j & 1) * 2 + wn;
        const unsigned off = g < 2 ? (unsigned)(blk * (int)(BLK_ELEMS * 2) + blk_off(row, chunk) + g * 8) : 0xfffffff0u;
        __builtin_amdgcn_raw_buffer_store_b64(hm[m], rh, off, 0, HST_AUX);
    };
    {
        const int s = tid >> 1, half = tid & 1;
        const int lrow = 16 + s, grow = 256 * mt + s;
        auto ld18 = [&](LAS const char* p, unsigned (&v)[9]) {
            const u32x4 a = *(LAS const u32x4*)p, c = *(LAS const u32x4*)(p + 16);
            v[0] = a[0]; v[1] = a[1]; v[2] = a[2]; v[3] = a[3]; v[4] = c[0]; v[5] = c[1]; v[6] = c[2]; v[7] = c[3];
            v[8] = *(LAS const unsigned*)(p + 32);
        };
        hstore(0);
        SCHED();
        unsigned qv[9];
        ld18(qt + lrow * QS + 96, qv);
        float lg[4];
#pragma unroll
        for (int ww = 0; ww < 4; ++ww) {
            const int wi = 4 * half + ww;
            unsigned kv[9];
            ld18(qt + (lrow - 2 * wi) * QS, kv);
            float a = 0.f;
#pragma unroll
            for (int i = 0; i < 9; ++i) a = __builtin_amdgcn_fdot2_f32_bf16(__builtin_bit_cast(bf16x2_t, qv[i]), __builtin_bit_cast(bf16x2_t, kv[i]), a, false);
            a = rbf(rbf(a) * 0.23570226039551584f);
            lg[ww] = (grow - 2 * wi >= 0) ? a : -1e9f;
        }
        SCHED();
        hstore(1);
        SCHED();
        float mx = fmaxf(fmaxf(lg[0], lg[1]), fmaxf(lg[2], lg[3]));
        mx = fmaxf(mx, __shfl_xor(mx, 1));
        float ex[4], sum = 0.f;
#pragma unroll
        for (int ww = 0; ww < 4; ++ww) { ex[ww] = __expf(lg[ww] - mx); sum += ex[ww]; }
        sum += __shfl_xor(sum, 1);
        const float inv = 1.0f / sum;
        float o[18];
#pragma unroll
        for (int i = 0; i < 18; ++i) o[i] = 0.f;
#pragma unroll
        for (int ww = 0; ww < 4; ++ww) {
            const int wi = 4 * half + ww;
            const float pw = (grow - 2 * wi >= 0) ? rbf(ex[ww] * inv) : 0.f;
            unsigned vv[9];
            ld18(qt + (lrow - 2 * wi) * QS + 48, vv);
#pragma unroll
            for (int i = 0; i < 9; ++i) { o[2 * i] += pw * bf_lo(vv[i]); o[2 * i + 1] += pw * bf_hi(vv[i]); }
        }
        SCHED();
        hstore(2);
        SCHED();
#pragma unroll
        for (int ii = 0; ii < 5; ++ii) {
            const float s0 = half ? o[2 * ii] : o[(10 + 2 * ii) % 18], s1 = half ? o[2 * ii + 1] : o[(11 + 2 * ii) % 18];
            const float r0 = __shfl_xor(s0, 1), r1 = __shfl_xor(s1, 1);
            const float m0 = half ? o[(10 + 2 * ii) % 18] : o[2 * ii], m1 = half ? o[(11 + 2 * ii) % 18] : o[2 * ii + 1];
            const int i = half * 5 + ii;
            if (i < 9) {
                const int col = FF + 18 * j + 2 * i;
                *(unsigned*)((char*)hb + (size_t)(col >> 5) * (BLK_ELEMS * 2) + blk_off(grow, (col >> 3) & 3) + (col & 7) * 2) = pk_bf16(m0 + r0, m1 + r1);
            }
        }
        SCHED();
        hstore(3);
    }
    STAMP(1, 15);
}

__global__ __launch_bounds__(512, 2) void k1_gateup(const u16* __restrict__ xnb, const u16* __restrict__ Wg,
                                                    const u16* __restrict__ Wu, const u16* __restrict__ wqh, const u16* __restrict__ gx,
                                                    const u16* __restrict__ Wd, const u16* __restrict__ wo, u16* __restrict__ hb, unsigned* __restrict__ cnt) {
    extern __shared__ __attribute__((aligned(16))) char smem[];
    LAS char* lds = (LAS char*)smem;
    const int tid = threadIdx.x, lane = tid & 63;
    if (blockIdx.x < 64 && tid == 0) cnt[blockIdx.x * 64] = 0u;
    const int w = __builtin_amdgcn_readfirstlane(tid >> 6);
    const int b = blockIdx.x, xcd = b & 7, idx = b >> 3;
    const int j = 4 * xcd + (idx & 3), mt = idx >> 2;
    const int wm = w >> 1, wn = w & 1;
    const unsigned lds0 = (unsigned)(size_t)lds;
    const int fr = lane & 15, g = lane >> 4, q4 = (lane >> 2) & 3, p4 = lane & 3;

    f32x4 acc[11][4], ach;
    {
        const unsigned srcA = (unsigned)((256 * mt + 32 * w) * 32 + lane * 8) * 2u;
        const int koct = w >> 1, mat = w & 1;
        const u16* W = mat ? Wu : Wg;
        unsigned srcB, srcE, srcQ, srcH;
        {
            const int r = lane >> 3, swz = ((r >> 1) & 1) | ((koct & 1) << 1), c = (lane & 7) ^ (swz << 1);
            srcB = (unsigned)((8 * koct + r) * FF + 128 * j + 8 * c) * 2u;
            const int re = ((lane >> 1) & 7) ^ ((koct & 1) << 2), ce = (lane & 1) ^ mat;
            srcE = (unsigned)(((mat * 32 + j) * 576 + 8 * koct + re) * 16 + 8 * ce) * 2u;
            const int rq = 4 * mat + ((lane >> 3) & 3), swq = ((rq >> 1) & 1) | ((koct & 1) << 1), cq = (lane & 7) ^ (swq << 1);
            srcQ = (unsigned)((j * 576 + 8 * koct + rq) * 64 + 8 * cq) * 2u;
            int hr = 256 * mt - 16 + 2 * w + ((lane >> 2) & 1); hr = hr < 0 ? 0 : hr;
            srcH = (unsigned)(hr * 64 + (lane & 3) * 16);
        }
        const unsigned dA = lds0 + RA + (2 * w) * 1024, dB = lds0 + RB + (2 * w) * 1024, dE = lds0 + RE + (mat * 4 + koct) * 256;
        const unsigned dQ = lds0 + RQ + koct * 1024 + mat * 512, dH = lds0 + RH + w * 128;
        auto issue1 = [&](int t, int sl, int i) {
            const u16* ba = xnb + (size_t)t * BLK_ELEMS;
            const u16* bw = W + (size_t)t * 32 * FF;
            if (i == 0) glds16(bw, srcB, dB + sl * 16384);
            if (i == 1) glds16(bw + 64, srcB, dB + sl * 16384 + 1024);
            if (i == 2) glds16_q(gx + (size_t)t * 32 * 16, srcE, dE + sl * 2048);
            if (i == 3) glds16(ba, srcA, dA + sl * 16384);
            if (i == 4) glds16_o1k(ba, srcA, dA + sl * 16384);
            if (i == 5) glds16_32(wqh + (size_t)t * 32 * 64, srcQ, dQ + sl * 4096);
            if (i == 6) glds16_8(ba, srcH, dH + sl * 1024);
        };
        STAMP(1, 0);
#pragma unroll
        for (int t = 0; t < 3; ++t)
#pragma unroll
            for (int i = 0; i < 7; ++i) issue1(t, t, i);

        const unsigned pa = lds0 + RA + blk_off(64 * wm + fr, g);
        const int swz = ((q4 >> 1) & 1) | ((g & 1) << 1);
        const unsigned pb0 = lds0 + RB + (g * 4 + wn) * 1024 + q4 * 128 + (swz << 5) + p4 * 8;
        const int emat = p4 >> 1;
        const unsigned pe0 = lds0 + RE + (emat * 4 + g) * 256 + ((q4 ^ ((g & 1) << 2)) * 32) + ((wn ^ emat) * 16) + (p4 & 1) * 8;
        const unsigned pq0 = lds0 + RQ + g * 1024 + q4 * 128 + (((2 * wn + (wm & 1)) ^ swz) << 5) + p4 * 8;
        const int hdelta = RH - RA - 4096 * wm;
        auto xr = [&](unsigned base, int imm) -> LAS char* {
            if (imm == 0) return (LAS char*)(size_t)base;
            unsigned r; asm volatile("v_xor_b32 %0, %1, %2" : "=v"(r) : "i"(imm), "v"(base)); return (LAS char*)(size_t)r;
        };
        auto rdB = [&](int sl, int nf, int t1) -> s16x4 {
            if (nf < 8) return tr_read(xr(pb0, (nf >> 1) << 5) + sl * 16384 + (nf & 1) * 2048 + t1 * 512);
            if (nf == 8) return tr_read(xr(pe0, t1 * 128) + sl * 2048);
            return tr_read(xr(pq0, (nf - 9) << 5) + sl * 4096 + t1 * 512);
        };
        auto rdA = [&](int sl, int m) -> bf16x8 { return row_frag((LAS char*)(size_t)pa + sl * 16384 + m * 1024); };
        auto rdH = [&](int sl) -> bf16x8 {
            unsigned r; asm volatile("v_add_u32 %0, %1, %2" : "=v"(r) : "s"(hdelta), "v"(pa)); return row_frag((LAS char*)(size_t)r + sl * 1024);
        };
#pragma unroll
        for (int n = 0; n < 11; ++n)
#pragma unroll
            for (int m = 0; m < 4; ++m) { acc[n][m] = (f32x4){0.f, 0.f, 0.f, 0.f}; asm volatile("" : "+v"(acc[n][m])); }
        ach = (f32x4){0.f, 0.f, 0.f, 0.f}; asm volatile("" : "+v"(ach));

        wait_vm<14>(); BAR();
        STAMP(1, 1);
        bf16x8 fa[2][2], fc[2], fh, fb[NB];
#pragma unroll
        for (int m = 0; m < 2; ++m) { fa[0][m] = rdA(0, m); fc[m] = rdA(0, 2 + m); }
#pragma unroll
        for (int i = 0; i < PF; ++i) { s16x4 lo = rdB(0, i, 0), hi = rdB(0, i, 1); fb[i] = __builtin_shufflevector(lo, hi, 0, 1, 2, 3, 4, 5, 6, 7); }
#define MM(C, A, B) mfma_ip(C, A, B)
        auto step = [&](auto S_, int t, bool do_issue, auto LAST1_, auto WAIT0_) {
            constexpr int S = decltype(S_)::value;
            constexpr bool has_next = !decltype(LAST1_)::value;
            constexpr bool wait0 = decltype(WAIT0_)::value;
            constexpr int sl = S, sn = (S + 1) & 3, cur = S & 1;
#pragma unroll
            for (int nf = 0; nf < 11; ++nf) {
                constexpr int dummy = 0; (void)dummy;
                const int r = 11 * S + nf;
                const int psl = (nf + PF < 11) ? sl : sn, nn = (nf + PF < 11) ? nf + PF : nf + PF - 11;
                const bool pf = (nf + PF < 11) || has_next;
                const bool lastg = (nf == 10);
                s16x4 lo, hi;
                if (!lastg) MM(acc[nf][0], fb[r % NB], fa[cur][0]); else MM(acc[nf][2], fb[r % NB], fc[0]);
                SCHED();
                if (nf == 0) {
                    if (!wait0) wait_vm<7>(); else wait_vm<0>();
                    BAR();
                }
                if (pf) lo = rdB(psl, nn, 0);
                SCHED();
                if (!lastg) MM(acc[nf][1], fb[r % NB], fa[cur][1]); else MM(acc[nf][3], fb[r % NB], fc[1]);
                SCHED();
                if (pf) hi = rdB(psl, nn, 1);
                if (lastg && has_next) fc[0] = rdA(sn, 2);
                SCHED();
                if (!lastg) MM(acc[nf][2], fb[r % NB], fc[0]); else MM(acc[nf][0], fb[r % NB], fa[cur][0]);
                SCHED();
                if (lastg && has_next) fc[1] = rdA(sn, 3);
                if (nf >= 4 && nf < 6 && has_next) fa[cur ^ 1][nf - 4] = rdA(sn, nf - 4);
                if (nf == 7) fh = rdH(sl);
                if ((nf < 4 || (nf >= 6 && nf < 9)) && do_issue) issue1(t + 3, (S + 3) & 3, nf < 4 ? nf : nf - 2);
                SCHED();
                if (!lastg) MM(acc[nf][3], fb[r % NB], fc[1]); else MM(acc[nf][1], fb[r % NB], fa[cur][1]);
                SCHED();
                if (nf == 9) { MM(ach, fb[r % NB], fh); SCHED(); }
                if (pf) fb[(r + PF) % NB] = __builtin_shufflevector(lo, hi, 0, 1, 2, 3, 4, 5, 6, 7);
            }
        };
        using I0 = std::integral_constant<int, 0>; using I1 = std::integral_constant<int, 1>;
        using I2 = std::integral_constant<int, 2>; using I3 = std::integral_constant<int, 3>;
        using F = std::false_type; using T = std::true_type;
#pragma unroll 1
        for (int t0 = 0; t0 < 16; t0 += 4) {
            step(I0{}, t0, true, F{}, F{});
            step(I1{}, t0 + 1, true, F{}, F{});
            step(I2{}, t0 + 2, true, F{}, F{});
            step(I3{}, t0 + 3, t0 + 3 < 15, F{}, F{});
        }
        step(I0{}, 16, false, F{}, T{});
        step(I1{}, 17, false, T{}, T{});
    }
    asm volatile("s_nop 7\n\ts_nop 7" ::: "memory");
    BAR();
    STAMP(1, 2);
    k1_epilogue(acc, ach, lds, lds0, w, xcd, idx, j, mt, Wd, wo, hb);
}

constexpr int K2_NT = 21;
constexpr int K2_RB = 0, K2_RE = 65536, K2_LDS = 81920;
constexpr int NSPLIT = 4;
__device__ __forceinline__ void gload2(bf16x8& d0, bf16x8& d1, const void* sbase, unsigned voff) {
    sbase = uni(sbase);
    asm volatile("s_nop 4\n\tglobal_load_dwordx4 %0, %2, %3\n\tglobal_load_dwordx4 %1, %2, %3 offset:1024"
                 : "=&v"(d0), "=&v"(d1) : "v"(voff), "s"(sbase) : "memory");
}
__global__ __launch_bounds__(512, 2) void k2_down(const u16* __restrict__ hb, const u16* __restrict__ Wd,
                                                  const u16* __restrict__ wo, const u16* __restrict__ dx, u16* __restrict__ part, unsigned* __restrict__ cnt,
                                                  const u16* __restrict__ x, u16* __restrict__ out) {
    extern __shared__ __attribute__((aligned(16))) char smem[];
    LAS char* lds = (LAS char*)smem;
    const int tid = threadIdx.x, lane = tid & 63;
    const int w = __builtin_amdgcn_readfirstlane(tid >> 6);
    const int b = blockIdx.x, xcd = b & 7, idx = b >> 3;
    const int split = xcd >> 1, mt = (xcd & 1) * 8 + (idx >> 2), nt = idx & 3;
    const int wm = w >> 1, kh = w & 1;
    const int T0 = split == 0 ? 0 : 21 + 20 * (split - 1);

    const unsigned srcA = (unsigned)(kh * (int)BLK_ELEMS * 2 + blk_off(128 * mt + 32 * wm + (lane & 15), lane >> 4));
    const int koct = w >> 1, sub = w & 1;
    unsigned offB, offE;
    {
        const int r = lane >> 3, swz = ((r >> 1) & 1) | ((koct & 1) << 1), c = (lane & 7) ^ (swz << 1);
        offB = (unsigned)((32 * sub + 8 * koct + r) * DM + 128 * nt + 8 * c) * 2u;
        const int re = ((lane >> 1) & 7) ^ ((koct & 1) << 2), ce = (lane & 1);
        offE = (unsigned)((nt * 5184 + 32 * sub + 8 * koct + re) * 16 + 8 * ce) * 2u;
    }
    const unsigned lds0 = (unsigned)(size_t)lds;
    const unsigned dB = lds0 + K2_RB + (2 * w) * 1024, dE = lds0 + K2_RE + (sub * 4 + koct) * 256;
    bf16x8 fa[4][2];
    auto issue1 = [&](int t, int sl, int i, bf16x8& a0, bf16x8& a1) {
        int T = T0 + t; if (t == K2_NT - 1 && split != 0) T -= 1;
        const u16* Wt = T < 72 ? Wd + (size_t)T * 64 * DM : wo + (size_t)(T - 72) * 64 * DM;
        const u16* ba = hb + (size_t)T * 2 * BLK_ELEMS;
        if (i == 0) glds16(Wt, offB, dB + sl * 16384);
        if (i == 1) glds16(Wt + 64, offB, dB + sl * 16384 + 1024);
        if (i == 2) glds16_q(dx + (size_t)T * 64 * 16, offE, dE + sl * 2048);
        if (i == 3) gload2(a0, a1, ba, srcA);
    };
    STAMP(2, 0);
#pragma unroll
    for (int t = 0; t < 3; ++t)
#pragma unroll
        for (int i = 0; i < 4; ++i) issue1(t, t, i, fa[t][0], fa[t][1]);

    const int fr = lane & 15, g = lane >> 4, q4 = (lane >> 2) & 3, p4 = lane & 3;
    const int swz = ((q4 >> 1) & 1) | ((g & 1) << 1);
    LAS char* pb[4];
#pragma unroll
    for (int u = 0; u < 4; ++u) pb[u] = lds + K2_RB + (g * 4 + kh * 2) * 1024 + q4 * 128 + ((u ^ swz) << 5) + p4 * 8;
    LAS char* pe0 = lds + K2_RE + (kh * 4 + g) * 256 + ((q4 ^ ((g & 1) << 2)) * 32) + p4 * 8;
    LAS char* pe1 = lds + K2_RE + (kh * 4 + g) * 256 + (((4 + q4) ^ ((g & 1) << 2)) * 32) + p4 * 8;
    auto rdB = [&](int sl, int nf, int t1) -> s16x4 {
        if (nf < 8) return tr_read(pb[nf & 3] + sl * 16384 + (nf >> 2) * 1024 + t1 * 512);
        return tr_read((t1 ? pe1 : pe0) + sl * 2048);
    };

    f32x4 acc[9][2];
#pragma unroll
    for (int n = 0; n < 9; ++n)
#pragma unroll
        for (int m = 0; m < 2; ++m) { acc[n][m] = (f32x4){0.f, 0.f, 0.f, 0.f}; asm volatile("" : "+v"(acc[n][m])); }

    wait_vm<10>(); BAR();
    STAMP(2, 1);
    constexpr int PF2 = 4;
    bf16x8 fb[9];
#pragma unroll
    for (int i = 0; i < PF2; ++i) { s16x4 lo = rdB(0, i, 0), hi = rdB(0, i, 1); fb[i] = __builtin_shufflevector(lo, hi, 0, 1, 2, 3, 4, 5, 6, 7); }
    auto step = [&](auto S_, int t, bool do_issue, bool wait0, auto LAST_) {
        constexpr int S = decltype(S_)::value;
        constexpr bool has_next = !decltype(LAST_)::value;
        constexpr int sl = S, sn = (S + 1) & 3;
        asm volatile("" : "+v"(fa[sl][0]), "+v"(fa[sl][1]));
        if (!has_next && split != 0) {
#pragma unroll
            for (int m = 0; m < 2; ++m) fa[sl][m] = (bf16x8){0, 0, 0, 0, 0, 0, 0, 0};
            asm volatile("s_nop 1" : "+v"(fa[sl][0]), "+v"(fa[sl][1]));
        }
#pragma unroll
        for (int nf = 0; nf < 9; ++nf) {
            const int psl = (nf + PF2 < 9) ? sl : sn, nn = (nf + PF2 < 9) ? nf + PF2 : nf + PF2 - 9;
            const bool pf = (nf + PF2 < 9) || has_next;
            s16x4 lo, hi;
            mfma_ip(acc[nf][0], fb[nf], fa[sl][0]);
            SCHED();
            if (nf == 0) {
                if (!wait0) wait_vm<5>(); else wait_vm<0>();
                BAR();
            }
            if (pf) lo = rdB(psl, nn, 0);
            SCHED();
            mfma_ip(acc[nf][1], fb[nf], fa[sl][1]);
            SCHED();
            if (pf) hi = rdB(psl, nn, 1);
            if (nf >= 1 && nf < 5 && do_issue) issue1(t + 3, (S + 3) & 3, nf - 1, fa[(S + 3) & 3][0], fa[(S + 3) & 3][1]);
            SCHED();
            if (pf) fb[nn] = __builtin_shufflevector(lo, hi, 0, 1, 2, 3, 4, 5, 6, 7);
        }
    };
    using I0 = std::integral_constant<int, 0>; using I1 = std::integral_constant<int, 1>;
    using I2 = std::integral_constant<int, 2>; using I3 = std::integral_constant<int, 3>;
#pragma unroll 1
    for (int t0 = 0; t0 < 20; t0 += 4) {
        step(I0{}, t0, true, false, std::false_type{});
        step(I1{}, t0 + 1, true, false, std::false_type{});
        step(I2{}, t0 + 2, t0 + 2 < 18, false, std::false_type{});
        step(I3{}, t0 + 3, t0 + 3 < 18, t0 + 3 == 19, std::false_type{});
    }
    step(I0{}, 20, false, true, std::true_type{});
    asm volatile("s_nop 7\n\ts_nop 7" ::: "memory");
    BAR();
    STAMP(2, 2);
    f32x4 val[10];
    {
        LAS char* xw = lds + w * 10240;
        LAS char* xr = lds + (w ^ 1) * 10240;
#pragma unroll
        for (int n = 0; n < 9; ++n) {
            const bool give = kh ? (n < 5) : (n >= 5);
            if (give) {
#pragma unroll
                for (int m = 0; m < 2; ++m) *(LAS f32x4*)(xw + ((kh ? n : n - 5) * 2 + m) * 1024 + lane * 16) = acc[n][m];
            }
        }
        asm volatile("s_waitcnt lgkmcnt(0)" ::: "memory");
        BAR();
#pragma unroll
        for (int i = 0; i < 10; ++i) {
            const f32x4 mine = kh ? acc[(5 + (i >> 1)) < 9 ? 5 + (i >> 1) : 8][i & 1] : acc[i >> 1][i & 1];
            val[i] = mine + *(LAS f32x4*)(xr + i * 1024 + lane * 16);
        }
    }
    {
        const int nit = kh ? 8 : 10;
        const int grp = mt * 4 + nt;
        const __amdgpu_buffer_rsrc_t rp = __builtin_amdgcn_make_buffer_rsrc((void*)part, 0, NSPLIT * 64 * 8 * 10 * 512, 0x00020000);
        const unsigned slot = (unsigned)((grp * 8 + w) * 10 * 512 + lane * 8);
        constexpr unsigned SPL = 64 * 8 * 10 * 512;
        u32x2 xv[3]; unsigned xoff[3];
#pragma unroll
        for (int q = 0; q < 3; ++q) {
            int i = split + 4 * q; i = i < nit ? i : split;
            const int n = kh ? 5 + (i >> 1) : (i >> 1), m = i & 1;
            const int row = 128 * mt + 32 * wm + 16 * m + fr;
            const int col = (n < 8 ? 128 * nt + 16 * n : 512 + 16 * nt) + 4 * g;
            xoff[q] = (unsigned)(row * DM + col);
            xv[q] = *(const u32x2*)(x + xoff[q]);
        }
#pragma unroll
        for (int i = 0; i < 10; ++i) {
            if (i < nit && (i & 3) != split) {
                u32x2 v; v[0] = pk_bf16(val[i][0], val[i][1]); v[1] = pk_bf16(val[i][2], val[i][3]);
                __builtin_amdgcn_raw_buffer_store_b64(v, rp, slot + split * SPL + i * 512, 0, 16);
            }
        }
        STAMP(2, 3);
        asm volatile("s_waitcnt vmcnt(0)" ::: "memory");
        BAR();
        STAMP(2, 4);
        unsigned* c = cnt + grp * 64;
        if (tid == 0) {
            __hip_atomic_fetch_add(c, 1u, __ATOMIC_RELAXED, __HIP_MEMORY_SCOPE_AGENT);
            unsigned spins = 0;
            while (__hip_atomic_load(c, __ATOMIC_RELAXED, __HIP_MEMORY_SCOPE_AGENT) < (unsigned)NSPLIT) {
                __builtin_amdgcn_s_sleep(1);
                if (++spins > (1u << 22)) break;
            }
        }
        BAR();
        STAMP(2, 5);
        u32x2 pv[3][3];
#pragma unroll
        for (int q = 0; q < 3; ++q) {
            const int i = split + 4 * q;
#pragma unroll
            for (int s2 = 1; s2 < NSPLIT; ++s2) {
                const int so = (split + s2) & 3;
                const unsigned off = i < nit ? slot + so * SPL + i * 512 : 0xfffffff0u;
                pv[q][s2 - 1] = __builtin_amdgcn_raw_buffer_load_b64(rp, off, 0, 16);
            }
        }
#pragma unroll
        for (int q = 0; q < 3; ++q) {
            const int i = split + 4 * q;
            if (i < nit) {
                f32x4 o = val[0];
#pragma unroll
                for (int ii = 0; ii < 10; ++ii) if (ii == i) o = val[ii];
#pragma unroll
                for (int s2 = 0; s2 < 3; ++s2) { o[0] += bf_lo(pv[q][s2][0]); o[1] += bf_hi(pv[q][s2][0]); o[2] += bf_lo(pv[q][s2][1]); o[3] += bf_hi(pv[q][s2][1]); }
                u32x2 v; v[0] = pk_bf16(o[0] + bf_lo(xv[q][0]), o[1] + bf_hi(xv[q][0])); v[1] = pk_bf16(o[2] + bf_lo(xv[q][1]), o[3] + bf_hi(xv[q][1]));
                *(u32x2*)(out + xoff[q]) = v;
            }
        }
    }
    STAMP(2, 15);
}

extern "C" void kernel_launch(void* const* d_in, const int* in_sizes, int n_in, void* d_out, int out_size, void* d_ws,
                              size_t ws_size, hipStream_t stream) {
    const u16* x = (const u16*)d_in[0];
    const u16* wqkv = (const u16*)d_in[1];
    const u16* wo = (const u16*)d_in[2];
    const u16* Wg = (const u16*)d_in[4];
    const u16* Wu = (const u16*)d_in[5];
    const u16* Wd = (const u16*)d_in[6];
    char* ws = (char*)d_ws;
    u16* xnb = (u16*)ws;
    u16* hb = (u16*)(ws + (4u << 20));
    u16* part = (u16*)(ws + (32u << 20));
    unsigned* cnt = (unsigned*)(ws + (56u << 20));
    u16* gx = (u16*)(ws + (52u << 20));
    u16* dx = (u16*)(ws + (54u << 20));
    u16* wqh = (u16*)(ws + (48u << 20));
    (void)hipFuncSetAttribute((const void*)k1_gateup, hipFuncAttributeMaxDynamicSharedMemorySize, K1_LDS);
    (void)hipFuncSetAttribute((const void*)k2_down, hipFuncAttributeMaxDynamicSharedMemorySize, K2_LDS);
    k0_ln<<<256, 256, 0, stream>>>(x, xnb, wqkv, wqh, Wg, Wu, gx, Wd, wo, dx);
    k1_gateup<<<256, 512, K1_LDS, stream>>>(xnb, Wg, Wu, wqh, gx, Wd, wo, hb, cnt);
    k2_down<<<256, 512, K2_LDS, stream>>>(hb, Wd, wo, dx, part, cnt, x, (u16*)d_out);
}
```

```cpp
#include <hip/hip_runtime.h>
#include <hip/hip_bf16.h>
#include <stdint.h>
#include <type_traits>

typedef unsigned short u16;
typedef short bf16x8 __attribute__((ext_vector_type(8)));
typedef short s16x4 __attribute__((ext_vector_type(4)));
typedef float f32x4 __attribute__((ext_vector_type(4)));
typedef float f32x2 __attribute__((ext_vector_type(2)));
typedef __bf16 bf16x2_t __attribute__((ext_vector_type(2)));
typedef unsigned u32x4 __attribute__((ext_vector_type(4)));
typedef unsigned u32x2 __attribute__((ext_vector_type(2)));
#define LAS __attribute__((address_space(3)))
#define GAS __attribute__((address_space(1)))

constexpr int SEQ = 2048, DM = 576, FF = 4608;
constexpr int NKB_X = 18;
constexpr int NKB_H = 162;
constexpr size_t BLK_ELEMS = (size_t)SEQ * 32;

__device__ __forceinline__ unsigned pk_bf16(float lo, float hi) {
    f32x2 v = {lo, hi};
    bf16x2_t b = __builtin_convertvector(v, bf16x2_t);
    return __builtin_bit_cast(unsigned, b);
}
__device__ __forceinline__ float bf_lo(unsigned u) { return __uint_as_float(u << 16); }
__device__ __forceinline__ float bf_hi(unsigned u) { return __uint_as_float(u & 0xffff0000u); }
__device__ __forceinline__ float rbf(float x) { return bf_lo(pk_bf16(x, x)); }

__device__ __forceinline__ const void* uni(const void* p) {
    const unsigned long long v = (unsigned long long)p;
    const unsigned lo = __builtin_amdgcn_readfirstlane((unsigned)v), hi = __builtin_amdgcn_readfirstlane((unsigned)(v >> 32));
    return (const void*)(((unsigned long long)hi << 32) | lo);
}
__device__ __forceinline__ void glds16(const void* sbase, unsigned voff, unsigned lds) {
    sbase = uni(sbase); lds = __builtin_amdgcn_readfirstlane(lds);
    asm volatile("s_mov_b32 m0, %2\n\ts_nop 3\n\tglobal_load_lds_dwordx4 %0, %1"
                 :: "v"(voff), "s"(sbase), "s"(lds) : "memory", "m0");
}
__device__ __forceinline__ void glds16_o1k(const void* sbase, unsigned voff, unsigned lds) {
    sbase = uni(sbase); lds = __builtin_amdgcn_readfirstlane(lds);
    asm volatile("s_mov_b32 m0, %2\n\ts_nop 3\n\tglobal_load_lds_dwordx4 %0, %1 offset:1024"
                 :: "v"(voff), "s"(sbase), "s"(lds) : "memory", "m0");
}
__device__ __forceinline__ void glds16_q(const void* sbase, unsigned voff, unsigned lds) {
    sbase = uni(sbase); lds = __builtin_amdgcn_readfirstlane(lds);
    unsigned sv; unsigned long long ex;
    asm volatile("s_mov_b32 %0, m0\n\ts_mov_b64 %1, exec\n\ts_mov_b32 m0, %4\n\ts_mov_b64 exec, 0xffff\n\ts_nop 1\n\tglobal_load_lds_dwordx4 %2, %3\n\ts_mov_b64 exec, %1\n\ts_mov_b32 m0, %0"
                 : "=&s"(sv), "=&s"(ex) : "v"(voff), "s"(sbase), "s"(lds) : "memory");
}
__device__ __forceinline__ void glds16_m(const void* sbase, unsigned voff, unsigned lds) {
    sbase = uni(sbase); lds = __builtin_amdgcn_readfirstlane(lds);
    unsigned sv; unsigned long long ex;
    asm volatile("s_mov_b32 %0, m0\n\ts_mov_b64 %1, exec\n\ts_mov_b32 m0, %4\n\ts_mov_b32 exec_lo, -1\n\ts_mov_b32 exec_hi, 0xff\n\ts_nop 1\n\tglobal_load_lds_dwordx4 %2, %3\n\ts_mov_b64 exec, %1\n\ts_mov_b32 m0, %0"
                 : "=&s"(sv), "=&s"(ex) : "v"(voff), "s"(sbase), "s"(lds) : "memory");
}
#define SCHED() __builtin_amdgcn_sched_barrier(0)
#define STAMP(kid, i) do {} while (0)
#define WSTAMP(i) do {} while (0)
__device__ __forceinline__ s16x4 tr_read(LAS char* p) {
    return __builtin_bit_cast(s16x4, __builtin_amdgcn_ds_read_tr16_b64_v4i16((LAS s16x4*)p));
}
__device__ __forceinline__ bf16x8 tr_frag(LAS char* p, int off_t1) {
    s16x4 lo = tr_read(p), hi = tr_read(p + off_t1);
    return __builtin_shufflevector(lo, hi, 0, 1, 2, 3, 4, 5, 6, 7);
}
__device__ __forceinline__ bf16x8 row_frag(LAS char* p) { return *(LAS bf16x8*)p; }
#define MFMA16(a, b, c) __builtin_amdgcn_mfma_f32_16x16x32_bf16((a), (b), (c), 0, 0, 0)
__device__ __forceinline__ void mfma_ip(f32x4& c, bf16x8 a, bf16x8 b) { asm volatile("v_mfma_f32_16x16x32_bf16 %0, %1, %2, %0" : "+v"(c) : "v"(a), "v"(b)); }
#define WAIT_V(n) asm volatile("s_waitcnt vmcnt(" #n ")" ::: "memory")
#define BAR() do { asm volatile("" ::: "memory"); __builtin_amdgcn_s_barrier(); asm volatile("" ::: "memory"); } while (0)

__device__ __forceinline__ int blk_off(int row, int chunk) { return row * 64 + ((chunk ^ (((row >> 3) & 1) << 1)) << 4); }

__global__ __launch_bounds__(256) void k0_ln(const u16* __restrict__ x, u16* __restrict__ xnb,
                                            const u16* __restrict__ wqkv, u16* __restrict__ wqh,
                                            const u16* __restrict__ Wg, const u16* __restrict__ Wu, u16* __restrict__ gx,
                                            const u16* __restrict__ Wd, const u16* __restrict__ wo, u16* __restrict__ dx) {
    if (threadIdx.x >= 64) {
        const int l = threadIdx.x - 64, head = blockIdx.x >> 3, k0 = (blockIdx.x & 7) * 72;
        unsigned v[12];
#pragma unroll
        for (int i = 0; i < 12; ++i) {
            const int id = l + 192 * i, k = k0 + (id >> 5), cp = id & 31;
            const int part = cp >= 18 ? 2 : (cp >= 9 ? 1 : 0), d2 = cp - part * 9;
            const int src = (part == 2 ? 0 : part + 1) * DM + 18 * head + 2 * d2;
            v[i] = cp < 27 ? *(const unsigned*)(wqkv + (size_t)k * (3 * DM) + src) : 0u;
        }
        u32x4 c[3];
#pragma unroll
        for (int i = 0; i < 2; ++i) {
            const int id = l + 192 * i;
            if (id < 288) {
                const int gid = blockIdx.x * 288 + id, mat = gid / 36864, r = gid - mat * 36864, k = r >> 6, ch = r & 63;
                c[i] = *(const u32x4*)((mat ? Wu : Wg) + (size_t)k * FF + 4096 + 8 * ch);
            }
        }
        if (l < 162) {
            const int gid = blockIdx.x * 162 + l, k = gid >> 3, ch = gid & 7;
            c[2] = *(const u32x4*)((k < FF ? Wd + (size_t)k * DM : wo + (size_t)(k - FF) * DM) + 512 + 8 * ch);
        }
#pragma unroll
        for (int i = 0; i < 12; ++i) {
            const int id = l + 192 * i;
            *(unsigned*)(wqh + ((size_t)head * 576 + k0) * 64 + 2 * id) = v[i];
        }
#pragma unroll
        for (int i = 0; i < 2; ++i) {
            const int id = l + 192 * i;
            if (id < 288) {
                const int gid = blockIdx.x * 288 + id, mat = gid / 36864, r = gid - mat * 36864, k = r >> 6, ch = r & 63;
                *(u32x4*)(gx + (((size_t)mat * 32 + (ch >> 1)) * 576 + k) * 16 + 8 * (ch & 1)) = c[i];
            }
        }
        if (l < 162) {
            const int gid = blockIdx.x * 162 + l, k = gid >> 3, ch = gid & 7;
            *(u32x4*)(dx + ((size_t)(ch >> 1) * 5184 + k) * 16 + 8 * (ch & 1)) = c[2];
        }
        return;
    }
    const int lane = threadIdx.x, i = lane & 7, row = blockIdx.x * 8 + (lane >> 3);
    u32x4 xv[9];
#pragma unroll
    for (int cc = 0; cc < 9; ++cc) xv[cc] = *(const u32x4*)(x + (size_t)row * DM + (i + 8 * cc) * 8);
    float s = 0.f;
#pragma unroll
    for (int cc = 0; cc < 9; ++cc)
#pragma unroll
        for (int e = 0; e < 4; ++e) s += bf_lo(xv[cc][e]) + bf_hi(xv[cc][e]);
    s += __shfl_xor(s, 1); s += __shfl_xor(s, 2); s += __shfl_xor(s, 4);
    const float mean = s * (1.0f / 576.0f);
    float q = 0.f;
#pragma unroll
    for (int cc = 0; cc < 9; ++cc)
#pragma unroll
        for (int e = 0; e < 4; ++e) { const float a = bf_lo(xv[cc][e]) - mean, c2 = bf_hi(xv[cc][e]) - mean; q += a * a + c2 * c2; }
    q += __shfl_xor(q, 1); q += __shfl_xor(q, 2); q += __shfl_xor(q, 4);
    const float mb = rbf(mean), vb = rbf(q * (1.0f / 576.0f));
    const float sb = rbf(sqrtf(rbf(vb + 1e-5f)));
    const float rs = 1.0f / sb;
#pragma unroll
    for (int cc = 0; cc < 9; ++cc) {
        u32x4 o;
#pragma unroll
        for (int e = 0; e < 4; ++e) {
            const unsigned d = pk_bf16(bf_lo(xv[cc][e]) - mb, bf_hi(xv[cc][e]) - mb);
            o[e] = pk_bf16(bf_lo(d) * rs, bf_hi(d) * rs);
        }
        const int blk = 2 * cc + (i >> 2);
        *(u32x4*)((char*)xnb + (size_t)blk * (BLK_ELEMS * 2) + blk_off(row, i & 3)) = o;
    }
}

constexpr int RA = 0, RB = 65536, RE = 131072, RQ = 139264, RH = 155648;
constexpr int T_DUMMY = 159744;
constexpr int K1_LDS = T_DUMMY + 1024;
constexpr int QS = 144;
#define HST_AUX 16
#define PF 3
constexpr int NB = PF + 1;
template <int N> __device__ __forceinline__ void wait_vm() { asm volatile("s_waitcnt vmcnt(%0)" :: "i"(N) : "memory"); }
#define GLDS_MASKED(NAME, LO, HI) \
__device__ __forceinline__ void NAME(const void* sbase, unsigned voff, unsigned lds) { \
    sbase = uni(sbase); lds = __builtin_amdgcn_readfirstlane(lds); \
    unsigned sv; unsigned long long ex; \
    asm volatile("s_mov_b32 %0, m0\n\ts_mov_b64 %1, exec\n\ts_mov_b32 m0, %4\n\ts_mov_b32 exec_lo, " #LO "\n\ts_mov_b32 exec_hi, " #HI "\n\tglobal_load_lds_dwordx4 %2, %3\n\ts_mov_b64 exec, %1\n\ts_mov_b32 m0, %0" \
                 : "=&s"(sv), "=&s"(ex) : "v"(voff), "s"(sbase), "s"(lds) : "memory"); }
GLDS_MASKED(glds16_32, -1, 0)
GLDS_MASKED(glds16_8, 0xff, 0)

__device__ __forceinline__ void k1_epilogue(f32x4 (&acc)[11][4], f32x4& ach, LAS char* lds, unsigned lds0, int w, int xcd, int idx,
                                            int j, int mt, const u16* __restrict__ Wd, const u16* __restrict__ wo, u16* __restrict__ hb) {
    int tid = threadIdx.x; asm volatile("" : "+v"(tid));
    const int lane = tid & 63, fr = lane & 15, g = lane >> 4;
    const int wm = w >> 1, wn = w & 1;
    {
        const int split = xcd >> 1;
        const unsigned start = (unsigned)((split == 0 ? 0 : 1344 + 1280 * (split - 1)) * DM * 2);
        const unsigned len = (unsigned)((split == 0 ? 1344 : 1280) * DM * 2), lwd = (unsigned)(704 * DM * 2);
#pragma unroll
        for (int i = 0; i < 6; ++i) {
            unsigned o = (unsigned)((idx * 8 + w) * 6144 + i * 1024); o = o + 1024 <= len ? o : 0u;
            const char* src = (split == 3 && o >= lwd) ? (const char*)wo + (o - lwd) : (const char*)Wd + start + o;
            glds16(src, (unsigned)lane * 16u, lds0 + T_DUMMY);
        }
    }
    LAS char* qt = lds;
    {
#pragma unroll
        for (int u = 0; u < 2; ++u) {
            const int nn = 16 * (2 * wn + (u ^ (wm & 1))) + 4 * g;
            const int h0 = nn / 18, h1 = (nn + 2) / 18;
            const int o0 = h0 * 48 + (nn - h0 * 18) * 2, o1 = h1 * 48 + (nn + 2 - h1 * 18) * 2;
#pragma unroll
            for (int m = 0; m < 4; ++m) {
                LAS char* rp = qt + (16 + 64 * wm + 16 * m + fr) * QS;
                if (h0 < 3) *(LAS unsigned*)(rp + o0) = pk_bf16(acc[9 + u][m][0], acc[9 + u][m][1]);
                if (h1 < 3) *(LAS unsigned*)(rp + o1) = pk_bf16(acc[9 + u][m][2], acc[9 + u][m][3]);
            }
            if (u == 0 && (w == 0 || w == 2 || w == 1)) {
                LAS char* rp = qt + fr * QS;
                if (h0 < 3) *(LAS unsigned*)(rp + o0) = pk_bf16(ach[0], ach[1]);
                if (h1 < 3) *(LAS unsigned*)(rp + o1) = pk_bf16(ach[2], ach[3]);
            }
        }
    }
    asm volatile("s_waitcnt lgkmcnt(0)" ::: "memory");
    BAR();
    STAMP(1, 3);
    const __amdgpu_buffer_rsrc_t rh = __builtin_amdgcn_make_buffer_rsrc((void*)hb, 0, (int)(NKB_H * BLK_ELEMS * 2), 0x00020000);
    u32x4 hp[4][2]; u32x2 hm[4];
#pragma unroll
    for (int m = 0; m < 4; ++m) {
#pragma unroll
        for (int pp = 0; pp < 2; ++pp) {
            unsigned v[2][2];
#pragma unroll
            for (int hh = 0; hh < 2; ++hh) {
                const int gt = 2 * pp + hh;
                float hv[4];
#pragma unroll
                for (int e = 0; e < 4; ++e) hv[e] = fmaxf(acc[2 * gt][m][e], 0.f) * acc[2 * gt + 1][m][e];
                v[hh][0] = pk_bf16(hv[0], hv[1]); v[hh][1] = pk_bf16(hv[2], hv[3]);
            }
            auto r0 = __builtin_amdgcn_permlane16_swap(v[0][0], v[1][0], false, false);
            auto r1 = __builtin_amdgcn_permlane16_swap(v[0][1], v[1][1], false, false);
            hp[m][pp][0] = r0[0]; hp[m][pp][1] = r1[0]; hp[m][pp][2] = r0[1]; hp[m][pp][3] = r1[1];
        }
        {
            float hv[4];
#pragma unroll
            for (int e = 0; e < 4; ++e) {
                const unsigned mine = __float_as_uint(acc[8][m][e]);
                auto r = __builtin_amdgcn_permlane32_swap(mine, mine, false, false);
                hv[e] = fmaxf(__uint_as_float(r[0]), 0.f) * __uint_as_float(r[1]);
            }
            hm[m][0] = pk_bf16(hv[0], hv[1]); hm[m][1] = pk_bf16(hv[2], hv[3]);
        }
        asm volatile("" : "+v"(hp[m][0]), "+v"(hp[m][1]), "+v"(hm[m]));
    }
    SCHED();
    auto hstore = [&](int m) {
        const int row = 256 * mt + 64 * wm + 16 * m + fr;
#pragma unroll
        for (int pp = 0; pp < 2; ++pp) {
            const int blk = 4 * j + 2 * wn + pp, chunk = ((g & 1) << 1) | (g >> 1);
            __builtin_amdgcn_raw_buffer_store_b128(hp[m][pp], rh, (unsigned)(blk * (int)(BLK_ELEMS * 2) + blk_off(row, chunk)), 0, HST_AUX);
        }
        const int blk = 128 + (j >> 1), chunk = (j & 1) * 2 + wn;
        const unsigned off = g < 2 ? (unsigned)(blk * (int)(BLK_ELEMS * 2) + blk_off(row, chunk) + g * 8) : 0xfffffff0u;
        __builtin_amdgcn_raw_buffer_store_b64(hm[m], rh, off, 0, HST_AUX);
    };
    {
        const int s = tid >> 1, half = tid & 1;
        const int lrow = 16 + s, grow = 256 * mt + s;
        auto ld18 = [&](LAS const char* p, unsigned (&v)[9]) {
            const u32x4 a = *(LAS const u32x4*)p, c = *(LAS const u32x4*)(p + 16);
            v[0] = a[0]; v[1] = a[1]; v[2] = a[2]; v[3] = a[3]; v[4] = c[0]; v[5] = c[1]; v[6] = c[2]; v[7] = c[3];
            v[8] = *(LAS const unsigned*)(p + 32);
        };
        hstore(0);
        SCHED();
        unsigned qv[9];
        ld18(qt + lrow * QS + 96, qv);
        float lg[4];
#pragma unroll
        for (int ww = 0; ww < 4; ++ww) {
            const int wi = 4 * half + ww;
            unsigned kv[9];
            ld18(qt + (lrow - 2 * wi) * QS, kv);
            float a = 0.f;
#pragma unroll
            for (int i = 0; i < 9; ++i) a = __builtin_amdgcn_fdot2_f32_bf16(__builtin_bit_cast(bf16x2_t, qv[i]), __builtin_bit_cast(bf16x2_t, kv[i]), a, false);
            a = rbf(rbf(a) * 0.23570226039551584f);
            lg[ww] = (grow - 2 * wi >= 0) ? a : -1e9f;
        }
        SCHED();
        hstore(1);
        SCHED();
        float mx = fmaxf(fmaxf(lg[0], lg[1]), fmaxf(lg[2], lg[3]));
        mx = fmaxf(mx, __shfl_xor(mx, 1));
        float ex[4], sum = 0.f;
#pragma unroll
        for (int ww = 0; ww < 4; ++ww) { ex[ww] = __expf(lg[ww] - mx); sum += ex[ww]; }
        sum += __shfl_xor(sum, 1);
        const float inv = 1.0f / sum;
        float o[18];
#pragma unroll
        for (int i = 0; i < 18; ++i) o[i] = 0.f;
#pragma unroll
        for (int ww = 0; ww < 4; ++ww) {
            const int wi = 4 * half + ww;
            const float pw = (grow - 2 * wi >= 0) ? rbf(ex[ww] * inv) : 0.f;
            unsigned vv[9];
            ld18(qt + (lrow - 2 * wi) * QS + 48, vv);
#pragma unroll
            for (int i = 0; i < 9; ++i) { o[2 * i] += pw * bf_lo(vv[i]); o[2 * i + 1] += pw * bf_hi(vv[i]); }
        }
        SCHED();
        hstore(2);
        SCHED();
#pragma unroll
        for (int ii = 0; ii < 5; ++ii) {
            const float s0 = half ? o[2 * ii] : o[(10 + 2 * ii) % 18], s1 = half ? o[2 * ii + 1] : o[(11 + 2 * ii) % 18];
            const float r0 = __shfl_xor(s0, 1), r1 = __shfl_xor(s1, 1);
            const float m0 = half ? o[(10 + 2 * ii) % 18] : o[2 * ii], m1 = half ? o[(11 + 2 * ii) % 18] : o[2 * ii + 1];
            const int i = half * 5 + ii;
            if (i < 9) {
                const int col = FF + 18 * j + 2 * i;
                *(unsigned*)((char*)hb + (size_t)(col >> 5) * (BLK_ELEMS * 2) + blk_off(grow, (col >> 3) & 3) + (col & 7) * 2) = pk_bf16(m0 + r0, m1 + r1);
            }
        }
        SCHED();
        hstore(3);
    }
    STAMP(1, 15);
}

__global__ __launch_bounds__(512, 2) void k1_gateup(const u16* __restrict__ xnb, const u16* __restrict__ Wg,
                                                    const u16* __restrict__ Wu, const u16* __restrict__ wqh, const u16* __restrict__ gx,
                                                    const u16* __restrict__ Wd, const u16* __restrict__ wo, u16* __restrict__ hb, unsigned* __restrict__ cnt) {
    extern __shared__ __attribute__((aligned(16))) char smem[];
    LAS char* lds = (LAS char*)smem;
    const int tid = threadIdx.x, lane = tid & 63;
    if (blockIdx.x < 64 && tid == 0) cnt[blockIdx.x * 64] = 0u;
    const int w = __builtin_amdgcn_readfirstlane(tid >> 6);
    const int b = blockIdx.x, xcd = b & 7, idx = b >> 3;
    const int j = 4 * xcd + (idx & 3), mt = idx >> 2;
    const int wm = w >> 1, wn = w & 1;
    const unsigned lds0 = (unsigned)(size_t)lds;
    const int fr = lane & 15, g = lane >> 4, q4 = (lane >> 2) & 3, p4 = lane & 3;

    f32x4 acc[11][4], ach;
    {
        const unsigned srcA = (unsigned)((256 * mt + 32 * w) * 32 + lane * 8) * 2u;
        const int koct = w >> 1, mat = w & 1;
        const u16* W = mat ? Wu : Wg;
        unsigned srcB, srcX;
        const u16* baseX; int strideX; unsigned dX;
        {
            const int r = lane >> 3, swz = ((r >> 1) & 1) | ((koct & 1) << 1), c = (lane & 7) ^ (swz << 1);
            srcB = (unsigned)((8 * koct + r) * FF + 128 * j + 8 * c) * 2u;
            if (w < 2) {
                const int ko = lane >> 4, re = ((lane >> 1) & 7) ^ ((ko & 1) << 2), ce = (lane & 1) ^ w;
                srcX = (unsigned)(((w * 32 + j) * 576 + 8 * ko + re) * 16 + 8 * ce) * 2u;
                baseX = gx; strideX = 32 * 16; dX = lds0 + RE + w * 1024;
            } else if (w < 6) {
                const int ko = w - 2, swq = ((r >> 1) & 1) | ((ko & 1) << 1), cq = (lane & 7) ^ (swq << 1);
                srcX = (unsigned)((j * 576 + 8 * ko + r) * 64 + 8 * cq) * 2u;
                baseX = wqh; strideX = 32 * 64; dX = lds0 + RQ + ko * 1024;
            } else {
                const int hr = mt > 0 ? 256 * mt - 16 : 0;
                srcX = (unsigned)(hr * 64 + lane * 16);
                baseX = xnb; strideX = (int)BLK_ELEMS; dX = lds0 + RH;
            }
        }
        const int slotX = w < 2 ? 2048 : (w < 6 ? 4096 : 1024);
        const unsigned dA = lds0 + RA + (2 * w) * 1024, dB = lds0 + RB + (2 * w) * 1024;
        auto issue1 = [&](int t, int sl, int i) {
            const u16* ba = xnb + (size_t)t * BLK_ELEMS;
            const u16* bw = W + (size_t)t * 32 * FF;
            if (i == 0) glds16(bw, srcB, dB + sl * 16384);
            if (i == 1) glds16(bw + 64, srcB, dB + sl * 16384 + 1024);
            if (i == 2) glds16(baseX + (size_t)t * strideX, srcX, dX + sl * slotX);
            if (i == 3) glds16(ba, srcA, dA + sl * 16384);
            if (i == 4) glds16_o1k(ba, srcA, dA + sl * 16384);
        };
        STAMP(1, 0);
#pragma unroll
        for (int t = 0; t < 3; ++t)
#pragma unroll
            for (int i = 0; i < 5; ++i) issue1(t, t, i);

        const unsigned pa = lds0 + RA + blk_off(64 * wm + fr, g);
        const int swz = ((q4 >> 1) & 1) | ((g & 1) << 1);
        const unsigned pb0 = lds0 + RB + (g * 4 + wn) * 1024 + q4 * 128 + (swz << 5) + p4 * 8;
        const int emat = p4 >> 1;
        const unsigned pe0 = lds0 + RE + (emat * 4 + g) * 256 + ((q4 ^ ((g & 1) << 2)) * 32) + ((wn ^ emat) * 16) + (p4 & 1) * 8;
        const unsigned pq0 = lds0 + RQ + g * 1024 + q4 * 128 + (((2 * wn + (wm & 1)) ^ swz) << 5) + p4 * 8;
        LAS char* pbv[4]; LAS char* pqv[2];
#pragma unroll
        for (int u = 0; u < 4; ++u) pbv[u] = (LAS char*)(size_t)(pb0 ^ (u << 5));
        pqv[0] = (LAS char*)(size_t)pq0; pqv[1] = (LAS char*)(size_t)(pq0 ^ 32);
        LAS char* pev[2] = {(LAS char*)(size_t)pe0, (LAS char*)(size_t)(pe0 ^ 128)};
        LAS char* phv = lds + RH + blk_off(fr, g);
        auto rdB = [&](int sl, int nf, int t1) -> s16x4 {
            if (nf < 8) return tr_read(pbv[nf >> 1] + sl * 16384 + (nf & 1) * 2048 + t1 * 512);
            if (nf == 8) return tr_read(pev[t1] + sl * 2048);
            return tr_read(pqv[nf - 9] + sl * 4096 + t1 * 512);
        };
        auto rdA = [&](int sl, int m) -> bf16x8 { return row_frag((LAS char*)(size_t)pa + sl * 16384 + m * 1024); };
        auto rdH = [&](int sl) -> bf16x8 { return row_frag(phv + sl * 1024); };
#pragma unroll
        for (int n = 0; n < 11; ++n)
#pragma unroll
            for (int m = 0; m < 4; ++m) { acc[n][m] = (f32x4){0.f, 0.f, 0.f, 0.f}; asm volatile("" : "+v"(acc[n][m])); }
        ach = (f32x4){0.f, 0.f, 0.f, 0.f}; asm volatile("" : "+v"(ach));

        wait_vm<10>(); BAR();
        STAMP(1, 1);
        bf16x8 fa[2][2], fc[2], fh, fb[NB];
#pragma unroll
        for (int m = 0; m < 2; ++m) { fa[0][m] = rdA(0, m); fc[m] = rdA(0, 2 + m); }
#pragma unroll
        for (int i = 0; i < PF; ++i) { s16x4 lo = rdB(0, i, 0), hi = rdB(0, i, 1); fb[i] = __builtin_shufflevector(lo, hi, 0, 1, 2, 3, 4, 5, 6, 7); }
#define MM(C, A, B) mfma_ip(C, A, B)
        auto step = [&](auto S_, int t, bool do_issue, auto LAST1_, auto WAIT0_) {
            constexpr int S = decltype(S_)::value;
            constexpr bool has_next = !decltype(LAST1_)::value;
            constexpr bool wait0 = decltype(WAIT0_)::value;
            constexpr int sl = S, sn = (S + 1) & 3, cur = S & 1;
#pragma unroll
            for (int nf = 0; nf < 11; ++nf) {
                constexpr int dummy = 0; (void)dummy;
                const int r = 11 * S + nf;
                const int psl = (nf + PF < 11) ? sl : sn, nn = (nf + PF < 11) ? nf + PF : nf + PF - 11;
                const bool pf = (nf + PF < 11) || has_next;
                const bool lastg = (nf == 10);
                s16x4 lo, hi;
                if (!lastg) MM(acc[nf][0], fb[r % NB], fa[cur][0]); else MM(acc[nf][2], fb[r % NB], fc[0]);
                SCHED();
                if (nf == 0) {
                    if (!wait0) wait_vm<5>(); else wait_vm<0>();
                    BAR();
                }
                if (pf) lo = rdB(psl, nn, 0);
                SCHED();
                if (!lastg) MM(acc[nf][1], fb[r % NB], fa[cur][1]); else MM(acc[nf][3], fb[r % NB], fc[1]);
                SCHED();
                if (pf) hi = rdB(psl, nn, 1);
                if (lastg && has_next) fc[0] = rdA(sn, 2);
                SCHED();
                if (!lastg) MM(acc[nf][2], fb[r % NB], fc[0]); else MM(acc[nf][0], fb[r % NB], fa[cur][0]);
                SCHED();
                if (lastg && has_next) fc[1] = rdA(sn, 3);
                if (nf >= 4 && nf < 6 && has_next) fa[cur ^ 1][nf - 4] = rdA(sn, nf - 4);
                if (nf == 7) fh = rdH(sl);
                if ((nf < 4 || nf == 6) && do_issue) issue1(t + 3, (S + 3) & 3, nf < 4 ? nf : 4);
                SCHED();
                if (!lastg) MM(acc[nf][3], fb[r % NB], fc[1]); else MM(acc[nf][1], fb[r % NB], fa[cur][1]);
                SCHED();
                if (nf == 9) { MM(ach, fb[r % NB], fh); SCHED(); }
                if (pf) fb[(r + PF) % NB] = __builtin_shufflevector(lo, hi, 0, 1, 2, 3, 4, 5, 6, 7);
            }
        };
        using I0 = std::integral_constant<int, 0>; using I1 = std::integral_constant<int, 1>;
        using I2 = std::integral_constant<int, 2>; using I3 = std::integral_constant<int, 3>;
        using F = std::false_type; using T = std::true_type;
#pragma unroll 1
        for (int t0 = 0; t0 < 16; t0 += 4) {
            step(I0{}, t0, true, F{}, F{});
            step(I1{}, t0 + 1, true, F{}, F{});
            step(I2{}, t0 + 2, true, F{}, F{});
            step(I3{}, t0 + 3, t0 + 3 < 15, F{}, F{});
        }
        step(I0{}, 16, false, F{}, T{});
        step(I1{}, 17, false, T{}, T{});
    }
    asm volatile("s_nop 7\n\ts_nop 7" ::: "memory");
    BAR();
    STAMP(1, 2);
    k1_epilogue(acc, ach, lds, lds0, w, xcd, idx, j, mt, Wd, wo, hb);
}

constexpr int K2_NT = 21;
constexpr int K2_RB = 0, K2_RE = 65536, K2_LDS = 81920;
constexpr int NSPLIT = 4;
__device__ __forceinline__ void gload2(bf16x8& d0, bf16x8& d1, const void* sbase, unsigned voff) {
    sbase = uni(sbase);
    asm volatile("s_nop 4\n\tglobal_load_dwordx4 %0, %2, %3\n\tglobal_load_dwordx4 %1, %2, %3 offset:1024"
                 : "=&v"(d0), "=&v"(d1) : "v"(voff), "s"(sbase) : "memory");
}
__global__ __launch_bounds__(512, 2) void k2_down(const u16* __restrict__ hb, const u16* __restrict__ Wd,
                                                  const u16* __restrict__ wo, const u16* __restrict__ dx, u16* __restrict__ part, unsigned* __restrict__ cnt,
                                                  const u16* __restrict__ x, u16* __restrict__ out) {
    extern __shared__ __attribute__((aligned(16))) char smem[];
    LAS char* lds = (LAS char*)smem;
    const int tid = threadIdx.x, lane = tid & 63;
    const int w = __builtin_amdgcn_readfirstlane(tid >> 6);
    const int b = blockIdx.x, xcd = b & 7, idx = b >> 3;
    const int split = xcd >> 1, mt = (xcd & 1) * 8 + (idx >> 2), nt = idx & 3;
    const int wm = w >> 1, kh = w & 1;
    const int T0 = split == 0 ? 0 : 21 + 20 * (split - 1);

    const unsigned srcA = (unsigned)(kh * (int)BLK_ELEMS * 2 + blk_off(128 * mt + 32 * wm + (lane & 15), lane >> 4));
    const int koct = w >> 1, sub = w & 1;
    unsigned offB, offE;
    {
        const int r = lane >> 3, swz = ((r >> 1) & 1) | ((koct & 1) << 1), c = (lane & 7) ^ (swz << 1);
        offB = (unsigned)((32 * sub + 8 * koct + r) * DM + 128 * nt + 8 * c) * 2u;
        const int re = ((lane >> 1) & 7) ^ ((koct & 1) << 2), ce = (lane & 1);
        offE = (unsigned)((nt * 5184 + 32 * sub + 8 * koct + re) * 16 + 8 * ce) * 2u;
    }
    const unsigned lds0 = (unsigned)(size_t)lds;
    const unsigned dB = lds0 + K2_RB + (2 * w) * 1024, dE = lds0 + K2_RE + (sub * 4 + koct) * 256;
    bf16x8 fa[4][2];
    auto issue1 = [&](int t, int sl, int i, bf16x8& a0, bf16x8& a1) {
        int T = T0 + t; if (t == K2_NT - 1 && split != 0) T -= 1;
        const u16* Wt = T < 72 ? Wd + (size_t)T * 64 * DM : wo + (size_t)(T - 72) * 64 * DM;
        const u16* ba = hb + (size_t)T * 2 * BLK_ELEMS;
        if (i == 0) glds16(Wt, offB, dB + sl * 16384);
        if (i == 1) glds16(Wt + 64, offB, dB + sl * 16384 + 1024);
        if (i == 2) glds16_q(dx + (size_t)T * 64 * 16, offE, dE + sl * 2048);
        if (i == 3) gload2(a0, a1, ba, srcA);
    };
    STAMP(2, 0);
#pragma unroll
    for (int t = 0; t < 3; ++t)
#pragma unroll
        for (int i = 0; i < 4; ++i) issue1(t, t, i, fa[t][0], fa[t][1]);

    const int fr = lane & 15, g = lane >> 4, q4 = (lane >> 2) & 3, p4 = lane & 3;
    const int swz = ((q4 >> 1) & 1) | ((g & 1) << 1);
    LAS char* pb[4];
#pragma unroll
    for (int u = 0; u < 4; ++u) pb[u] = lds + K2_RB + (g * 4 + kh * 2) * 1024 + q4 * 128 + ((u ^ swz) << 5) + p4 * 8;
    LAS char* pe0 = lds + K2_RE + (kh * 4 + g) * 256 + ((q4 ^ ((g & 1) << 2)) * 32) + p4 * 8;
    LAS char* pe1 = lds + K2_RE + (kh * 4 + g) * 256 + (((4 + q4) ^ ((g & 1) << 2)) * 32) + p4 * 8;
    auto rdB = [&](int sl, int nf, int t1) -> s16x4 {
        if (nf < 8) return tr_read(pb[nf & 3] + sl * 16384 + (nf >> 2) * 1024 + t1 * 512);
        return tr_read((t1 ? pe1 : pe0) + sl * 2048);
    };

    f32x4 acc[9][2];
#pragma unroll
    for (int n = 0; n < 9; ++n)
#pragma unroll
        for (int m = 0; m < 2; ++m) { acc[n][m] = (f32x4){0.f, 0.f, 0.f, 0.f}; asm volatile("" : "+v"(acc[n][m])); }

    wait_vm<10>(); BAR();
    STAMP(2, 1);
    constexpr int PF2 = 4;
    bf16x8 fb[9];
#pragma unroll
    for (int i = 0; i < PF2; ++i) { s16x4 lo = rdB(0, i, 0), hi = rdB(0, i, 1); fb[i] = __builtin_shufflevector(lo, hi, 0, 1, 2, 3, 4, 5, 6, 7); }
    auto step = [&](auto S_, int t, bool do_issue, bool wait0, auto LAST_) {
        constexpr int S = decltype(S_)::value;
        constexpr bool has_next = !decltype(LAST_)::value;
        constexpr int sl = S, sn = (S + 1) & 3;
        asm volatile("" : "+v"(fa[sl][0]), "+v"(fa[sl][1]));
        if (!has_next && split != 0) {
#pragma unroll
            for (int m = 0; m < 2; ++m) fa[sl][m] = (bf16x8){0, 0, 0, 0, 0, 0, 0, 0};
            asm volatile("s_nop 1" : "+v"(fa[sl][0]), "+v"(fa[sl][1]));
        }
#pragma unroll
        for (int nf = 0; nf < 9; ++nf) {
            const int psl = (nf + PF2 < 9) ? sl : sn, nn = (nf + PF2 < 9) ? nf + PF2 : nf + PF2 - 9;
            const bool pf = (nf + PF2 < 9) || has_next;
            s16x4 lo, hi;
            mfma_ip(acc[nf][0], fb[nf], fa[sl][0]);
            SCHED();
            if (nf == 0) {
                if (!wait0) wait_vm<5>(); else wait_vm<0>();
                BAR();
            }
            if (pf) lo = rdB(psl, nn, 0);
            SCHED();
            mfma_ip(acc[nf][1], fb[nf], fa[sl][1]);
            SCHED();
            if (pf) hi = rdB(psl, nn, 1);
            if (nf >= 1 && nf < 5 && do_issue) issue1(t + 3, (S + 3) & 3, nf - 1, fa[(S + 3) & 3][0], fa[(S + 3) & 3][1]);
            SCHED();
            if (pf) fb[nn] = __builtin_shufflevector(lo, hi, 0, 1, 2, 3, 4, 5, 6, 7);
        }
    };
    using I0 = std::integral_constant<int, 0>; using I1 = std::integral_constant<int, 1>;
    using I2 = std::integral_constant<int, 2>; using I3 = std::integral_constant<int, 3>;
#pragma unroll 1
    for (int t0 = 0; t0 < 20; t0 += 4) {
        step(I0{}, t0, true, false, std::false_type{});
        step(I1{}, t0 + 1, true, false, std::false_type{});
        step(I2{}, t0 + 2, t0 + 2 < 18, false, std::false_type{});
        step(I3{}, t0 + 3, t0 + 3 < 18, t0 + 3 == 19, std::false_type{});
    }
    step(I0{}, 20, false, true, std::true_type{});
    asm volatile("s_nop 7\n\ts_nop 7" ::: "memory");
    BAR();
    STAMP(2, 2);
    f32x4 val[10];
    {
        LAS char* xw = lds + w * 10240;
        LAS char* xr = lds + (w ^ 1) * 10240;
#pragma unroll
        for (int n = 0; n < 9; ++n) {
            const bool give = kh ? (n < 5) : (n >= 5);
            if (give) {
#pragma unroll
                for (int m = 0; m < 2; ++m) *(LAS f32x4*)(xw + ((kh ? n : n - 5) * 2 + m) * 1024 + lane * 16) = acc[n][m];
            }
        }
        asm volatile("s_waitcnt lgkmcnt(0)" ::: "memory");
        BAR();
#pragma unroll
        for (int i = 0; i < 10; ++i) {
            const f32x4 mine = kh ? acc[(5 + (i >> 1)) < 9 ? 5 + (i >> 1) : 8][i & 1] : acc[i >> 1][i & 1];
            val[i] = mine + *(LAS f32x4*)(xr + i * 1024 + lane * 16);
        }
    }
    {
        const int nit = kh ? 8 : 10;
        const int grp = mt * 4 + nt;
        const __amdgpu_buffer_rsrc_t rp = __builtin_amdgcn_make_buffer_rsrc((void*)part, 0, NSPLIT * 64 * 8 * 10 * 512, 0x00020000);
        const unsigned slot = (unsigned)((grp * 8 + w) * 10 * 512 + lane * 8);
        constexpr unsigned SPL = 64 * 8 * 10 * 512;
        u32x2 xv[3]; unsigned xoff[3];
#pragma unroll
        for (int q = 0; q < 3; ++q) {
            int i = split + 4 * q; i = i < nit ? i : split;
            const int n = kh ? 5 + (i >> 1) : (i >> 1), m = i & 1;
            const int row = 128 * mt + 32 * wm + 16 * m + fr;
            const int col = (n < 8 ? 128 * nt + 16 * n : 512 + 16 * nt) + 4 * g;
            xoff[q] = (unsigned)(row * DM + col);
            xv[q] = *(const u32x2*)(x + xoff[q]);
        }
#pragma unroll
        for (int i = 0; i < 10; ++i) {
            if (i < nit && (i & 3) != split) {
                u32x2 v; v[0] = pk_bf16(val[i][0], val[i][1]); v[1] = pk_bf16(val[i][2], val[i][3]);
                __builtin_amdgcn_raw_buffer_store_b64(v, rp, slot + split * SPL + i * 512, 0, 16);
            }
        }
        STAMP(2, 3);
        asm volatile("s_waitcnt vmcnt(0)" ::: "memory");
        BAR();
        STAMP(2, 4);
        unsigned* c = cnt + grp * 64;
        if (tid == 0) {
            __hip_atomic_fetch_add(c, 1u, __ATOMIC_RELAXED, __HIP_MEMORY_SCOPE_AGENT);
            unsigned spins = 0;
            while (__hip_atomic_load(c, __ATOMIC_RELAXED, __HIP_MEMORY_SCOPE_AGENT) < (unsigned)NSPLIT) {
                __builtin_amdgcn_s_sleep(1);
                if (++spins > (1u << 22)) break;
            }
        }
        BAR();
        STAMP(2, 5);
        u32x2 pv[3][3];
#pragma unroll
        for (int q = 0; q < 3; ++q) {
            const int i = split + 4 * q;
#pragma unroll
            for (int s2 = 1; s2 < NSPLIT; ++s2) {
                const int so = (split + s2) & 3;
                const unsigned off = i < nit ? slot + so * SPL + i * 512 : 0xfffffff0u;
                pv[q][s2 - 1] = __builtin_amdgcn_raw_buffer_load_b64(rp, off, 0, 16);
            }
        }
#pragma unroll
        for (int q = 0; q < 3; ++q) {
            const int i = split + 4 * q;
            if (i < nit) {
                f32x4 o = val[0];
#pragma unroll
                for (int ii = 0; ii < 10; ++ii) if (ii == i) o = val[ii];
#pragma unroll
                for (int s2 = 0; s2 < 3; ++s2) { o[0] += bf_lo(pv[q][s2][0]); o[1] += bf_hi(pv[q][s2][0]); o[2] += bf_lo(pv[q][s2][1]); o[3] += bf_hi(pv[q][s2][1]); }
                u32x2 v; v[0] = pk_bf16(o[0] + bf_lo(xv[q][0]), o[1] + bf_hi(xv[q][0])); v[1] = pk_bf16(o[2] + bf_lo(xv[q][1]), o[3] + bf_hi(xv[q][1]));
                *(u32x2*)(out + xoff[q]) = v;
            }
        }
    }
    STAMP(2, 15);
}

extern "C" void kernel_launch(void* const* d_in, const int* in_sizes, int n_in, void* d_out, int out_size, void* d_ws,
                              size_t ws_size, hipStream_t stream) {
    const u16* x = (const u16*)d_in[0];
    const u16* wqkv = (const u16*)d_in[1];
    const u16* wo = (const u16*)d_in[2];
    const u16* Wg = (const u16*)d_in[4];
    const u16* Wu = (const u16*)d_in[5];
    const u16* Wd = (const u16*)d_in[6];
    char* ws = (char*)d_ws;
    u16* xnb = (u16*)ws;
    u16* hb = (u16*)(ws + (4u << 20));
    u16* part = (u16*)(ws + (32u << 20));
    unsigned* cnt = (unsigned*)(ws + (56u << 20));
    u16* gx = (u16*)(ws + (52u << 20));
    u16* dx = (u16*)(ws + (54u << 20));
    u16* wqh = (u16*)(ws + (48u << 20));
    (void)hipFuncSetAttribute((const void*)k1_gateup, hipFuncAttributeMaxDynamicSharedMemorySize, K1_LDS);
    (void)hipFuncSetAttribute((const void*)k2_down, hipFuncAttributeMaxDynamicSharedMemorySize, K2_LDS);
    k0_ln<<<256, 256, 0, stream>>>(x, xnb, wqkv, wqh, Wg, Wu, gx, Wd, wo, dx);
    k1_gateup<<<256, 512, K1_LDS, stream>>>(xnb, Wg, Wu, wqh, gx, Wd, wo, hb, cnt);
    k2_down<<<256, 512, K2_LDS, stream>>>(hb, Wd, wo, dx, part, cnt, x, (u16*)d_out);
}
```

```cpp
#include <hip/hip_runtime.h>
#include <hip/hip_bf16.h>
#include <stdint.h>
#include <type_traits>

typedef unsigned short u16;
typedef short bf16x8 __attribute__((ext_vector_type(8)));
typedef short s16x4 __attribute__((ext_vector_type(4)));
typedef float f32x4 __attribute__((ext_vector_type(4)));
typedef float f32x2 __attribute__((ext_vector_type(2)));
typedef __bf16 bf16x2_t __attribute__((ext_vector_type(2)));
typedef unsigned u32x4 __attribute__((ext_vector_type(4)));
typedef unsigned u32x2 __attribute__((ext_vector_type(2)));
#define LAS __attribute__((address_space(3)))
#define GAS __attribute__((address_space(1)))

constexpr int SEQ = 2048, DM = 576, FF = 4608;
constexpr int NKB_X = 18;
constexpr int NKB_H = 162;
constexpr size_t BLK_ELEMS = (size_t)SEQ * 32;

__device__ __forceinline__ unsigned pk_bf16(float lo, float hi) {
    f32x2 v = {lo, hi};
    bf16x2_t b = __builtin_convertvector(v, bf16x2_t);
    return __builtin_bit_cast(unsigned, b);
}
__device__ __forceinline__ float bf_lo(unsigned u) { return __uint_as_float(u << 16); }
__device__ __forceinline__ float bf_hi(unsigned u) { return __uint_as_float(u & 0xffff0000u); }
__device__ __forceinline__ float rbf(float x) { return bf_lo(pk_bf16(x, x)); }

__device__ __forceinline__ const void* uni(const void* p) {
    const unsigned long long v = (unsigned long long)p;
    const unsigned lo = __builtin_amdgcn_readfirstlane((unsigned)v), hi = __builtin_amdgcn_readfirstlane((unsigned)(v >> 32));
    return (const void*)(((unsigned long long)hi << 32) | lo);
}
__device__ __forceinline__ void glds16(const void* sbase, unsigned voff, unsigned lds) {
    sbase = uni(sbase); lds = __builtin_amdgcn_readfirstlane(lds);
    asm volatile("s_mov_b32 m0, %2\n\ts_nop 3\n\tglobal_load_lds_dwordx4 %0, %1"
                 :: "v"(voff), "s"(sbase), "s"(lds) : "memory", "m0");
}
__device__ __forceinline__ void glds16_o1k(const void* sbase, unsigned voff, unsigned lds) {
    sbase = uni(sbase); lds = __builtin_amdgcn_readfirstlane(lds);
    asm volatile("s_mov_b32 m0, %2\n\ts_nop 3\n\tglobal_load_lds_dwordx4 %0, %1 offset:1024"
                 :: "v"(voff), "s"(sbase), "s"(lds) : "memory", "m0");
}
__device__ __forceinline__ void glds16_q(const void* sbase, unsigned voff, unsigned lds) {
    sbase = uni(sbase); lds = __builtin_amdgcn_readfirstlane(lds);
    unsigned sv; unsigned long long ex;
    asm volatile("s_mov_b32 %0, m0\n\ts_mov_b64 %1, exec\n\ts_mov_b32 m0, %4\n\ts_mov_b64 exec, 0xffff\n\ts_nop 1\n\tglobal_load_lds_dwordx4 %2, %3\n\ts_mov_b64 exec, %1\n\ts_mov_b32 m0, %0"
                 : "=&s"(sv), "=&s"(ex) : "v"(voff), "s"(sbase), "s"(lds) : "memory");
}
__device__ __forceinline__ void glds16_m(const void* sbase, unsigned voff, unsigned lds) {
    sbase = uni(sbase); lds = __builtin_amdgcn_readfirstlane(lds);
    unsigned sv; unsigned long long ex;
    asm volatile("s_mov_b32 %0, m0\n\ts_mov_b64 %1, exec\n\ts_mov_b32 m0, %4\n\ts_mov_b32 exec_lo, -1\n\ts_mov_b32 exec_hi, 0xff\n\ts_nop 1\n\tglobal_load_lds_dwordx4 %2, %3\n\ts_mov_b64 exec, %1\n\ts_mov_b32 m0, %0"
                 : "=&s"(sv), "=&s"(ex) : "v"(voff), "s"(sbase), "s"(lds) : "memory");
}
#define SCHED() __builtin_amdgcn_sched_barrier(0)
#define STAMP(kid, i) do {} while (0)
#define WSTAMP(i) do {} while (0)
__device__ __forceinline__ s16x4 tr_read(LAS char* p) {
    return __builtin_bit_cast(s16x4, __builtin_amdgcn_ds_read_tr16_b64_v4i16((LAS s16x4*)p));
}
__device__ __forceinline__ bf16x8 tr_frag(LAS char* p, int off_t1) {
    s16x4 lo = tr_read(p), hi = tr_read(p + off_t1);
    return __builtin_shufflevector(lo, hi, 0, 1, 2, 3, 4, 5, 6, 7);
}
__device__ __forceinline__ bf16x8 row_frag(LAS char* p) { return *(LAS bf16x8*)p; }
#define MFMA16(a, b, c) __builtin_amdgcn_mfma_f32_16x16x32_bf16((a), (b), (c), 0, 0, 0)
__device__ __forceinline__ void mfma_ip(f32x4& c, bf16x8 a, bf16x8 b) { asm volatile("v_mfma_f32_16x16x32_bf16 %0, %1, %2, %0" : "+v"(c) : "v"(a), "v"(b)); }
#define WAIT_V(n) asm volatile("s_waitcnt vmcnt(" #n ")" ::: "memory")
#define BAR() do { asm volatile("" ::: "memory"); __builtin_amdgcn_s_barrier(); asm volatile("" ::: "memory"); } while (0)

__device__ __forceinline__ int blk_off(int row, int chunk) { return row * 64 + ((chunk ^ (((row >> 3) & 1) << 1)) << 4); }

__global__ __launch_bounds__(256) void k0_ln(const u16* __restrict__ x, u16* __restrict__ xnb,
                                            const u16* __restrict__ wqkv, u16* __restrict__ wqh,
                                            const u16* __restrict__ Wg, const u16* __restrict__ Wu, u16* __restrict__ gx,
                                            const u16* __restrict__ Wd, const u16* __restrict__ wo, u16* __restrict__ dx) {
    if (threadIdx.x >= 64) {
        const int l = threadIdx.x - 64, head = blockIdx.x >> 3, k0 = (blockIdx.x & 7) * 72;
        unsigned v[12];
#pragma unroll
        for (int i = 0; i < 12; ++i) {
            const int id = l + 192 * i, k = k0 + (id >> 5), cp = id & 31;
            const int part = cp >= 18 ? 2 : (cp >= 9 ? 1 : 0), d2 = cp - part * 9;
            const int src = (part == 2 ? 0 : part + 1) * DM + 18 * head + 2 * d2;
            v[i] = cp < 27 ? *(const unsigned*)(wqkv + (size_t)k * (3 * DM) + src) : 0u;
        }
        u32x4 c[3];
#pragma unroll
        for (int i = 0; i < 2; ++i) {
            const int id = l + 192 * i;
            if (id < 288) {
                const int gid = blockIdx.x * 288 + id, mat = gid / 36864, r = gid - mat * 36864, k = r >> 6, ch = r & 63;
                c[i] = *(const u32x4*)((mat ? Wu : Wg) + (size_t)k * FF + 4096 + 8 * ch);
            }
        }
        if (l < 162) {
            const int gid = blockIdx.x * 162 + l, k = gid >> 3, ch = gid & 7;
            c[2] = *(const u32x4*)((k < FF ? Wd + (size_t)k * DM : wo + (size_t)(k - FF) * DM) + 512 + 8 * ch);
        }
#pragma unroll
        for (int i = 0; i < 12; ++i) {
            const int id = l + 192 * i;
            *(unsigned*)(wqh + ((size_t)head * 576 + k0) * 64 + 2 * id) = v[i];
        }
#pragma unroll
        for (int i = 0; i < 2; ++i) {
            const int id = l + 192 * i;
            if (id < 288) {
                const int gid = blockIdx.x * 288 + id, mat = gid / 36864, r = gid - mat * 36864, k = r >> 6, ch = r & 63;
                *(u32x4*)(gx + (((size_t)mat * 32 + (ch >> 1)) * 576 + k) * 16 + 8 * (ch & 1)) = c[i];
            }
        }
        if (l < 162) {
            const int gid = blockIdx.x * 162 + l, k = gid >> 3, ch = gid & 7;
            *(u32x4*)(dx + ((size_t)(ch >> 1) * 5184 + k) * 16 + 8 * (ch & 1)) = c[2];
        }
        return;
    }
    const int lane = threadIdx.x, i = lane & 7, row = blockIdx.x * 8 + (lane >> 3);
    u32x4 xv[9];
#pragma unroll
    for (int cc = 0; cc < 9; ++cc) xv[cc] = *(const u32x4*)(x + (size_t)row * DM + (i + 8 * cc) * 8);
    float s = 0.f;
#pragma unroll
    for (int cc = 0; cc < 9; ++cc)
#pragma unroll
        for (int e = 0; e < 4; ++e) s += bf_lo(xv[cc][e]) + bf_hi(xv[cc][e]);
    s += __shfl_xor(s, 1); s += __shfl_xor(s, 2); s += __shfl_xor(s, 4);
    const float mean = s * (1.0f / 576.0f);
    float q = 0.f;
#pragma unroll
    for (int cc = 0; cc < 9; ++cc)
#pragma unroll
        for (int e = 0; e < 4; ++e) { const float a = bf_lo(xv[cc][e]) - mean, c2 = bf_hi(xv[cc][e]) - mean; q += a * a + c2 * c2; }
    q += __shfl_xor(q, 1); q += __shfl_xor(q, 2); q += __shfl_xor(q, 4);
    const float mb = rbf(mean), vb = rbf(q * (1.0f / 576.0f));
    const float sb = rbf(sqrtf(rbf(vb + 1e-5f)));
    const float rs = 1.0f / sb;
#pragma unroll
    for (int cc = 0; cc < 9; ++cc) {
        u32x4 o;
#pragma unroll
        for (int e = 0; e < 4; ++e) {
            const unsigned d = pk_bf16(bf_lo(xv[cc][e]) - mb, bf_hi(xv[cc][e]) - mb);
            o[e] = pk_bf16(bf_lo(d) * rs, bf_hi(d) * rs);
        }
        const int blk = 2 * cc + (i >> 2);
        *(u32x4*)((char*)xnb + (size_t)blk * (BLK_ELEMS * 2) + blk_off(row, i & 3)) = o;
    }
}

constexpr int RA = 0, RB = 65536, RE = 131072, RQ = 139264, RH = 155648;
constexpr int T_DUMMY = 159744;
constexpr int K1_LDS = T_DUMMY + 1024;
constexpr int QS = 144;
#define HST_AUX 16
#define PF 3
constexpr int NB = PF + 1;
template <int N> __device__ __forceinline__ void wait_vm() { asm volatile("s_waitcnt vmcnt(%0)" :: "i"(N) : "memory"); }
#define GLDS_MASKED(NAME, LO, HI) \
__device__ __forceinline__ void NAME(const void* sbase, unsigned voff, unsigned lds) { \
    sbase = uni(sbase); lds = __builtin_amdgcn_readfirstlane(lds); \
    unsigned sv; unsigned long long ex; \
    asm volatile("s_mov_b32 %0, m0\n\ts_mov_b64 %1, exec\n\ts_mov_b32 m0, %4\n\ts_mov_b32 exec_lo, " #LO "\n\ts_mov_b32 exec_hi, " #HI "\n\tglobal_load_lds_dwordx4 %2, %3\n\ts_mov_b64 exec, %1\n\ts_mov_b32 m0, %0" \
                 : "=&s"(sv), "=&s"(ex) : "v"(voff), "s"(sbase), "s"(lds) : "memory"); }
GLDS_MASKED(glds16_32, -1, 0)
GLDS_MASKED(glds16_8, 0xff, 0)

__device__ __forceinline__ void k1_epilogue(f32x4 (&acc)[11][4], f32x4& ach, LAS char* lds, unsigned lds0, int w, int xcd, int idx,
                                            int j, int mt, const u16* __restrict__ Wd, const u16* __restrict__ wo, u16* __restrict__ hb) {
    int tid = threadIdx.x; asm volatile("" : "+v"(tid));
    const int lane = tid & 63, fr = lane & 15, g = lane >> 4;
    const int wm = w >> 1, wn = w & 1;
    const __amdgpu_buffer_rsrc_t rh = __builtin_amdgcn_make_buffer_rsrc((void*)hb, 0, (int)(NKB_H * BLK_ELEMS * 2), 0x00020000);
    auto hs = [&](int m, int part) {
        const int row = 256 * mt + 64 * wm + 16 * m + fr;
        SCHED();
        if (part < 2) {
            const int pp = part;
            unsigned v[2][2];
#pragma unroll
            for (int hh = 0; hh < 2; ++hh) {
                const int gt = 2 * pp + hh;
                float hv[4];
#pragma unroll
                for (int e = 0; e < 4; ++e) hv[e] = fmaxf(acc[2 * gt][m][e], 0.f) * acc[2 * gt + 1][m][e];
                v[hh][0] = pk_bf16(hv[0], hv[1]); v[hh][1] = pk_bf16(hv[2], hv[3]);
            }
            auto r0 = __builtin_amdgcn_permlane16_swap(v[0][0], v[1][0], false, false);
            auto r1 = __builtin_amdgcn_permlane16_swap(v[0][1], v[1][1], false, false);
            u32x4 o; o[0] = r0[0]; o[1] = r1[0]; o[2] = r0[1]; o[3] = r1[1];
            const int blk = 4 * j + 2 * wn + pp, chunk = ((g & 1) << 1) | (g >> 1);
            __builtin_amdgcn_raw_buffer_store_b128(o, rh, (unsigned)(blk * (int)(BLK_ELEMS * 2) + blk_off(row, chunk)), 0, HST_AUX);
        } else {
            float hv[4];
#pragma unroll
            for (int e = 0; e < 4; ++e) {
                const unsigned mine = __float_as_uint(acc[8][m][e]);
                auto r = __builtin_amdgcn_permlane32_swap(mine, mine, false, false);
                hv[e] = fmaxf(__uint_as_float(r[0]), 0.f) * __uint_as_float(r[1]);
            }
            u32x2 v; v[0] = pk_bf16(hv[0], hv[1]); v[1] = pk_bf16(hv[2], hv[3]);
            const int blk = 128 + (j >> 1), chunk = (j & 1) * 2 + wn;
            const unsigned off = g < 2 ? (unsigned)(blk * (int)(BLK_ELEMS * 2) + blk_off(row, chunk) + g * 8) : 0xfffffff0u;
            __builtin_amdgcn_raw_buffer_store_b64(v, rh, off, 0, HST_AUX);
        }
        SCHED();
    };
    hs(0, 0);
    BAR();
    STAMP(1, 2);
    LAS char* qt = lds;
    {
#pragma unroll
        for (int u = 0; u < 2; ++u) {
            const int nn = 16 * (2 * wn + (u ^ (wm & 1))) + 4 * g;
            const int h0 = nn / 18, h1 = (nn + 2) / 18;
            const int o0 = h0 * 48 + (nn - h0 * 18) * 2, o1 = h1 * 48 + (nn + 2 - h1 * 18) * 2;
#pragma unroll
            for (int m = 0; m < 4; ++m) {
                LAS char* rp = qt + (16 + 64 * wm + 16 * m + fr) * QS;
                if (h0 < 3) *(LAS unsigned*)(rp + o0) = pk_bf16(acc[9 + u][m][0], acc[9 + u][m][1]);
                if (h1 < 3) *(LAS unsigned*)(rp + o1) = pk_bf16(acc[9 + u][m][2], acc[9 + u][m][3]);
            }
            if (u == 0 && (w == 0 || w == 2 || w == 1)) {
                LAS char* rp = qt + fr * QS;
                if (h0 < 3) *(LAS unsigned*)(rp + o0) = pk_bf16(ach[0], ach[1]);
                if (h1 < 3) *(LAS unsigned*)(rp + o1) = pk_bf16(ach[2], ach[3]);
            }
            if (u == 0) hs(0, 1);
        }
    }
    asm volatile("s_waitcnt lgkmcnt(0)" ::: "memory");
    BAR();
    STAMP(1, 3);
    {
        const int s = tid >> 1, half = tid & 1;
        const int lrow = 16 + s, grow = 256 * mt + s;
        auto ld18 = [&](LAS const char* p, unsigned (&v)[9]) {
            const u32x4 a = *(LAS const u32x4*)p, c = *(LAS const u32x4*)(p + 16);
            v[0] = a[0]; v[1] = a[1]; v[2] = a[2]; v[3] = a[3]; v[4] = c[0]; v[5] = c[1]; v[6] = c[2]; v[7] = c[3];
            v[8] = *(LAS const unsigned*)(p + 32);
        };
        hs(0, 2);
        unsigned qv[9];
        ld18(qt + lrow * QS + 96, qv);
        float lg[4];
#pragma unroll
        for (int ww = 0; ww < 4; ++ww) {
            const int wi = 4 * half + ww;
            unsigned kv[9];
            ld18(qt + (lrow - 2 * wi) * QS, kv);
            float a = 0.f;
#pragma unroll
            for (int i = 0; i < 9; ++i) a = __builtin_amdgcn_fdot2_f32_bf16(__builtin_bit_cast(bf16x2_t, qv[i]), __builtin_bit_cast(bf16x2_t, kv[i]), a, false);
            a = rbf(rbf(a) * 0.23570226039551584f);
            lg[ww] = (grow - 2 * wi >= 0) ? a : -1e9f;
            if (ww == 0) hs(1, 0);
            if (ww == 2) hs(1, 1);
        }
        float mx = fmaxf(fmaxf(lg[0], lg[1]), fmaxf(lg[2], lg[3]));
        mx = fmaxf(mx, __shfl_xor(mx, 1));
        float ex[4], sum = 0.f;
#pragma unroll
        for (int ww = 0; ww < 4; ++ww) { ex[ww] = __expf(lg[ww] - mx); sum += ex[ww]; }
        sum += __shfl_xor(sum, 1);
        const float inv = 1.0f / sum;
        hs(1, 2);
        float o[18];
#pragma unroll
        for (int i = 0; i < 18; ++i) o[i] = 0.f;
#pragma unroll
        for (int ww = 0; ww < 4; ++ww) {
            const int wi = 4 * half + ww;
            const float pw = (grow - 2 * wi >= 0) ? rbf(ex[ww] * inv) : 0.f;
            unsigned vv[9];
            ld18(qt + (lrow - 2 * wi) * QS + 48, vv);
#pragma unroll
            for (int i = 0; i < 9; ++i) { o[2 * i] += pw * bf_lo(vv[i]); o[2 * i + 1] += pw * bf_hi(vv[i]); }
            if (ww < 3) hs(2, ww);
        }
        hs(3, 0);
#pragma unroll
        for (int ii = 0; ii < 5; ++ii) {
            const float s0 = half ? o[2 * ii] : o[(10 + 2 * ii) % 18], s1 = half ? o[2 * ii + 1] : o[(11 + 2 * ii) % 18];
            const float r0 = __shfl_xor(s0, 1), r1 = __shfl_xor(s1, 1);
            const float m0 = half ? o[(10 + 2 * ii) % 18] : o[2 * ii], m1 = half ? o[(11 + 2 * ii) % 18] : o[2 * ii + 1];
            const int i = half * 5 + ii;
            if (i < 9) {
                const int col = FF + 18 * j + 2 * i;
                *(unsigned*)((char*)hb + (size_t)(col >> 5) * (BLK_ELEMS * 2) + blk_off(grow, (col >> 3) & 3) + (col & 7) * 2) = pk_bf16(m0 + r0, m1 + r1);
            }
            if (ii == 2) hs(3, 1);
        }
        hs(3, 2);
    }
    STAMP(1, 15);
}

__global__ __launch_bounds__(512, 2) void k1_gateup(const u16* __restrict__ xnb, const u16* __restrict__ Wg,
                                                    const u16* __restrict__ Wu, const u16* __restrict__ wqh, const u16* __restrict__ gx,
                                                    const u16* __restrict__ Wd, const u16* __restrict__ wo, u16* __restrict__ hb, unsigned* __restrict__ cnt) {
    extern __shared__ __attribute__((aligned(16))) char smem[];
    LAS char* lds = (LAS char*)smem;
    const int tid = threadIdx.x, lane = tid & 63;
    if (blockIdx.x < 64 && tid == 0) cnt[blockIdx.x * 64] = 0u;
    const int w = __builtin_amdgcn_readfirstlane(tid >> 6);
    const int b = blockIdx.x, xcd = b & 7, idx = b >> 3;
    const int j = 4 * xcd + (idx & 3), mt = idx >> 2;
    const int wm = w >> 1, wn = w & 1;
    const unsigned lds0 = (unsigned)(size_t)lds;
    const int fr = lane & 15, g = lane >> 4, q4 = (lane >> 2) & 3, p4 = lane & 3;

    f32x4 acc[11][4], ach;
    {
        const unsigned srcA = (unsigned)((256 * mt + 32 * w) * 32 + lane * 8) * 2u;
        const int koct = w >> 1, mat = w & 1;
        const u16* W = mat ? Wu : Wg;
        unsigned srcB, srcX;
        const u16* baseX; int strideX; unsigned dX;
        {
            const int r = lane >> 3, swz = ((r >> 1) & 1) | ((koct & 1) << 1), c = (lane & 7) ^ (swz << 1);
            srcB = (unsigned)((8 * koct + r) * FF + 128 * j + 8 * c) * 2u;
            if (w < 2) {
                const int ko = lane >> 4, re = ((lane >> 1) & 7) ^ ((ko & 1) << 2), ce = (lane & 1) ^ w;
                srcX = (unsigned)(((w * 32 + j) * 576 + 8 * ko + re) * 16 + 8 * ce) * 2u;
                baseX = gx; strideX = 32 * 16; dX = lds0 + RE + w * 1024;
            } else if (w < 6) {
                const int ko = w - 2, swq = ((r >> 1) & 1) | ((ko & 1) << 1), cq = (lane & 7) ^ (swq << 1);
                srcX = (unsigned)((j * 576 + 8 * ko + r) * 64 + 8 * cq) * 2u;
                baseX = wqh; strideX = 32 * 64; dX = lds0 + RQ + ko * 1024;
            } else {
                const int hr = mt > 0 ? 256 * mt - 16 : 0;
                srcX = (unsigned)(hr * 64 + lane * 16);
                baseX = xnb; strideX = (int)BLK_ELEMS; dX = lds0 + RH;
            }
        }
        const int slotX = w < 2 ? 2048 : (w < 6 ? 4096 : 1024);
        const unsigned dA = lds0 + RA + (2 * w) * 1024, dB = lds0 + RB + (2 * w) * 1024;
        auto issue1 = [&](int t, int sl, int i) {
            const u16* ba = xnb + (size_t)t * BLK_ELEMS;
            const u16* bw = W + (size_t)t * 32 * FF;
            if (i == 0) glds16(bw, srcB, dB + sl * 16384);
            if (i == 1) glds16(bw + 64, srcB, dB + sl * 16384 + 1024);
            if (i == 2) glds16(baseX + (size_t)t * strideX, srcX, dX + sl * slotX);
            if (i == 3) glds16(ba, srcA, dA + sl * 16384);
            if (i == 4) glds16_o1k(ba, srcA, dA + sl * 16384);
        };
        auto prefetch = [&](int p) {
            const int split = xcd >> 1;
            const unsigned start = (unsigned)((split == 0 ? 0 : 1344 + 1280 * (split - 1)) * DM * 2);
            const unsigned len = (unsigned)((split == 0 ? 1344 : 1280) * DM * 2), lwd = (unsigned)(704 * DM * 2);
            p = p < 6 ? p : 5;
            unsigned o = (unsigned)((idx * 8 + w) * 6144 + p * 1024); o = o + 1024 <= len ? o : 0u;
            const char* src = (split == 3 && o >= lwd) ? (const char*)wo + (o - lwd) : (const char*)Wd + start + o;
            glds16(src, (unsigned)lane * 16u, lds0 + T_DUMMY);
        };
        STAMP(1, 0);
#pragma unroll
        for (int t = 0; t < 3; ++t) {
#pragma unroll
            for (int i = 0; i < 5; ++i) issue1(t, t, i);
            if (t == 1) prefetch(0);
        }

        const unsigned pa = lds0 + RA + blk_off(64 * wm + fr, g);
        const int swz = ((q4 >> 1) & 1) | ((g & 1) << 1);
        const unsigned pb0 = lds0 + RB + (g * 4 + wn) * 1024 + q4 * 128 + (swz << 5) + p4 * 8;
        const int emat = p4 >> 1;
        const unsigned pe0 = lds0 + RE + (emat * 4 + g) * 256 + ((q4 ^ ((g & 1) << 2)) * 32) + ((wn ^ emat) * 16) + (p4 & 1) * 8;
        const unsigned pq0 = lds0 + RQ + g * 1024 + q4 * 128 + (((2 * wn + (wm & 1)) ^ swz) << 5) + p4 * 8;
        LAS char* pbv[4]; LAS char* pqv[2];
#pragma unroll
        for (int u = 0; u < 4; ++u) pbv[u] = (LAS char*)(size_t)(pb0 ^ (u << 5));
        pqv[0] = (LAS char*)(size_t)pq0; pqv[1] = (LAS char*)(size_t)(pq0 ^ 32);
        LAS char* pev[2] = {(LAS char*)(size_t)pe0, (LAS char*)(size_t)(pe0 ^ 128)};
        LAS char* phv = lds + RH + blk_off(fr, g);
        auto rdB = [&](int sl, int nf, int t1) -> s16x4 {
            if (nf < 8) return tr_read(pbv[nf >> 1] + sl * 16384 + (nf & 1) * 2048 + t1 * 512);
            if (nf == 8) return tr_read(pev[t1] + sl * 2048);
            return tr_read(pqv[nf - 9] + sl * 4096 + t1 * 512);
        };
        auto rdA = [&](int sl, int m) -> bf16x8 { return row_frag((LAS char*)(size_t)pa + sl * 16384 + m * 1024); };
        auto rdH = [&](int sl) -> bf16x8 { return row_frag(phv + sl * 1024); };
#pragma unroll
        for (int n = 0; n < 11; ++n)
#pragma unroll
            for (int m = 0; m < 4; ++m) { acc[n][m] = (f32x4){0.f, 0.f, 0.f, 0.f}; asm volatile("" : "+v"(acc[n][m])); }
        ach = (f32x4){0.f, 0.f, 0.f, 0.f}; asm volatile("" : "+v"(ach));

        wait_vm<11>(); BAR();
        STAMP(1, 1);
        bf16x8 fa[2][2], fc[2], fh, fb[NB];
#pragma unroll
        for (int m = 0; m < 2; ++m) { fa[0][m] = rdA(0, m); fc[m] = rdA(0, 2 + m); }
#pragma unroll
        for (int i = 0; i < PF; ++i) { s16x4 lo = rdB(0, i, 0), hi = rdB(0, i, 1); fb[i] = __builtin_shufflevector(lo, hi, 0, 1, 2, 3, 4, 5, 6, 7); }
#define MM(C, A, B) mfma_ip(C, A, B)
        auto step = [&](auto S_, int t, bool do_issue, auto LAST1_, auto WAIT0_) {
            constexpr int S = decltype(S_)::value;
            constexpr bool has_next = !decltype(LAST1_)::value;
            constexpr bool wait0 = decltype(WAIT0_)::value;
            constexpr int sl = S, sn = (S + 1) & 3, cur = S & 1;
#pragma unroll
            for (int nf = 0; nf < 11; ++nf) {
                constexpr int dummy = 0; (void)dummy;
                const int r = 11 * S + nf;
                const int psl = (nf + PF < 11) ? sl : sn, nn = (nf + PF < 11) ? nf + PF : nf + PF - 11;
                const bool pf = (nf + PF < 11) || has_next;
                const bool lastg = (nf == 10);
                s16x4 lo, hi;
                if (!lastg) MM(acc[nf][0], fb[r % NB], fa[cur][0]); else MM(acc[nf][2], fb[r % NB], fc[0]);
                SCHED();
                if (nf == 0) {
                    if (!wait0) wait_vm<6>(); else wait_vm<0>();
                    BAR();
                }
                if (pf) lo = rdB(psl, nn, 0);
                SCHED();
                if (!lastg) MM(acc[nf][1], fb[r % NB], fa[cur][1]); else MM(acc[nf][3], fb[r % NB], fc[1]);
                SCHED();
                if (pf) hi = rdB(psl, nn, 1);
                if (lastg && has_next) fc[0] = rdA(sn, 2);
                SCHED();
                if (!lastg) MM(acc[nf][2], fb[r % NB], fc[0]); else MM(acc[nf][0], fb[r % NB], fa[cur][0]);
                SCHED();
                if (lastg && has_next) fc[1] = rdA(sn, 3);
                if (nf >= 4 && nf < 6 && has_next) fa[cur ^ 1][nf - 4] = rdA(sn, nf - 4);
                if (nf == 7) fh = rdH(sl);
                if ((nf < 4 || nf == 6) && do_issue) issue1(t + 3, (S + 3) & 3, nf < 4 ? nf : 4);
                if (nf == 8 && (S & 1) == 0 && do_issue) prefetch(1 + (t >> 1));
                SCHED();
                if (!lastg) MM(acc[nf][3], fb[r % NB], fc[1]); else MM(acc[nf][1], fb[r % NB], fa[cur][1]);
                SCHED();
                if (nf == 9) { MM(ach, fb[r % NB], fh); SCHED(); }
                if (pf) fb[(r + PF) % NB] = __builtin_shufflevector(lo, hi, 0, 1, 2, 3, 4, 5, 6, 7);
            }
        };
        using I0 = std::integral_constant<int, 0>; using I1 = std::integral_constant<int, 1>;
        using I2 = std::integral_constant<int, 2>; using I3 = std::integral_constant<int, 3>;
        using F = std::false_type; using T = std::true_type;
#pragma unroll 1
        for (int t0 = 0; t0 < 16; t0 += 4) {
            step(I0{}, t0, true, F{}, F{});
            step(I1{}, t0 + 1, true, F{}, F{});
            step(I2{}, t0 + 2, true, F{}, F{});
            step(I3{}, t0 + 3, t0 + 3 < 15, F{}, F{});
        }
        step(I0{}, 16, false, F{}, T{});
        step(I1{}, 17, false, T{}, T{});
    }
    asm volatile("s_nop 7\n\ts_nop 7" ::: "memory");
    STAMP(1, 4);
    k1_epilogue(acc, ach, lds, lds0, w, xcd, idx, j, mt, Wd, wo, hb);
}

constexpr int K2_NT = 21;
constexpr int K2_RB = 0, K2_RE = 65536, K2_LDS = 81920;
constexpr int NSPLIT = 4;
__device__ __forceinline__ void gload2(bf16x8& d0, bf16x8& d1, const void* sbase, unsigned voff) {
    sbase = uni(sbase);
    asm volatile("s_nop 4\n\tglobal_load_dwordx4 %0, %2, %3\n\tglobal_load_dwordx4 %1, %2, %3 offset:1024"
                 : "=&v"(d0), "=&v"(d1) : "v"(voff), "s"(sbase) : "memory");
}
__global__ __launch_bounds__(512, 2) void k2_down(const u16* __restrict__ hb, const u16* __restrict__ Wd,
                                                  const u16* __restrict__ wo, const u16* __restrict__ dx, u16* __restrict__ part, unsigned* __restrict__ cnt,
                                                  const u16* __restrict__ x, u16* __restrict__ out) {
    extern __shared__ __attribute__((aligned(16))) char smem[];
    LAS char* lds = (LAS char*)smem;
    const int tid = threadIdx.x, lane = tid & 63;
    const int w = __builtin_amdgcn_readfirstlane(tid >> 6);
    const int b = blockIdx.x, xcd = b & 7, idx = b >> 3;
    const int split = xcd >> 1, mt = (xcd & 1) * 8 + (idx >> 2), nt = idx & 3;
    const int wm = w >> 1, kh = w & 1;
    const int T0 = split == 0 ? 0 : 21 + 20 * (split - 1);

    const unsigned srcA = (unsigned)(kh * (int)BLK_ELEMS * 2 + blk_off(128 * mt + 32 * wm + (lane & 15), lane >> 4));
    const int koct = w >> 1, sub = w & 1;
    unsigned offB, offE;
    {
        const int r = lane >> 3, swz = ((r >> 1) & 1) | ((koct & 1) << 1), c = (lane & 7) ^ (swz << 1);
        offB = (unsigned)((32 * sub + 8 * koct + r) * DM + 128 * nt + 8 * c) * 2u;
        const int re = ((lane >> 1) & 7) ^ ((koct & 1) << 2), ce = (lane & 1);
        offE = (unsigned)((nt * 5184 + 32 * sub + 8 * koct + re) * 16 + 8 * ce) * 2u;
    }
    const unsigned lds0 = (unsigned)(size_t)lds;
    const unsigned dB = lds0 + K2_RB + (2 * w) * 1024, dE = lds0 + K2_RE + (sub * 4 + koct) * 256;
    bf16x8 fa[4][2];
    auto issue1 = [&](int t, int sl, int i, bf16x8& a0, bf16x8& a1) {
        int T = T0 + t; if (t == K2_NT - 1 && split != 0) T -= 1;
        const u16* Wt = T < 72 ? Wd + (size_t)T * 64 * DM : wo + (size_t)(T - 72) * 64 * DM;
        const u16* ba = hb + (size_t)T * 2 * BLK_ELEMS;
        if (i == 0) glds16(Wt, offB, dB + sl * 16384);
        if (i == 1) glds16(Wt + 64, offB, dB + sl * 16384 + 1024);
        if (i == 2) glds16_q(dx + (size_t)T * 64 * 16, offE, dE + sl * 2048);
        if (i == 3) gload2(a0, a1, ba, srcA);
    };
    STAMP(2, 0);
#pragma unroll
    for (int t = 0; t < 3; ++t)
#pragma unroll
        for (int i = 0; i < 4; ++i) issue1(t, t, i, fa[t][0], fa[t][1]);

    const int fr = lane & 15, g = lane >> 4, q4 = (lane >> 2) & 3, p4 = lane & 3;
    const int swz = ((q4 >> 1) & 1) | ((g & 1) << 1);
    LAS char* pb[4];
#pragma unroll
    for (int u = 0; u < 4; ++u) pb[u] = lds + K2_RB + (g * 4 + kh * 2) * 1024 + q4 * 128 + ((u ^ swz) << 5) + p4 * 8;
    LAS char* pe0 = lds + K2_RE + (kh * 4 + g) * 256 + ((q4 ^ ((g & 1) << 2)) * 32) + p4 * 8;
    LAS char* pe1 = lds + K2_RE + (kh * 4 + g) * 256 + (((4 + q4) ^ ((g & 1) << 2)) * 32) + p4 * 8;
    auto rdB = [&](int sl, int nf, int t1) -> s16x4 {
        if (nf < 8) return tr_read(pb[nf & 3] + sl * 16384 + (nf >> 2) * 1024 + t1 * 512);
        return tr_read((t1 ? pe1 : pe0) + sl * 2048);
    };

    f32x4 acc[9][2];
#pragma unroll
    for (int n = 0; n < 9; ++n)
#pragma unroll
        for (int m = 0; m < 2; ++m) { acc[n][m] = (f32x4){0.f, 0.f, 0.f, 0.f}; asm volatile("" : "+v"(acc[n][m])); }

    wait_vm<10>(); BAR();
    STAMP(2, 1);
    constexpr int PF2 = 4;
    bf16x8 fb[9];
#pragma unroll
    for (int i = 0; i < PF2; ++i) { s16x4 lo = rdB(0, i, 0), hi = rdB(0, i, 1); fb[i] = __builtin_shufflevector(lo, hi, 0, 1, 2, 3, 4, 5, 6, 7); }
    auto step = [&](auto S_, int t, bool do_issue, bool wait0, auto LAST_) {
        constexpr int S = decltype(S_)::value;
        constexpr bool has_next = !decltype(LAST_)::value;
        constexpr int sl = S, sn = (S + 1) & 3;
        asm volatile("" : "+v"(fa[sl][0]), "+v"(fa[sl][1]));
        if (!has_next && split != 0) {
#pragma unroll
            for (int m = 0; m < 2; ++m) fa[sl][m] = (bf16x8){0, 0, 0, 0, 0, 0, 0, 0};
            asm volatile("s_nop 1" : "+v"(fa[sl][0]), "+v"(fa[sl][1]));
        }
#pragma unroll
        for (int nf = 0; nf < 9; ++nf) {
            const int psl = (nf + PF2 < 9) ? sl : sn, nn = (nf + PF2 < 9) ? nf + PF2 : nf + PF2 - 9;
            const bool pf = (nf + PF2 < 9) || has_next;
            s16x4 lo, hi;
            mfma_ip(acc[nf][0], fb[nf], fa[sl][0]);
            SCHED();
            if (nf == 0) {
                if (!wait0) wait_vm<5>(); else wait_vm<0>();
                BAR();
            }
            if (pf) lo = rdB(psl, nn, 0);
            SCHED();
            mfma_ip(acc[nf][1], fb[nf], fa[sl][1]);
            SCHED();
            if (pf) hi = rdB(psl, nn, 1);
            if (nf >= 1 && nf < 5 && do_issue) issue1(t + 3, (S + 3) & 3, nf - 1, fa[(S + 3) & 3][0], fa[(S + 3) & 3][1]);
            SCHED();
            if (pf) fb[nn] = __builtin_shufflevector(lo, hi, 0, 1, 2, 3, 4, 5, 6, 7);
        }
    };
    using I0 = std::integral_constant<int, 0>; using I1 = std::integral_constant<int, 1>;
    using I2 = std::integral_constant<int, 2>; using I3 = std::integral_constant<int, 3>;
#pragma unroll 1
    for (int t0 = 0; t0 < 20; t0 += 4) {
        step(I0{}, t0, true, false, std::false_type{});
        step(I1{}, t0 + 1, true, false, std::false_type{});
        step(I2{}, t0 + 2, t0 + 2 < 18, false, std::false_type{});
        step(I3{}, t0 + 3, t0 + 3 < 18, t0 + 3 == 19, std::false_type{});
    }
    step(I0{}, 20, false, true, std::true_type{});
    asm volatile("s_nop 7\n\ts_nop 7" ::: "memory");
    BAR();
    STAMP(2, 2);
    f32x4 val[10];
    {
        LAS char* xw = lds + w * 10240;
        LAS char* xr = lds + (w ^ 1) * 10240;
#pragma unroll
        for (int n = 0; n < 9; ++n) {
            const bool give = kh ? (n < 5) : (n >= 5);
            if (give) {
#pragma unroll
                for (int m = 0; m < 2; ++m) *(LAS f32x4*)(xw + ((kh ? n : n - 5) * 2 + m) * 1024 + lane * 16) = acc[n][m];
            }
        }
        asm volatile("s_waitcnt lgkmcnt(0)" ::: "memory");
        BAR();
#pragma unroll
        for (int i = 0; i < 10; ++i) {
            const f32x4 mine = kh ? acc[(5 + (i >> 1)) < 9 ? 5 + (i >> 1) : 8][i & 1] : acc[i >> 1][i & 1];
            val[i] = mine + *(LAS f32x4*)(xr + i * 1024 + lane * 16);
        }
    }
    {
        const int nit = kh ? 8 : 10;
        const int grp = mt * 4 + nt;
        const __amdgpu_buffer_rsrc_t rp = __builtin_amdgcn_make_buffer_rsrc((void*)part, 0, NSPLIT * 64 * 8 * 10 * 512, 0x00020000);
        const unsigned slot = (unsigned)((grp * 8 + w) * 10 * 512 + lane * 8);
        constexpr unsigned SPL = 64 * 8 * 10 * 512;
        u32x2 xv[3]; unsigned xoff[3];
#pragma unroll
        for (int q = 0; q < 3; ++q) {
            int i = split + 4 * q; i = i < nit ? i : split;
            const int n = kh ? 5 + (i >> 1) : (i >> 1), m = i & 1;
            const int row = 128 * mt + 32 * wm + 16 * m + fr;
            const int col = (n < 8 ? 128 * nt + 16 * n : 512 + 16 * nt) + 4 * g;
            xoff[q] = (unsigned)(row * DM + col);
            xv[q] = *(const u32x2*)(x + xoff[q]);
        }
#pragma unroll
        for (int i = 0; i < 10; ++i) {
            if (i < nit && (i & 3) != split) {
                u32x2 v; v[0] = pk_bf16(val[i][0], val[i][1]); v[1] = pk_bf16(val[i][2], val[i][3]);
                __builtin_amdgcn_raw_buffer_store_b64(v, rp, slot + split * SPL + i * 512, 0, 16);
            }
        }
        STAMP(2, 3);
        asm volatile("s_waitcnt vmcnt(0)" ::: "memory");
        BAR();
        STAMP(2, 4);
        unsigned* c = cnt + grp * 64;
        if (tid == 0) {
            __hip_atomic_fetch_add(c, 1u, __ATOMIC_RELAXED, __HIP_MEMORY_SCOPE_AGENT);
            unsigned spins = 0;
            while (__hip_atomic_load(c, __ATOMIC_RELAXED, __HIP_MEMORY_SCOPE_AGENT) < (unsigned)NSPLIT) {
                __builtin_amdgcn_s_sleep(1);
                if (++spins > (1u << 22)) break;
            }
        }
        BAR();
        STAMP(2, 5);
        u32x2 pv[3][3];
#pragma unroll
        for (int q = 0; q < 3; ++q) {
            const int i = split + 4 * q;
#pragma unroll
            for (int s2 = 1; s2 < NSPLIT; ++s2) {
                const int so = (split + s2) & 3;
                const unsigned off = i < nit ? slot + so * SPL + i * 512 : 0xfffffff0u;
                pv[q][s2 - 1] = __builtin_amdgcn_raw_buffer_load_b64(rp, off, 0, 16);
            }
        }
#pragma unroll
        for (int q = 0; q < 3; ++q) {
            const int i = split + 4 * q;
            if (i < nit) {
                f32x4 o = val[0];
#pragma unroll
                for (int ii = 0; ii < 10; ++ii) if (ii == i) o = val[ii];
#pragma unroll
                for (int s2 = 0; s2 < 3; ++s2) { o[0] += bf_lo(pv[q][s2][0]); o[1] += bf_hi(pv[q][s2][0]); o[2] += bf_lo(pv[q][s2][1]); o[3] += bf_hi(pv[q][s2][1]); }
                u32x2 v; v[0] = pk_bf16(o[0] + bf_lo(xv[q][0]), o[1] + bf_hi(xv[q][0])); v[1] = pk_bf16(o[2] + bf_lo(xv[q][1]), o[3] + bf_hi(xv[q][1]));
                *(u32x2*)(out + xoff[q]) = v;
            }
        }
    }
    STAMP(2, 15);
}

extern "C" void kernel_launch(void* const* d_in, const int* in_sizes, int n_in, void* d_out, int out_size, void* d_ws,
                              size_t ws_size, hipStream_t stream) {
    const u16* x = (const u16*)d_in[0];
    const u16* wqkv = (const u16*)d_in[1];
    const u16* wo = (const u16*)d_in[2];
    const u16* Wg = (const u16*)d_in[4];
    const u16* Wu = (const u16*)d_in[5];
    const u16* Wd = (const u16*)d_in[6];
    char* ws = (char*)d_ws;
    u16* xnb = (u16*)ws;
    u16* hb = (u16*)(ws + (4u << 20));
    u16* part = (u16*)(ws + (32u << 20));
    unsigned* cnt = (unsigned*)(ws + (56u << 20));
    u16* gx = (u16*)(ws + (52u << 20));
    u16* dx = (u16*)(ws + (54u << 20));
    u16* wqh = (u16*)(ws + (48u << 20));
    (void)hipFuncSetAttribute((const void*)k1_gateup, hipFuncAttributeMaxDynamicSharedMemorySize, K1_LDS);
    (void)hipFuncSetAttribute((const void*)k2_down, hipFuncAttributeMaxDynamicSharedMemorySize, K2_LDS);
    k0_ln<<<256, 256, 0, stream>>>(x, xnb, wqkv, wqh, Wg, Wu, gx, Wd, wo, dx);
    k1_gateup<<<256, 512, K1_LDS, stream>>>(xnb, Wg, Wu, wqh, gx, Wd, wo, hb, cnt);
    k2_down<<<256, 512, K2_LDS, stream>>>(hb, Wd, wo, dx, part, cnt, x, (u16*)d_out);
}
```

```cpp
#define NO_TOUCH
#include <hip/hip_runtime.h>
#include <hip/hip_bf16.h>
#include <stdint.h>
#include <type_traits>

typedef unsigned short u16;
typedef short bf16x8 __attribute__((ext_vector_type(8)));
typedef short s16x4 __attribute__((ext_vector_type(4)));
typedef float f32x4 __attribute__((ext_vector_type(4)));
typedef float f32x2 __attribute__((ext_vector_type(2)));
typedef __bf16 bf16x2_t __attribute__((ext_vector_type(2)));
typedef unsigned u32x4 __attribute__((ext_vector_type(4)));
typedef unsigned u32x2 __attribute__((ext_vector_type(2)));
#define LAS __attribute__((address_space(3)))
#define GAS __attribute__((address_space(1)))

constexpr int SEQ = 2048, DM = 576, FF = 4608;
constexpr int NKB_X = 18;
constexpr int NKB_H = 162;
constexpr size_t BLK_ELEMS = (size_t)SEQ * 32;

constexpr size_t WS_HB = 18 * 131072, WS_PART = WS_HB + 162 * 131072, WS_WQH = WS_PART + 4 * 64 * 8 * 10 * 512, WS_GX = WS_WQH + 32 * 576 * 64 * 2,
                 WS_DX = WS_GX + 2 * 32 * 576 * 16 * 2, WS_CNT = WS_DX + 4 * 5184 * 16 * 2, WS_END = WS_CNT + 64 * 256;

__device__ __forceinline__ unsigned pk_bf16(float lo, float hi) {
    f32x2 v = {lo, hi};
    bf16x2_t b = __builtin_convertvector(v, bf16x2_t);
    return __builtin_bit_cast(unsigned, b);
}
__device__ __forceinline__ float bf_lo(unsigned u) { return __uint_as_float(u << 16); }
__device__ __forceinline__ float bf_hi(unsigned u) { return __uint_as_float(u & 0xffff0000u); }
__device__ __forceinline__ float rbf(float x) { return bf_lo(pk_bf16(x, x)); }

__device__ __forceinline__ const void* uni(const void* p) {
    const unsigned long long v = (unsigned long long)p;
    const unsigned lo = __builtin_amdgcn_readfirstlane((unsigned)v), hi = __builtin_amdgcn_readfirstlane((unsigned)(v >> 32));
    return (const void*)(((unsigned long long)hi << 32) | lo);
}
__device__ __forceinline__ void glds16(const void* sbase, unsigned voff, unsigned lds) {
    sbase = uni(sbase); lds = __builtin_amdgcn_readfirstlane(lds);
    asm volatile("s_mov_b32 m0, %2\n\ts_nop 3\n\tglobal_load_lds_dwordx4 %0, %1"
                 :: "v"(voff), "s"(sbase), "s"(lds) : "memory", "m0");
}
__device__ __forceinline__ void glds16_o1k(const void* sbase, unsigned voff, unsigned lds) {
    sbase = uni(sbase); lds = __builtin_amdgcn_readfirstlane(lds);
    asm volatile("s_mov_b32 m0, %2\n\ts_nop 3\n\tglobal_load_lds_dwordx4 %0, %1 offset:1024"
                 :: "v"(voff), "s"(sbase), "s"(lds) : "memory", "m0");
}
__device__ __forceinline__ void glds16_q(const void* sbase, unsigned voff, unsigned lds) {
    sbase = uni(sbase); lds = __builtin_amdgcn_readfirstlane(lds);
    unsigned sv; unsigned long long ex;
    asm volatile("s_mov_b32 %0, m0\n\ts_mov_b64 %1, exec\n\ts_mov_b32 m0, %4\n\ts_mov_b64 exec, 0xffff\n\ts_nop 1\n\tglobal_load_lds_dwordx4 %2, %3\n\ts_mov_b64 exec, %1\n\ts_mov_b32 m0, %0"
                 : "=&s"(sv), "=&s"(ex) : "v"(voff), "s"(sbase), "s"(lds) : "memory");
}
__device__ __forceinline__ void glds16_m(const void* sbase, unsigned voff, unsigned lds) {
    sbase = uni(sbase); lds = __builtin_amdgcn_readfirstlane(lds);
    unsigned sv; unsigned long long ex;
    asm volatile("s_mov_b32 %0, m0\n\ts_mov_b64 %1, exec\n\ts_mov_b32 m0, %4\n\ts_mov_b32 exec_lo, -1\n\ts_mov_b32 exec_hi, 0xff\n\ts_nop 1\n\tglobal_load_lds_dwordx4 %2, %3\n\ts_mov_b64 exec, %1\n\ts_mov_b32 m0, %0"
                 : "=&s"(sv), "=&s"(ex) : "v"(voff), "s"(sbase), "s"(lds) : "memory");
}
#define SCHED() __builtin_amdgcn_sched_barrier(0)
#define STAMP(kid, i) do {} while (0)
#define WSTAMP(i) do {} while (0)
__device__ __forceinline__ s16x4 tr_read(LAS char* p) {
    return __builtin_bit_cast(s16x4, __builtin_amdgcn_ds_read_tr16_b64_v4i16((LAS s16x4*)p));
}
__device__ __forceinline__ bf16x8 tr_frag(LAS char* p, int off_t1) {
    s16x4 lo = tr_read(p), hi = tr_read(p + off_t1);
    return __builtin_shufflevector(lo, hi, 0, 1, 2, 3, 4, 5, 6, 7);
}
__device__ __forceinline__ bf16x8 row_frag(LAS char* p) { return *(LAS bf16x8*)p; }
#define MFMA16(a, b, c) __builtin_amdgcn_mfma_f32_16x16x32_bf16((a), (b), (c), 0, 0, 0)
__device__ __forceinline__ void mfma_ip(f32x4& c, bf16x8 a, bf16x8 b) { asm volatile("v_mfma_f32_16x16x32_bf16 %0, %1, %2, %0" : "+v"(c) : "v"(a), "v"(b)); }
#define WAIT_V(n) asm volatile("s_waitcnt vmcnt(" #n ")" ::: "memory")
#define BAR() do { asm volatile("" ::: "memory"); __builtin_amdgcn_s_barrier(); asm volatile("" ::: "memory"); } while (0)

__device__ __forceinline__ int blk_off(int row, int chunk) { return row * 64 + ((chunk ^ (((row >> 3) & 1) << 1)) << 4); }

__global__ __launch_bounds__(256) void k0_ln(const u16* __restrict__ x, u16* __restrict__ xnb,
                                            const u16* __restrict__ wqkv, u16* __restrict__ wqh,
                                            const u16* __restrict__ Wg, const u16* __restrict__ Wu, u16* __restrict__ gx,
                                            const u16* __restrict__ Wd, const u16* __restrict__ wo, u16* __restrict__ dx,
                                            const char* __restrict__ wsb, const char* __restrict__ outb) {
    if (threadIdx.x >= 64) {
        const int l = threadIdx.x - 64, head = blockIdx.x >> 3, k0 = (blockIdx.x & 7) * 72;
        unsigned v[12];
#pragma unroll
        for (int i = 0; i < 12; ++i) {
            const int id = l + 192 * i, k = k0 + (id >> 5), cp = id & 31;
            const int part = cp >= 18 ? 2 : (cp >= 9 ? 1 : 0), d2 = cp - part * 9;
            const int src = (part == 2 ? 0 : part + 1) * DM + 18 * head + 2 * d2;
            v[i] = cp < 27 ? *(const unsigned*)(wqkv + (size_t)k * (3 * DM) + src) : 0u;
        }
        u32x4 c[3];
#pragma unroll
        for (int i = 0; i < 2; ++i) {
            const int id = l + 192 * i;
            if (id < 288) {
                const int gid = blockIdx.x * 288 + id, mat = gid / 36864, r = gid - mat * 36864, k = r >> 6, ch = r & 63;
                c[i] = *(const u32x4*)((mat ? Wu : Wg) + (size_t)k * FF + 4096 + 8 * ch);
            }
        }
        if (l < 162) {
            const int gid = blockIdx.x * 162 + l, k = gid >> 3, ch = gid & 7;
            c[2] = *(const u32x4*)((k < FF ? Wd + (size_t)k * DM : wo + (size_t)(k - FF) * DM) + 512 + 8 * ch);
        }
#pragma unroll
        for (int i = 0; i < 12; ++i) {
            const int id = l + 192 * i;
            *(unsigned*)(wqh + ((size_t)head * 576 + k0) * 64 + 2 * id) = v[i];
        }
#pragma unroll
        for (int i = 0; i < 2; ++i) {
            const int id = l + 192 * i;
            if (id < 288) {
                const int gid = blockIdx.x * 288 + id, mat = gid / 36864, r = gid - mat * 36864, k = r >> 6, ch = r & 63;
                *(u32x4*)(gx + (((size_t)mat * 32 + (ch >> 1)) * 576 + k) * 16 + 8 * (ch & 1)) = c[i];
            }
        }
        if (l < 162) {
            const int gid = blockIdx.x * 162 + l, k = gid >> 3, ch = gid & 7;
            *(u32x4*)(dx + ((size_t)(ch >> 1) * 5184 + k) * 16 + 8 * (ch & 1)) = c[2];
        }
        return;
    }
    const int lane = threadIdx.x, i = lane & 7, row = blockIdx.x * 8 + (lane >> 3);
    u32x4 xv[9];
#pragma unroll
    for (int cc = 0; cc < 9; ++cc) xv[cc] = *(const u32x4*)(x + (size_t)row * DM + (i + 8 * cc) * 8);
    float s = 0.f;
#pragma unroll
    for (int cc = 0; cc < 9; ++cc)
#pragma unroll
        for (int e = 0; e < 4; ++e) s += bf_lo(xv[cc][e]) + bf_hi(xv[cc][e]);
    s += __shfl_xor(s, 1); s += __shfl_xor(s, 2); s += __shfl_xor(s, 4);
    const float mean = s * (1.0f / 576.0f);
    float q = 0.f;
#pragma unroll
    for (int cc = 0; cc < 9; ++cc)
#pragma unroll
        for (int e = 0; e < 4; ++e) { const float a = bf_lo(xv[cc][e]) - mean, c2 = bf_hi(xv[cc][e]) - mean; q += a * a + c2 * c2; }
    q += __shfl_xor(q, 1); q += __shfl_xor(q, 2); q += __shfl_xor(q, 4);
    const float mb = rbf(mean), vb = rbf(q * (1.0f / 576.0f));
    const float sb = rbf(sqrtf(rbf(vb + 1e-5f)));
    const float rs = 1.0f / sb;
#pragma unroll
    for (int cc = 0; cc < 9; ++cc) {
        u32x4 o;
#pragma unroll
        for (int e = 0; e < 4; ++e) {
            const unsigned d = pk_bf16(bf_lo(xv[cc][e]) - mb, bf_hi(xv[cc][e]) - mb);
            o[e] = pk_bf16(bf_lo(d) * rs, bf_hi(d) * rs);
        }
        const int blk = 2 * cc + (i >> 2);
        *(u32x4*)((char*)xnb + (size_t)blk * (BLK_ELEMS * 2) + blk_off(row, i & 3)) = o;
    }
}

constexpr int RA = 0, RB = 65536, RE = 131072, RQ = 139264, RH = 155648;
constexpr int T_DUMMY = 159744;
constexpr int K1_LDS = T_DUMMY + 1024;
constexpr int QS = 144;
#define HST_AUX 16
#define PF 3
constexpr int NB = PF + 1;
template <int N> __device__ __forceinline__ void wait_vm() { asm volatile("s_waitcnt vmcnt(%0)" :: "i"(N) : "memory"); }
#define GLDS_MASKED(NAME, LO, HI) \
__device__ __forceinline__ void NAME(const void* sbase, unsigned voff, unsigned lds) { \
    sbase = uni(sbase); lds = __builtin_amdgcn_readfirstlane(lds); \
    unsigned sv; unsigned long long ex; \
    asm volatile("s_mov_b32 %0, m0\n\ts_mov_b64 %1, exec\n\ts_mov_b32 m0, %4\n\ts_mov_b32 exec_lo, " #LO "\n\ts_mov_b32 exec_hi, " #HI "\n\tglobal_load_lds_dwordx4 %2, %3\n\ts_mov_b64 exec, %1\n\ts_mov_b32 m0, %0" \
                 : "=&s"(sv), "=&s"(ex) : "v"(voff), "s"(sbase), "s"(lds) : "memory"); }
GLDS_MASKED(glds16_32, -1, 0)
GLDS_MASKED(glds16_8, 0xff, 0)

__device__ __forceinline__ void k1_epilogue(f32x4 (&acc)[11][4], f32x4& ach, LAS char* lds, unsigned lds0, int w, int xcd, int idx,
                                            int j, int mt, const u16* __restrict__ Wd, const u16* __restrict__ wo, u16* __restrict__ hb) {
    int tid = threadIdx.x; asm volatile("" : "+v"(tid));
    const int lane = tid & 63, fr = lane & 15, g = lane >> 4;
    const int wm = w >> 1, wn = w & 1;
    const __amdgpu_buffer_rsrc_t rh = __builtin_amdgcn_make_buffer_rsrc((void*)hb, 0, (int)(NKB_H * BLK_ELEMS * 2), 0x00020000);
    auto hs = [&](int m, int part) {
        const int row = 256 * mt + 64 * wm + 16 * m + fr;
        SCHED();
        if (part < 2) {
            const int pp = part;
            unsigned v[2][2];
#pragma unroll
            for (int hh = 0; hh < 2; ++hh) {
                const int gt = 2 * pp + hh;
                float hv[4];
#pragma unroll
                for (int e = 0; e < 4; ++e) hv[e] = fmaxf(acc[2 * gt][m][e], 0.f) * acc[2 * gt + 1][m][e];
                v[hh][0] = pk_bf16(hv[0], hv[1]); v[hh][1] = pk_bf16(hv[2], hv[3]);
            }
            auto r0 = __builtin_amdgcn_permlane16_swap(v[0][0], v[1][0], false, false);
            auto r1 = __builtin_amdgcn_permlane16_swap(v[0][1], v[1][1], false, false);
            u32x4 o; o[0] = r0[0]; o[1] = r1[0]; o[2] = r0[1]; o[3] = r1[1];
            const int blk = 4 * j + 2 * wn + pp, chunk = ((g & 1) << 1) | (g >> 1);
            __builtin_amdgcn_raw_buffer_store_b128(o, rh, (unsigned)(blk * (int)(BLK_ELEMS * 2) + blk_off(row, chunk)), 0, HST_AUX);
        } else {
            float hv[4];
#pragma unroll
            for (int e = 0; e < 4; ++e) {
                const unsigned mine = __float_as_uint(acc[8][m][e]);
                auto r = __builtin_amdgcn_permlane32_swap(mine, mine, false, false);
                hv[e] = fmaxf(__uint_as_float(r[0]), 0.f) * __uint_as_float(r[1]);
            }
            u32x2 v; v[0] = pk_bf16(hv[0], hv[1]); v[1] = pk_bf16(hv[2], hv[3]);
            const int blk = 128 + (j >> 1), chunk = (j & 1) * 2 + wn;
            const unsigned off = g < 2 ? (unsigned)(blk * (int)(BLK_ELEMS * 2) + blk_off(row, chunk) + g * 8) : 0xfffffff0u;
            __builtin_amdgcn_raw_buffer_store_b64(v, rh, off, 0, HST_AUX);
        }
        SCHED();
    };
    hs(0, 0);
    BAR();
    STAMP(1, 2);
    LAS char* qt = lds;
    {
#pragma unroll
        for (int u = 0; u < 2; ++u) {
            const int nn = 16 * (2 * wn + (u ^ (wm & 1))) + 4 * g;
            const int h0 = nn / 18, h1 = (nn + 2) / 18;
            const int o0 = h0 * 48 + (nn - h0 * 18) * 2, o1 = h1 * 48 + (nn + 2 - h1 * 18) * 2;
#pragma unroll
            for (int m = 0; m < 4; ++m) {
                LAS char* rp = qt + (16 + 64 * wm + 16 * m + fr) * QS;
                if (h0 < 3) *(LAS unsigned*)(rp + o0) = pk_bf16(acc[9 + u][m][0], acc[9 + u][m][1]);
                if (h1 < 3) *(LAS unsigned*)(rp + o1) = pk_bf16(acc[9 + u][m][2], acc[9 + u][m][3]);
            }
            if (u == 0 && (w == 0 || w == 2 || w == 1)) {
                LAS char* rp = qt + fr * QS;
                if (h0 < 3) *(LAS unsigned*)(rp + o0) = pk_bf16(ach[0], ach[1]);
                if (h1 < 3) *(LAS unsigned*)(rp + o1) = pk_bf16(ach[2], ach[3]);
            }
            if (u == 0) hs(0, 1);
        }
    }
    asm volatile("s_waitcnt lgkmcnt(0)" ::: "memory");
    BAR();
    STAMP(1, 3);
    {
        const int s = tid >> 1, half = tid & 1;
        const int lrow = 16 + s, grow = 256 * mt + s;
        auto ld18 = [&](LAS const char* p, unsigned (&v)[9]) {
            const u32x4 a = *(LAS const u32x4*)p, c = *(LAS const u32x4*)(p + 16);
            v[0] = a[0]; v[1] = a[1]; v[2] = a[2]; v[3] = a[3]; v[4] = c[0]; v[5] = c[1]; v[6] = c[2]; v[7] = c[3];
            v[8] = *(LAS const unsigned*)(p + 32);
        };
        hs(0, 2);
        unsigned qv[9];
        ld18(qt + lrow * QS + 96, qv);
        float lg[4];
#pragma unroll
        for (int ww = 0; ww < 4; ++ww) {
            const int wi = 4 * half + ww;
            unsigned kv[9];
            ld18(qt + (lrow - 2 * wi) * QS, kv);
            float a = 0.f;
#pragma unroll
            for (int i = 0; i < 9; ++i) a = __builtin_amdgcn_fdot2_f32_bf16(__builtin_bit_cast(bf16x2_t, qv[i]), __builtin_bit_cast(bf16x2_t, kv[i]), a, false);
            a = rbf(rbf(a) * 0.23570226039551584f);
            lg[ww] = (grow - 2 * wi >= 0) ? a : -1e9f;
            if (ww == 0) hs(1, 0);
            if (ww == 2) hs(1, 1);
        }
        float mx = fmaxf(fmaxf(lg[0], lg[1]), fmaxf(lg[2], lg[3]));
        mx = fmaxf(mx, __shfl_xor(mx, 1));
        float ex[4], sum = 0.f;
#pragma unroll
        for (int ww = 0; ww < 4; ++ww) { ex[ww] = __expf(lg[ww] - mx); sum += ex[ww]; }
        sum += __shfl_xor(sum, 1);
        const float inv = 1.0f / sum;
        hs(1, 2);
        float o[18];
#pragma unroll
        for (int i = 0; i < 18; ++i) o[i] = 0.f;
#pragma unroll
        for (int ww = 0; ww < 4; ++ww) {
            const int wi = 4 * half + ww;
            const float pw = (grow - 2 * wi >= 0) ? rbf(ex[ww] * inv) : 0.f;
            unsigned vv[9];
            ld18(qt + (lrow - 2 * wi) * QS + 48, vv);
#pragma unroll
            for (int i = 0; i < 9; ++i) { o[2 * i] += pw * bf_lo(vv[i]); o[2 * i + 1] += pw * bf_hi(vv[i]); }
            if (ww < 3) hs(2, ww);
        }
        hs(3, 0);
#pragma unroll
        for (int ii = 0; ii < 5; ++ii) {
            const float s0 = half ? o[2 * ii] : o[(10 + 2 * ii) % 18], s1 = half ? o[2 * ii + 1] : o[(11 + 2 * ii) % 18];
            const float r0 = __shfl_xor(s0, 1), r1 = __shfl_xor(s1, 1);
            const float m0 = half ? o[(10 + 2 * ii) % 18] : o[2 * ii], m1 = half ? o[(11 + 2 * ii) % 18] : o[2 * ii + 1];
            const int i = half * 5 + ii;
            if (i < 9) {
                const int col = FF + 18 * j + 2 * i;
                *(unsigned*)((char*)hb + (size_t)(col >> 5) * (BLK_ELEMS * 2) + blk_off(grow, (col >> 3) & 3) + (col & 7) * 2) = pk_bf16(m0 + r0, m1 + r1);
            }
            if (ii == 2) hs(3, 1);
        }
        hs(3, 2);
    }
    STAMP(1, 15);
}

__global__ __launch_bounds__(512, 2) void k1_gateup(const u16* __restrict__ xnb, const u16* __restrict__ Wg,
                                                    const u16* __restrict__ Wu, const u16* __restrict__ wqh, const u16* __restrict__ gx,
                                                    const u16* __restrict__ Wd, const u16* __restrict__ wo, u16* __restrict__ hb, unsigned* __restrict__ cnt) {
    extern __shared__ __attribute__((aligned(16))) char smem[];
    LAS char* lds = (LAS char*)smem;
    const int tid = threadIdx.x, lane = tid & 63;
    if (blockIdx.x < 64 && tid == 0) cnt[blockIdx.x * 64] = 0u;
    const int w = __builtin_amdgcn_readfirstlane(tid >> 6);
    const int b = blockIdx.x, xcd = b & 7, idx = b >> 3;
    const int j = 4 * xcd + (idx & 3), mt = idx >> 2;
    const int wm = w >> 1, wn = w & 1;
    const unsigned lds0 = (unsigned)(size_t)lds;
    const int fr = lane & 15, g = lane >> 4, q4 = (lane >> 2) & 3, p4 = lane & 3;

    f32x4 acc[11][4], ach;
    {
        const unsigned srcA = (unsigned)((256 * mt + 32 * w) * 32 + lane * 8) * 2u;
        const int koct = w >> 1, mat = w & 1;
        const u16* W = mat ? Wu : Wg;
        unsigned srcB, srcX;
        const u16* baseX; int strideX; unsigned dX;
        {
            const int r = lane >> 3, swz = ((r >> 1) & 1) | ((koct & 1) << 1), c = (lane & 7) ^ (swz << 1);
            srcB = (unsigned)((8 * koct + r) * FF + 128 * j + 8 * c) * 2u;
            if (w < 2) {
                const int ko = lane >> 4, re = ((lane >> 1) & 7) ^ ((ko & 1) << 2), ce = (lane & 1) ^ w;
                srcX = (unsigned)(((w * 32 + j) * 576 + 8 * ko + re) * 16 + 8 * ce) * 2u;
                baseX = gx; strideX = 32 * 16; dX = lds0 + RE + w * 1024;
            } else if (w < 6) {
                const int ko = w - 2, swq = ((r >> 1) & 1) | ((ko & 1) << 1), cq = (lane & 7) ^ (swq << 1);
                srcX = (unsigned)((j * 576 + 8 * ko + r) * 64 + 8 * cq) * 2u;
                baseX = wqh; strideX = 32 * 64; dX = lds0 + RQ + ko * 1024;
            } else {
                const int hr = mt > 0 ? 256 * mt - 16 : 0;
                srcX = (unsigned)(hr * 64 + lane * 16);
                baseX = xnb; strideX = (int)BLK_ELEMS; dX = lds0 + RH;
            }
        }
        const int slotX = w < 2 ? 2048 : (w < 6 ? 4096 : 1024);
        const unsigned dA = lds0 + RA + (2 * w) * 1024, dB = lds0 + RB + (2 * w) * 1024;
        auto issue1 = [&](int t, int sl, int i) {
            const u16* ba = xnb + (size_t)t * BLK_ELEMS;
            const u16* bw = W + (size_t)t * 32 * FF;
            if (i == 0) glds16(bw, srcB, dB + sl * 16384);
            if (i == 1) glds16(bw + 64, srcB, dB + sl * 16384 + 1024);
            if (i == 2) glds16(baseX + (size_t)t * strideX, srcX, dX + sl * slotX);
            if (i == 3) glds16(ba, srcA, dA + sl * 16384);
            if (i == 4) glds16_o1k(ba, srcA, dA + sl * 16384);
        };
        auto prefetch = [&](int p) {
            const int split = xcd >> 1;
            const unsigned start = (unsigned)((split == 0 ? 0 : 1344 + 1280 * (split - 1)) * DM * 2);
            const unsigned len = (unsigned)((split == 0 ? 1344 : 1280) * DM * 2), lwd = (unsigned)(704 * DM * 2);
            p = p < 6 ? p : 5;
            unsigned o = (unsigned)((idx * 8 + w) * 6144 + p * 1024); o = o + 1024 <= len ? o : 0u;
            const char* src = (split == 3 && o >= lwd) ? (const char*)wo + (o - lwd) : (const char*)Wd + start + o;
            glds16(src, (unsigned)lane * 16u, lds0 + T_DUMMY);
        };
        STAMP(1, 0);
#pragma unroll
        for (int t = 0; t < 3; ++t) {
#pragma unroll
            for (int i = 0; i < 5; ++i) issue1(t, t, i);
            if (t == 1) prefetch(0);
        }

        const unsigned pa = lds0 + RA + blk_off(64 * wm + fr, g);
        const int swz = ((q4 >> 1) & 1) | ((g & 1) << 1);
        const unsigned pb0 = lds0 + RB + (g * 4 + wn) * 1024 + q4 * 128 + (swz << 5) + p4 * 8;
        const int emat = p4 >> 1;
        const unsigned pe0 = lds0 + RE + (emat * 4 + g) * 256 + ((q4 ^ ((g & 1) << 2)) * 32) + ((wn ^ emat) * 16) + (p4 & 1) * 8;
        const unsigned pq0 = lds0 + RQ + g * 1024 + q4 * 128 + (((2 * wn + (wm & 1)) ^ swz) << 5) + p4 * 8;
        LAS char* pbv[4]; LAS char* pqv[2];
#pragma unroll
        for (int u = 0; u < 4; ++u) pbv[u] = (LAS char*)(size_t)(pb0 ^ (u << 5));
        pqv[0] = (LAS char*)(size_t)pq0; pqv[1] = (LAS char*)(size_t)(pq0 ^ 32);
        LAS char* pev[2] = {(LAS char*)(size_t)pe0, (LAS char*)(size_t)(pe0 ^ 128)};
        LAS char* phv = lds + RH + blk_off(fr, g);
        auto rdB = [&](int sl, int nf, int t1) -> s16x4 {
            if (nf < 8) return tr_read(pbv[nf >> 1] + sl * 16384 + (nf & 1) * 2048 + t1 * 512);
            if (nf == 8) return tr_read(pev[t1] + sl * 2048);
            return tr_read(pqv[nf - 9] + sl * 4096 + t1 * 512);
        };
        auto rdA = [&](int sl, int m) -> bf16x8 { return row_frag((LAS char*)(size_t)pa + sl * 16384 + m * 1024); };
        auto rdH = [&](int sl) -> bf16x8 { return row_frag(phv + sl * 1024); };
#pragma unroll
        for (int n = 0; n < 11; ++n)
#pragma unroll
            for (int m = 0; m < 4; ++m) { acc[n][m] = (f32x4){0.f, 0.f, 0.f, 0.f}; asm volatile("" : "+v"(acc[n][m])); }
        ach = (f32x4){0.f, 0.f, 0.f, 0.f}; asm volatile("" : "+v"(ach));

        wait_vm<11>(); BAR();
        STAMP(1, 1);
        bf16x8 fa[2][2], fc[2], fh, fb[NB];
#pragma unroll
        for (int m = 0; m < 2; ++m) { fa[0][m] = rdA(0, m); fc[m] = rdA(0, 2 + m); }
#pragma unroll
        for (int i = 0; i < PF; ++i) { s16x4 lo = rdB(0, i, 0), hi = rdB(0, i, 1); fb[i] = __builtin_shufflevector(lo, hi, 0, 1, 2, 3, 4, 5, 6, 7); }
#define MM(C, A, B) mfma_ip(C, A, B)
        auto step = [&](auto S_, int t, bool do_issue, auto LAST1_, auto WAIT0_) {
            constexpr int S = decltype(S_)::value;
            constexpr bool has_next = !decltype(LAST1_)::value;
            constexpr bool wait0 = decltype(WAIT0_)::value;
            constexpr int sl = S, sn = (S + 1) & 3, cur = S & 1;
#pragma unroll
            for (int nf = 0; nf < 11; ++nf) {
                constexpr int dummy = 0; (void)dummy;
                const int r = 11 * S + nf;
                const int psl = (nf + PF < 11) ? sl : sn, nn = (nf + PF < 11) ? nf + PF : nf + PF - 11;
                const bool pf = (nf + PF < 11) || has_next;
                const bool lastg = (nf == 10);
                s16x4 lo, hi;
                if (!lastg) MM(acc[nf][0], fb[r % NB], fa[cur][0]); else MM(acc[nf][2], fb[r % NB], fc[0]);
                SCHED();
                if (nf == 0) {
                    if (!wait0) wait_vm<6>(); else wait_vm<0>();
                    BAR();
                }
                if (pf) lo = rdB(psl, nn, 0);
                SCHED();
                if (!lastg) MM(acc[nf][1], fb[r % NB], fa[cur][1]); else MM(acc[nf][3], fb[r % NB], fc[1]);
                SCHED();
                if (pf) hi = rdB(psl, nn, 1);
                if (lastg && has_next) fc[0] = rdA(sn, 2);
                SCHED();
                if (!lastg) MM(acc[nf][2], fb[r % NB], fc[0]); else MM(acc[nf][0], fb[r % NB], fa[cur][0]);
                SCHED();
                if (lastg && has_next) fc[1] = rdA(sn, 3);
                if (nf >= 4 && nf < 6 && has_next) fa[cur ^ 1][nf - 4] = rdA(sn, nf - 4);
                if (nf == 7) fh = rdH(sl);
                if ((nf < 4 || nf == 6) && do_issue) issue1(t + 3, (S + 3) & 3, nf < 4 ? nf : 4);
                if (nf == 8 && (S & 1) == 0 && do_issue) prefetch(1 + (t >> 1));
                SCHED();
                if (!lastg) MM(acc[nf][3], fb[r % NB], fc[1]); else MM(acc[nf][1], fb[r % NB], fa[cur][1]);
                SCHED();
                if (nf == 9) { MM(ach, fb[r % NB], fh); SCHED(); }
                if (pf) fb[(r + PF) % NB] = __builtin_shufflevector(lo, hi, 0, 1, 2, 3, 4, 5, 6, 7);
            }
        };
        using I0 = std::integral_constant<int, 0>; using I1 = std::integral_constant<int, 1>;
        using I2 = std::integral_constant<int, 2>; using I3 = std::integral_constant<int, 3>;
        using F = std::false_type; using T = std::true_type;
#pragma unroll 1
        for (int t0 = 0; t0 < 16; t0 += 4) {
            step(I0{}, t0, true, F{}, F{});
            step(I1{}, t0 + 1, true, F{}, F{});
            step(I2{}, t0 + 2, true, F{}, F{});
            step(I3{}, t0 + 3, t0 + 3 < 15, F{}, F{});
        }
        step(I0{}, 16, false, F{}, T{});
        step(I1{}, 17, false, T{}, T{});
    }
    asm volatile("s_nop 7\n\ts_nop 7" ::: "memory");
    STAMP(1, 4);
    k1_epilogue(acc, ach, lds, lds0, w, xcd, idx, j, mt, Wd, wo, hb);
}

constexpr int K2_NT = 21;
constexpr int K2_RB = 0, K2_RE = 65536, K2_LDS = 81920;
constexpr int NSPLIT = 4;
__device__ __forceinline__ void gload2(bf16x8& d0, bf16x8& d1, const void* sbase, unsigned voff) {
    sbase = uni(sbase);
    asm volatile("s_nop 4\n\tglobal_load_dwordx4 %0, %2, %3\n\tglobal_load_dwordx4 %1, %2, %3 offset:1024"
                 : "=&v"(d0), "=&v"(d1) : "v"(voff), "s"(sbase) : "memory");
}
__global__ __launch_bounds__(512, 2) void k2_down(const u16* __restrict__ hb, const u16* __restrict__ Wd,
                                                  const u16* __restrict__ wo, const u16* __restrict__ dx, u16* __restrict__ part, unsigned* __restrict__ cnt,
                                                  const u16* __restrict__ x, u16* __restrict__ out) {
    extern __shared__ __attribute__((aligned(16))) char smem[];
    LAS char* lds = (LAS char*)smem;
    const int tid = threadIdx.x, lane = tid & 63;
    const int w = __builtin_amdgcn_readfirstlane(tid >> 6);
    const int b = blockIdx.x, xcd = b & 7, idx = b >> 3;
    const int split = xcd >> 1, mt = (xcd & 1) * 8 + (idx >> 2), nt = idx & 3;
    const int wm = w >> 1, kh = w & 1;
    const int T0 = split == 0 ? 0 : 21 + 20 * (split - 1);

    const unsigned srcA = (unsigned)(kh * (int)BLK_ELEMS * 2 + blk_off(128 * mt + 32 * wm + (lane & 15), lane >> 4));
    const int koct = w >> 1, sub = w & 1;
    unsigned offB, offE;
    {
        const int r = lane >> 3, swz = ((r >> 1) & 1) | ((koct & 1) << 1), c = (lane & 7) ^ (swz << 1);
        offB = (unsigned)((32 * sub + 8 * koct + r) * DM + 128 * nt + 8 * c) * 2u;
        const int re = ((lane >> 1) & 7) ^ ((koct & 1) << 2), ce = (lane & 1);
        offE = (unsigned)((nt * 5184 + 32 * sub + 8 * koct + re) * 16 + 8 * ce) * 2u;
    }
    const unsigned lds0 = (unsigned)(size_t)lds;
    const unsigned dB = lds0 + K2_RB + (2 * w) * 1024, dE = lds0 + K2_RE + (sub * 4 + koct) * 256;
    bf16x8 fa[4][2];
    auto issue1 = [&](int t, int sl, int i, bf16x8& a0, bf16x8& a1) {
        int T = T0 + t; if (t == K2_NT - 1 && split != 0) T -= 1;
        const u16* Wt = T < 72 ? Wd + (size_t)T * 64 * DM : wo + (size_t)(T - 72) * 64 * DM;
        const u16* ba = hb + (size_t)T * 2 * BLK_ELEMS;
        if (i == 0) glds16(Wt, offB, dB + sl * 16384);
        if (i == 1) glds16(Wt + 64, offB, dB + sl * 16384 + 1024);
        if (i == 2) glds16_q(dx + (size_t)T * 64 * 16, offE, dE + sl * 2048);
        if (i == 3) gload2(a0, a1, ba, srcA);
    };
    STAMP(2, 0);
#pragma unroll
    for (int t = 0; t < 3; ++t)
#pragma unroll
        for (int i = 0; i < 4; ++i) issue1(t, t, i, fa[t][0], fa[t][1]);

    const int fr = lane & 15, g = lane >> 4, q4 = (lane >> 2) & 3, p4 = lane & 3;
    const int swz = ((q4 >> 1) & 1) | ((g & 1) << 1);
    LAS char* pb[4];
#pragma unroll
    for (int u = 0; u < 4; ++u) pb[u] = lds + K2_RB + (g * 4 + kh * 2) * 1024 + q4 * 128 + ((u ^ swz) << 5) + p4 * 8;
    LAS char* pe0 = lds + K2_RE + (kh * 4 + g) * 256 + ((q4 ^ ((g & 1) << 2)) * 32) + p4 * 8;
    LAS char* pe1 = lds + K2_RE + (kh * 4 + g) * 256 + (((4 + q4) ^ ((g & 1) << 2)) * 32) + p4 * 8;
    auto rdB = [&](int sl, int nf, int t1) -> s16x4 {
        if (nf < 8) return tr_read(pb[nf & 3] + sl * 16384 + (nf >> 2) * 1024 + t1 * 512);
        return tr_read((t1 ? pe1 : pe0) + sl * 2048);
    };

    f32x4 acc[9][2];
#pragma unroll
    for (int n = 0; n < 9; ++n)
#pragma unroll
        for (int m = 0; m < 2; ++m) { acc[n][m] = (f32x4){0.f, 0.f, 0.f, 0.f}; asm volatile("" : "+v"(acc[n][m])); }

    wait_vm<10>(); BAR();
    STAMP(2, 1);
    constexpr int PF2 = 4;
    bf16x8 fb[9];
#pragma unroll
    for (int i = 0; i < PF2; ++i) { s16x4 lo = rdB(0, i, 0), hi = rdB(0, i, 1); fb[i] = __builtin_shufflevector(lo, hi, 0, 1, 2, 3, 4, 5, 6, 7); }
    auto step = [&](auto S_, int t, bool do_issue, bool wait0, auto LAST_) {
        constexpr int S = decltype(S_)::value;
        constexpr bool has_next = !decltype(LAST_)::value;
        constexpr int sl = S, sn = (S + 1) & 3;
        asm volatile("" : "+v"(fa[sl][0]), "+v"(fa[sl][1]));
        if (!has_next && split != 0) {
#pragma unroll
            for (int m = 0; m < 2; ++m) fa[sl][m] = (bf16x8){0, 0, 0, 0, 0, 0, 0, 0};
            asm volatile("s_nop 1" : "+v"(fa[sl][0]), "+v"(fa[sl][1]));
        }
#pragma unroll
        for (int nf = 0; nf < 9; ++nf) {
            const int psl = (nf + PF2 < 9) ? sl : sn, nn = (nf + PF2 < 9) ? nf + PF2 : nf + PF2 - 9;
            const bool pf = (nf + PF2 < 9) || has_next;
            s16x4 lo, hi;
            mfma_ip(acc[nf][0], fb[nf], fa[sl][0]);
            SCHED();
            if (nf == 0) {
                if (!wait0) wait_vm<5>(); else wait_vm<0>();
                BAR();
            }
            if (pf) lo = rdB(psl, nn, 0);
            SCHED();
            mfma_ip(acc[nf][1], fb[nf], fa[sl][1]);
            SCHED();
            if (pf) hi = rdB(psl, nn, 1);
            if (nf >= 1 && nf < 5 && do_issue) issue1(t + 3, (S + 3) & 3, nf - 1, fa[(S + 3) & 3][0], fa[(S + 3) & 3][1]);
            SCHED();
            if (pf) fb[nn] = __builtin_shufflevector(lo, hi, 0, 1, 2, 3, 4, 5, 6, 7);
        }
    };
    using I0 = std::integral_constant<int, 0>; using I1 = std::integral_constant<int, 1>;
    using I2 = std::integral_constant<int, 2>; using I3 = std::integral_constant<int, 3>;
#pragma unroll 1
    for (int t0 = 0; t0 < 20; t0 += 4) {
        step(I0{}, t0, true, false, std::false_type{});
        step(I1{}, t0 + 1, true, false, std::false_type{});
        step(I2{}, t0 + 2, t0 + 2 < 18, false, std::false_type{});
        step(I3{}, t0 + 3, t0 + 3 < 18, t0 + 3 == 19, std::false_type{});
    }
    step(I0{}, 20, false, true, std::true_type{});
    asm volatile("s_nop 7\n\ts_nop 7" ::: "memory");
    BAR();
    STAMP(2, 2);
    f32x4 val[10];
    {
        LAS char* xw = lds + w * 10240;
        LAS char* xr = lds + (w ^ 1) * 10240;
#pragma unroll
        for (int n = 0; n < 9; ++n) {
            const bool give = kh ? (n < 5) : (n >= 5);
            if (give) {
#pragma unroll
                for (int m = 0; m < 2; ++m) *(LAS f32x4*)(xw + ((kh ? n : n - 5) * 2 + m) * 1024 + lane * 16) = acc[n][m];
            }
        }
        asm volatile("s_waitcnt lgkmcnt(0)" ::: "memory");
        BAR();
#pragma unroll
        for (int i = 0; i < 10; ++i) {
            const f32x4 mine = kh ? acc[(5 + (i >> 1)) < 9 ? 5 + (i >> 1) : 8][i & 1] : acc[i >> 1][i & 1];
            val[i] = mine + *(LAS f32x4*)(xr + i * 1024 + lane * 16);
        }
    }
    {
        const int nit = kh ? 8 : 10;
        const int grp = mt * 4 + nt;
        const __amdgpu_buffer_rsrc_t rp = __builtin_amdgcn_make_buffer_rsrc((void*)part, 0, NSPLIT * 64 * 8 * 10 * 512, 0x00020000);
        const unsigned slot = (unsigned)((grp * 8 + w) * 10 * 512 + lane * 8);
        constexpr unsigned SPL = 64 * 8 * 10 * 512;
        u32x2 xv[3]; unsigned xoff[3];
#pragma unroll
        for (int q = 0; q < 3; ++q) {
            int i = split + 4 * q; i = i < nit ? i : split;
            const int n = kh ? 5 + (i >> 1) : (i >> 1), m = i & 1;
            const int row = 128 * mt + 32 * wm + 16 * m + fr;
            const int col = (n < 8 ? 128 * nt + 16 * n : 512 + 16 * nt) + 4 * g;
            xoff[q] = (unsigned)(row * DM + col);
            xv[q] = *(const u32x2*)(x + xoff[q]);
        }
#pragma unroll
        for (int i = 0; i < 10; ++i) {
            if (i < nit && (i & 3) != split) {
                u32x2 v; v[0] = pk_bf16(val[i][0], val[i][1]); v[1] = pk_bf16(val[i][2], val[i][3]);
                __builtin_amdgcn_raw_buffer_store_b64(v, rp, slot + split * SPL + i * 512, 0, 16);
            }
        }
        STAMP(2, 3);
        asm volatile("s_waitcnt vmcnt(0)" ::: "memory");
        BAR();
        STAMP(2, 4);
        unsigned* c = cnt + grp * 64;
        if (tid == 0) {
            __hip_atomic_fetch_add(c, 1u, __ATOMIC_RELAXED, __HIP_MEMORY_SCOPE_AGENT);
            unsigned spins = 0;
            while (__hip_atomic_load(c, __ATOMIC_RELAXED, __HIP_MEMORY_SCOPE_AGENT) < (unsigned)NSPLIT) {
                __builtin_amdgcn_s_sleep(1);
                if (++spins > (1u << 22)) break;
            }
        }
        BAR();
        STAMP(2, 5);
        u32x2 pv[3][3];
#pragma unroll
        for (int q = 0; q < 3; ++q) {
            const int i = split + 4 * q;
#pragma unroll
            for (int s2 = 1; s2 < NSPLIT; ++s2) {
                const int so = (split + s2) & 3;
                const unsigned off = i < nit ? slot + so * SPL + i * 512 : 0xfffffff0u;
                pv[q][s2 - 1] = __builtin_amdgcn_raw_buffer_load_b64(rp, off, 0, 16);
            }
        }
#pragma unroll
        for (int q = 0; q < 3; ++q) {
            const int i = split + 4 * q;
            if (i < nit) {
                f32x4 o = val[0];
#pragma unroll
                for (int ii = 0; ii < 10; ++ii) if (ii == i) o = val[ii];
#pragma unroll
                for (int s2 = 0; s2 < 3; ++s2) { o[0] += bf_lo(pv[q][s2][0]); o[1] += bf_hi(pv[q][s2][0]); o[2] += bf_lo(pv[q][s2][1]); o[3] += bf_hi(pv[q][s2][1]); }
                u32x2 v; v[0] = pk_bf16(o[0] + bf_lo(xv[q][0]), o[1] + bf_hi(xv[q][0])); v[1] = pk_bf16(o[2] + bf_lo(xv[q][1]), o[3] + bf_hi(xv[q][1]));
                *(u32x2*)(out + xoff[q]) = v;
            }
        }
    }
    STAMP(2, 15);
}

extern "C" void kernel_launch(void* const* d_in, const int* in_sizes, int n_in, void* d_out, int out_size, void* d_ws,
                              size_t ws_size, hipStream_t stream) {
    const u16* x = (const u16*)d_in[0];
    const u16* wqkv = (const u16*)d_in[1];
    const u16* wo = (const u16*)d_in[2];
    const u16* Wg = (const u16*)d_in[4];
    const u16* Wu = (const u16*)d_in[5];
    const u16* Wd = (const u16*)d_in[6];
    char* ws = (char*)d_ws;
    u16* xnb = (u16*)ws;
    u16* hb = (u16*)(ws + WS_HB);
    u16* part = (u16*)(ws + WS_PART);
    u16* wqh = (u16*)(ws + WS_WQH);
    u16* gx = (u16*)(ws + WS_GX);
    u16* dx = (u16*)(ws + WS_DX);
    unsigned* cnt = (unsigned*)(ws + WS_CNT);
    (void)hipFuncSetAttribute((const void*)k1_gateup, hipFuncAttributeMaxDynamicSharedMemorySize, K1_LDS);
    (void)hipFuncSetAttribute((const void*)k2_down, hipFuncAttributeMaxDynamicSharedMemorySize, K2_LDS);
    k0_ln<<<256, 256, 0, stream>>>(x, xnb, wqkv, wqh, Wg, Wu, gx, Wd, wo, dx, (const char*)ws, (const char*)d_out);
    k1_gateup<<<256, 512, K1_LDS, stream>>>(xnb, Wg, Wu, wqh, gx, Wd, wo, hb, cnt);
    k2_down<<<256, 512, K2_LDS, stream>>>(hb, Wd, wo, dx, part, cnt, x, (u16*)d_out);
}
```

```cpp
#include <hip/hip_runtime.h>
#include <hip/hip_bf16.h>
#include <stdint.h>
#include <type_traits>

typedef unsigned short u16;
typedef short bf16x8 __attribute__((ext_vector_type(8)));
typedef short s16x4 __attribute__((ext_vector_type(4)));
typedef float f32x4 __attribute__((ext_vector_type(4)));
typedef float f32x2 __attribute__((ext_vector_type(2)));
typedef __bf16 bf16x2_t __attribute__((ext_vector_type(2)));
typedef unsigned u32x4 __attribute__((ext_vector_type(4)));
typedef unsigned u32x2 __attribute__((ext_vector_type(2)));
#define LAS __attribute__((address_space(3)))
#define GAS __attribute__((address_space(1)))

constexpr int SEQ = 2048, DM = 576, FF = 4608;
constexpr int NKB_X = 18;
constexpr int NKB_H = 162;
constexpr size_t BLK_ELEMS = (size_t)SEQ * 32;

constexpr size_t WS_HB = 18 * 131072, WS_PART = WS_HB + 162 * 131072, WS_WQH = WS_PART + 4 * 64 * 8 * 10 * 512, WS_GX = WS_WQH + 32 * 576 * 64 * 2,
                 WS_DX = WS_GX + 2 * 32 * 576 * 16 * 2, WS_CNT = WS_DX + 4 * 5184 * 16 * 2, WS_END = WS_CNT + 80 * 256;

__device__ __forceinline__ unsigned pk_bf16(float lo, float hi) {
    f32x2 v = {lo, hi};
    bf16x2_t b = __builtin_convertvector(v, bf16x2_t);
    return __builtin_bit_cast(unsigned, b);
}
__device__ __forceinline__ float bf_lo(unsigned u) { return __uint_as_float(u << 16); }
__device__ __forceinline__ float bf_hi(unsigned u) { return __uint_as_float(u & 0xffff0000u); }
__device__ __forceinline__ float rbf(float x) { return bf_lo(pk_bf16(x, x)); }

__device__ __forceinline__ const void* uni(const void* p) {
    const unsigned long long v = (unsigned long long)p;
    const unsigned lo = __builtin_amdgcn_readfirstlane((unsigned)v), hi = __builtin_amdgcn_readfirstlane((unsigned)(v >> 32));
    return (const void*)(((unsigned long long)hi << 32) | lo);
}
__device__ __forceinline__ void glds16(const void* sbase, unsigned voff, unsigned lds) {
    sbase = uni(sbase); lds = __builtin_amdgcn_readfirstlane(lds);
    asm volatile("s_mov_b32 m0, %2\n\ts_nop 3\n\tglobal_load_lds_dwordx4 %0, %1"
                 :: "v"(voff), "s"(sbase), "s"(lds) : "memory", "m0");
}
__device__ __forceinline__ void glds16_o1k(const void* sbase, unsigned voff, unsigned lds) {
    sbase = uni(sbase); lds = __builtin_amdgcn_readfirstlane(lds);
    asm volatile("s_mov_b32 m0, %2\n\ts_nop 3\n\tglobal_load_lds_dwordx4 %0, %1 offset:1024"
                 :: "v"(voff), "s"(sbase), "s"(lds) : "memory", "m0");
}
__device__ __forceinline__ void glds16_q(const void* sbase, unsigned voff, unsigned lds) {
    sbase = uni(sbase); lds = __builtin_amdgcn_readfirstlane(lds);
    unsigned sv; unsigned long long ex;
    asm volatile("s_mov_b32 %0, m0\n\ts_mov_b64 %1, exec\n\ts_mov_b32 m0, %4\n\ts_mov_b64 exec, 0xffff\n\ts_nop 1\n\tglobal_load_lds_dwordx4 %2, %3\n\ts_mov_b64 exec, %1\n\ts_mov_b32 m0, %0"
                 : "=&s"(sv), "=&s"(ex) : "v"(voff), "s"(sbase), "s"(lds) : "memory");
}
__device__ __forceinline__ void glds16_m(const void* sbase, unsigned voff, unsigned lds) {
    sbase = uni(sbase); lds = __builtin_amdgcn_readfirstlane(lds);
    unsigned sv; unsigned long long ex;
    asm volatile("s_mov_b32 %0, m0\n\ts_mov_b64 %1, exec\n\ts_mov_b32 m0, %4\n\ts_mov_b32 exec_lo, -1\n\ts_mov_b32 exec_hi, 0xff\n\ts_nop 1\n\tglobal_load_lds_dwordx4 %2, %3\n\ts_mov_b64 exec, %1\n\ts_mov_b32 m0, %0"
                 : "=&s"(sv), "=&s"(ex) : "v"(voff), "s"(sbase), "s"(lds) : "memory");
}
#define SCHED() __builtin_amdgcn_sched_barrier(0)
#define STAMP(kid, i) do {} while (0)
#define WSTAMP(i) do {} while (0)
__device__ __forceinline__ s16x4 tr_read(LAS char* p) {
    return __builtin_bit_cast(s16x4, __builtin_amdgcn_ds_read_tr16_b64_v4i16((LAS s16x4*)p));
}
__device__ __forceinline__ bf16x8 tr_frag(LAS char* p, int off_t1) {
    s16x4 lo = tr_read(p), hi = tr_read(p + off_t1);
    return __builtin_shufflevector(lo, hi, 0, 1, 2, 3, 4, 5, 6, 7);
}
__device__ __forceinline__ bf16x8 row_frag(LAS char* p) { return *(LAS bf16x8*)p; }
#define MFMA16(a, b, c) __builtin_amdgcn_mfma_f32_16x16x32_bf16((a), (b), (c), 0, 0, 0)
__device__ __forceinline__ void mfma_ip(f32x4& c, bf16x8 a, bf16x8 b) { asm volatile("v_mfma_f32_16x16x32_bf16 %0, %1, %2, %0" : "+v"(c) : "v"(a), "v"(b)); }
#define WAIT_V(n) asm volatile("s_waitcnt vmcnt(" #n ")" ::: "memory")
#define BAR() do { asm volatile("" ::: "memory"); __builtin_amdgcn_s_barrier(); asm volatile("" ::: "memory"); } while (0)

__device__ __forceinline__ int blk_off(int row, int chunk) { return row * 64 + ((chunk ^ (((row >> 3) & 1) << 1)) << 4); }

__global__ __launch_bounds__(256) void k0_ln(const u16* __restrict__ x, u16* __restrict__ xnb,
                                            const u16* __restrict__ wqkv, u16* __restrict__ wqh,
                                            const u16* __restrict__ Wg, const u16* __restrict__ Wu, u16* __restrict__ gx,
                                            const u16* __restrict__ Wd, const u16* __restrict__ wo, u16* __restrict__ dx,
                                            const char* __restrict__ wsb, const char* __restrict__ outb, unsigned* __restrict__ cnt) {
    if (threadIdx.x >= 64) {
        const int l = threadIdx.x - 64, head = blockIdx.x >> 3, k0 = (blockIdx.x & 7) * 72;
        unsigned v[12];
#pragma unroll
        for (int i = 0; i < 12; ++i) {
            const int id = l + 192 * i, k = k0 + (id >> 5), cp = id & 31;
            const int part = cp >= 18 ? 2 : (cp >= 9 ? 1 : 0), d2 = cp - part * 9;
            const int src = (part == 2 ? 0 : part + 1) * DM + 18 * head + 2 * d2;
            v[i] = cp < 27 ? *(const unsigned*)(wqkv + (size_t)k * (3 * DM) + src) : 0u;
        }
        u32x4 c[3];
#pragma unroll
        for (int i = 0; i < 2; ++i) {
            const int id = l + 192 * i;
            if (id < 288) {
                const int gid = blockIdx.x * 288 + id, mat = gid / 36864, r = gid - mat * 36864, k = r >> 6, ch = r & 63;
                c[i] = *(const u32x4*)((mat ? Wu : Wg) + (size_t)k * FF + 4096 + 8 * ch);
            }
        }
        if (l < 162) {
            const int gid = blockIdx.x * 162 + l, k = gid >> 3, ch = gid & 7;
            c[2] = *(const u32x4*)((k < FF ? Wd + (size_t)k * DM : wo + (size_t)(k - FF) * DM) + 512 + 8 * ch);
        }
#pragma unroll
        for (int i = 0; i < 12; ++i) {
            const int id = l + 192 * i;
            *(unsigned*)(wqh + ((size_t)head * 576 + k0) * 64 + 2 * id) = v[i];
        }
#pragma unroll
        for (int i = 0; i < 2; ++i) {
            const int id = l + 192 * i;
            if (id < 288) {
                const int gid = blockIdx.x * 288 + id, mat = gid / 36864, r = gid - mat * 36864, k = r >> 6, ch = r & 63;
                *(u32x4*)(gx + (((size_t)mat * 32 + (ch >> 1)) * 576 + k) * 16 + 8 * (ch & 1)) = c[i];
            }
        }
        if (l < 162) {
            const int gid = blockIdx.x * 162 + l, k = gid >> 3, ch = gid & 7;
            *(u32x4*)(dx + ((size_t)(ch >> 1) * 5184 + k) * 16 + 8 * (ch & 1)) = c[2];
        }
    if (threadIdx.x >= 192) {
        const int l = threadIdx.x - 192, slot = (blockIdx.x >> 3) * 64 + l;
        constexpr int NW = (FF * DM * 2 + 65535) / 65536;
        constexpr int NS = (int)((WS_END + 65535) / 65536), NO = (SEQ * DM * 2 + 65535) / 65536, NWO = (DM * DM * 2 + 65535) / 65536;
        const char* p = nullptr;
        int i = slot;
        if (i < NW) p = (const char*)Wg + (size_t)i * 65536; i -= NW;
        if (i >= 0 && i < NW) p = (const char*)Wu + (size_t)i * 65536; i -= NW;
        if (i >= 0 && i < NW) p = (const char*)Wd + (size_t)i * 65536; i -= NW;
        if (i >= 0 && i < NS) p = wsb + (size_t)i * 65536; i -= NS;
        if (i >= 0 && i < NO) p = outb + (size_t)i * 65536; i -= NO;
        if (i >= 0 && i < NWO) p = (const char*)wo + (size_t)i * 65536;
        if (p) { unsigned v; asm volatile("global_load_dword %0, %1, off\n\ts_waitcnt vmcnt(0)" : "=&v"(v) : "v"(p) : "memory"); }
    }
        return;
    }
    if (blockIdx.x < 80 && threadIdx.x == 0) cnt[blockIdx.x * 64] = 0u;
    const int lane = threadIdx.x, i = lane & 7, row = blockIdx.x * 8 + (lane >> 3);
    u32x4 xv[9];
#pragma unroll
    for (int cc = 0; cc < 9; ++cc) xv[cc] = *(const u32x4*)(x + (size_t)row * DM + (i + 8 * cc) * 8);
    float s = 0.f;
#pragma unroll
    for (int cc = 0; cc < 9; ++cc)
#pragma unroll
        for (int e = 0; e < 4; ++e) s += bf_lo(xv[cc][e]) + bf_hi(xv[cc][e]);
    s += __shfl_xor(s, 1); s += __shfl_xor(s, 2); s += __shfl_xor(s, 4);
    const float mean = s * (1.0f / 576.0f);
    float q = 0.f;
#pragma unroll
    for (int cc = 0; cc < 9; ++cc)
#pragma unroll
        for (int e = 0; e < 4; ++e) { const float a = bf_lo(xv[cc][e]) - mean, c2 = bf_hi(xv[cc][e]) - mean; q += a * a + c2 * c2; }
    q += __shfl_xor(q, 1); q += __shfl_xor(q, 2); q += __shfl_xor(q, 4);
    const float mb = rbf(mean), vb = rbf(q * (1.0f / 576.0f));
    const float sb = rbf(sqrtf(rbf(vb + 1e-5f)));
    const float rs = 1.0f / sb;
#pragma unroll
    for (int cc = 0; cc < 9; ++cc) {
        u32x4 o;
#pragma unroll
        for (int e = 0; e < 4; ++e) {
            const unsigned d = pk_bf16(bf_lo(xv[cc][e]) - mb, bf_hi(xv[cc][e]) - mb);
            o[e] = pk_bf16(bf_lo(d) * rs, bf_hi(d) * rs);
        }
        const int blk = 2 * cc + (i >> 2);
        *(u32x4*)((char*)xnb + (size_t)blk * (BLK_ELEMS * 2) + blk_off(row, i & 3)) = o;
    }
}

constexpr int RA = 0, RB = 65536, RE = 131072, RQ = 139264, RH = 155648;
constexpr int T_DUMMY = 159744;
constexpr int K1_LDS = T_DUMMY + 1024;
constexpr int QS = 144;
#define HST_AUX 16
#define PF 3
constexpr int NB = PF + 1;
template <int N> __device__ __forceinline__ void wait_vm() { asm volatile("s_waitcnt vmcnt(%0)" :: "i"(N) : "memory"); }
#define GLDS_MASKED(NAME, LO, HI) \
__device__ __forceinline__ void NAME(const void* sbase, unsigned voff, unsigned lds) { \
    sbase = uni(sbase); lds = __builtin_amdgcn_readfirstlane(lds); \
    unsigned sv; unsigned long long ex; \
    asm volatile("s_mov_b32 %0, m0\n\ts_mov_b64 %1, exec\n\ts_mov_b32 m0, %4\n\ts_mov_b32 exec_lo, " #LO "\n\ts_mov_b32 exec_hi, " #HI "\n\tglobal_load_lds_dwordx4 %2, %3\n\ts_mov_b64 exec, %1\n\ts_mov_b32 m0, %0" \
                 : "=&s"(sv), "=&s"(ex) : "v"(voff), "s"(sbase), "s"(lds) : "memory"); }
GLDS_MASKED(glds16_32, -1, 0)
GLDS_MASKED(glds16_8, 0xff, 0)

constexpr int K2_NT = 21;
constexpr int K2_RB = 0, K2_RE = 65536, K2_LDS = 81920;
constexpr int NSPLIT = 4;
__device__ __forceinline__ void gload2(bf16x8& d0, bf16x8& d1, const void* sbase, unsigned voff) {
    sbase = uni(sbase);
    asm volatile("s_nop 4\n\tglobal_load_dwordx4 %0, %2, %3\n\tglobal_load_dwordx4 %1, %2, %3 offset:1024"
                 : "=&v"(d0), "=&v"(d1) : "v"(voff), "s"(sbase) : "memory");
}
__device__ __forceinline__ void phase2(LAS char* lds, const u16* __restrict__ hb, const u16* __restrict__ Wd,
                                       const u16* __restrict__ wo, const u16* __restrict__ dx, u16* __restrict__ part, unsigned* __restrict__ cnt,
                                       const u16* __restrict__ x, u16* __restrict__ out) {
    int tid = threadIdx.x; asm volatile("" : "+v"(tid));
    const int lane = tid & 63;
    const int w = __builtin_amdgcn_readfirstlane(tid >> 6);
    const int b = blockIdx.x, xcd = b & 7, idx = b >> 3;
    const int split = xcd >> 1, mt = (xcd & 1) * 8 + (idx >> 2), nt = idx & 3;
    const int wm = w >> 1, kh = w & 1;
    const int T0 = split == 0 ? 0 : 21 + 20 * (split - 1);

    const unsigned srcA = (unsigned)(kh * (int)BLK_ELEMS * 2 + blk_off(128 * mt + 32 * wm + (lane & 15), lane >> 4));
    const int koct = w >> 1, sub = w & 1;
    unsigned offB, offE;
    {
        const int r = lane >> 3, swz = ((r >> 1) & 1) | ((koct & 1) << 1), c = (lane & 7) ^ (swz << 1);
        offB = (unsigned)((32 * sub + 8 * koct + r) * DM + 128 * nt + 8 * c) * 2u;
        const int re = ((lane >> 1) & 7) ^ ((koct & 1) << 2), ce = (lane & 1);
        offE = (unsigned)((nt * 5184 + 32 * sub + 8 * koct + re) * 16 + 8 * ce) * 2u;
    }
    const unsigned lds0 = (unsigned)(size_t)lds;
    const unsigned dB = lds0 + K2_RB + (2 * w) * 1024, dE = lds0 + K2_RE + (sub * 4 + koct) * 256;
    bf16x8 fa[4][2];
    auto issue1 = [&](int t, int sl, int i, bf16x8& a0, bf16x8& a1) {
        int T = T0 + t; if (t == K2_NT - 1 && split != 0) T -= 1;
        const u16* Wt = T < 72 ? Wd + (size_t)T * 64 * DM : wo + (size_t)(T - 72) * 64 * DM;
        const u16* ba = hb + (size_t)T * 2 * BLK_ELEMS;
        if (i == 0) glds16(Wt, offB, dB + sl * 16384);
        if (i == 1) glds16(Wt + 64, offB, dB + sl * 16384 + 1024);
        if (i == 2) glds16_q(dx + (size_t)T * 64 * 16, offE, dE + sl * 2048);
        if (i == 3) gload2(a0, a1, ba, srcA);
    };
    asm volatile("s_waitcnt vmcnt(0)" ::: "memory");
    BAR();
    unsigned* gbar = cnt + 64 * 64;
    if (tid == 0) __hip_atomic_fetch_add(gbar + (b & 15) * 64, 1u, __ATOMIC_RELAXED, __HIP_MEMORY_SCOPE_AGENT);
    STAMP(2, 0);
#pragma unroll
    for (int t = 0; t < 3; ++t)
#pragma unroll
        for (int i = 0; i < 3; ++i) issue1(t, t, i, fa[t][0], fa[t][1]);
    if (w == 0) {
        unsigned spins = 0;
        for (;;) {
            const unsigned v = __hip_atomic_load(gbar + (lane & 15) * 64, __ATOMIC_RELAXED, __HIP_MEMORY_SCOPE_AGENT);
            if (__all(v >= 16u)) break;
            __builtin_amdgcn_s_sleep(2);
            if (++spins > (1u << 22)) break;
        }
        __builtin_amdgcn_fence(__ATOMIC_ACQUIRE, "agent");
        asm volatile("s_waitcnt vmcnt(0)" ::: "memory");
    }
    BAR();
    STAMP(2, 6);
#pragma unroll
    for (int t = 0; t < 3; ++t) issue1(t, t, 3, fa[t][0], fa[t][1]);

    const int fr = lane & 15, g = lane >> 4, q4 = (lane >> 2) & 3, p4 = lane & 3;
    const int swz = ((q4 >> 1) & 1) | ((g & 1) << 1);
    LAS char* pb[4];
#pragma unroll
    for (int u = 0; u < 4; ++u) pb[u] = lds + K2_RB + (g * 4 + kh * 2) * 1024 + q4 * 128 + ((u ^ swz) << 5) + p4 * 8;
    LAS char* pe0 = lds + K2_RE + (kh * 4 + g) * 256 + ((q4 ^ ((g & 1) << 2)) * 32) + p4 * 8;
    LAS char* pe1 = lds + K2_RE + (kh * 4 + g) * 256 + (((4 + q4) ^ ((g & 1) << 2)) * 32) + p4 * 8;
    auto rdB = [&](int sl, int nf, int t1) -> s16x4 {
        if (nf < 8) return tr_read(pb[nf & 3] + sl * 16384 + (nf >> 2) * 1024 + t1 * 512);
        return tr_read((t1 ? pe1 : pe0) + sl * 2048);
    };

    f32x4 acc[9][2];
#pragma unroll
    for (int n = 0; n < 9; ++n)
#pragma unroll
        for (int m = 0; m < 2; ++m) { acc[n][m] = (f32x4){0.f, 0.f, 0.f, 0.f}; asm volatile("" : "+v"(acc[n][m])); }

    wait_vm<4>(); BAR();
    STAMP(2, 1);
    constexpr int PF2 = 4;
    bf16x8 fb[9];
#pragma unroll
    for (int i = 0; i < PF2; ++i) { s16x4 lo = rdB(0, i, 0), hi = rdB(0, i, 1); fb[i] = __builtin_shufflevector(lo, hi, 0, 1, 2, 3, 4, 5, 6, 7); }
    auto step = [&](auto S_, int t, bool do_issue, bool wait0, auto LAST_) {
        constexpr int S = decltype(S_)::value;
        constexpr bool has_next = !decltype(LAST_)::value;
        constexpr int sl = S, sn = (S + 1) & 3;
        asm volatile("" : "+v"(fa[sl][0]), "+v"(fa[sl][1]));
        if (!has_next && split != 0) {
#pragma unroll
            for (int m = 0; m < 2; ++m) fa[sl][m] = (bf16x8){0, 0, 0, 0, 0, 0, 0, 0};
            asm volatile("s_nop 1" : "+v"(fa[sl][0]), "+v"(fa[sl][1]));
        }
#pragma unroll
        for (int nf = 0; nf < 9; ++nf) {
            const int psl = (nf + PF2 < 9) ? sl : sn, nn = (nf + PF2 < 9) ? nf + PF2 : nf + PF2 - 9;
            const bool pf = (nf + PF2 < 9) || has_next;
            s16x4 lo, hi;
            mfma_ip(acc[nf][0], fb[nf], fa[sl][0]);
            SCHED();
            if (nf == 0) {
                if (wait0) wait_vm<0>(); else if (S == 0 && t == 0) wait_vm<2>(); else wait_vm<5>();
                BAR();
            }
            if (pf) lo = rdB(psl, nn, 0);
            SCHED();
            mfma_ip(acc[nf][1], fb[nf], fa[sl][1]);
            SCHED();
            if (pf) hi = rdB(psl, nn, 1);
            if (nf >= 1 && nf < 5 && do_issue) issue1(t + 3, (S + 3) & 3, nf - 1, fa[(S + 3) & 3][0], fa[(S + 3) & 3][1]);
            SCHED();
            if (pf) fb[nn] = __builtin_shufflevector(lo, hi, 0, 1, 2, 3, 4, 5, 6, 7);
        }
    };
    using I0 = std::integral_constant<int, 0>; using I1 = std::integral_constant<int, 1>;
    using I2 = std::integral_constant<int, 2>; using I3 = std::integral_constant<int, 3>;
#pragma unroll 1
    for (int t0 = 0; t0 < 20; t0 += 4) {
        step(I0{}, t0, true, false, std::false_type{});
        step(I1{}, t0 + 1, true, false, std::false_type{});
        step(I2{}, t0 + 2, t0 + 2 < 18, false, std::false_type{});
        step(I3{}, t0 + 3, t0 + 3 < 18, t0 + 3 == 19, std::false_type{});
    }
    step(I0{}, 20, false, true, std::true_type{});
    asm volatile("s_nop 7\n\ts_nop 7" ::: "memory");
    BAR();
    STAMP(2, 2);
    f32x4 val[10];
    {
        LAS char* xw = lds + w * 10240;
        LAS char* xr = lds + (w ^ 1) * 10240;
#pragma unroll
        for (int n = 0; n < 9; ++n) {
            const bool give = kh ? (n < 5) : (n >= 5);
            if (give) {
#pragma unroll
                for (int m = 0; m < 2; ++m) *(LAS f32x4*)(xw + ((kh ? n : n - 5) * 2 + m) * 1024 + lane * 16) = acc[n][m];
            }
        }
        asm volatile("s_waitcnt lgkmcnt(0)" ::: "memory");
        BAR();
#pragma unroll
        for (int i = 0; i < 10; ++i) {
            const f32x4 mine = kh ? acc[(5 + (i >> 1)) < 9 ? 5 + (i >> 1) : 8][i & 1] : acc[i >> 1][i & 1];
            val[i] = mine + *(LAS f32x4*)(xr + i * 1024 + lane * 16);
        }
    }
    {
        const int nit = kh ? 8 : 10;
        const int grp = mt * 4 + nt;
        const __amdgpu_buffer_rsrc_t rp = __builtin_amdgcn_make_buffer_rsrc((void*)part, 0, NSPLIT * 64 * 8 * 10 * 512, 0x00020000);
        const unsigned slot = (unsigned)((grp * 8 + w) * 10 * 512 + lane * 8);
        constexpr unsigned SPL = 64 * 8 * 10 * 512;
        u32x2 xv[3]; unsigned xoff[3];
#pragma unroll
        for (int q = 0; q < 3; ++q) {
            int i = split + 4 * q; i = i < nit ? i : split;
            const int n = kh ? 5 + (i >> 1) : (i >> 1), m = i & 1;
            const int row = 128 * mt + 32 * wm + 16 * m + fr;
            const int col = (n < 8 ? 128 * nt + 16 * n : 512 + 16 * nt) + 4 * g;
            xoff[q] = (unsigned)(row * DM + col);
            xv[q] = *(const u32x2*)(x + xoff[q]);
        }
#pragma unroll
        for (int i = 0; i < 10; ++i) {
            if (i < nit && (i & 3) != split) {
                u32x2 v; v[0] = pk_bf16(val[i][0], val[i][1]); v[1] = pk_bf16(val[i][2], val[i][3]);
                __builtin_amdgcn_raw_buffer_store_b64(v, rp, slot + split * SPL + i * 512, 0, 16);
            }
        }
        STAMP(2, 3);
        asm volatile("s_waitcnt vmcnt(0)" ::: "memory");
        BAR();
        STAMP(2, 4);
        unsigned* c = cnt + grp * 64;
        if (tid == 0) {
            __hip_atomic_fetch_add(c, 1u, __ATOMIC_RELAXED, __HIP_MEMORY_SCOPE_AGENT);
            unsigned spins = 0;
            while (__hip_atomic_load(c, __ATOMIC_RELAXED, __HIP_MEMORY_SCOPE_AGENT) < (unsigned)NSPLIT) {
                __builtin_amdgcn_s_sleep(1);
                if (++spins > (1u << 22)) break;
            }
        }
        BAR();
        STAMP(2, 5);
        u32x2 pv[3][3];
#pragma unroll
        for (int q = 0; q < 3; ++q) {
            const int i = split + 4 * q;
#pragma unroll
            for (int s2 = 1; s2 < NSPLIT; ++s2) {
                const int so = (split + s2) & 3;
                const unsigned off = i < nit ? slot + so * SPL + i * 512 : 0xfffffff0u;
                pv[q][s2 - 1] = __builtin_amdgcn_raw_buffer_load_b64(rp, off, 0, 16);
            }
        }
#pragma unroll
        for (int q = 0; q < 3; ++q) {
            const int i = split + 4 * q;
            if (i < nit) {
                f32x4 o = val[0];
#pragma unroll
                for (int ii = 0; ii < 10; ++ii) if (ii == i) o = val[ii];
#pragma unroll
                for (int s2 = 0; s2 < 3; ++s2) { o[0] += bf_lo(pv[q][s2][0]); o[1] += bf_hi(pv[q][s2][0]); o[2] += bf_lo(pv[q][s2][1]); o[3] += bf_hi(pv[q][s2][1]); }
                u32x2 v; v[0] = pk_bf16(o[0] + bf_lo(xv[q][0]), o[1] + bf_hi(xv[q][0])); v[1] = pk_bf16(o[2] + bf_lo(xv[q][1]), o[3] + bf_hi(xv[q][1]));
                *(u32x2*)(out + xoff[q]) = v;
            }
        }
    }
    STAMP(2, 15);
}

__device__ __forceinline__ void k1_epilogue(f32x4 (&acc)[11][4], f32x4& ach, LAS char* lds, unsigned lds0, int w, int xcd, int idx,
                                            int j, int mt, const u16* __restrict__ Wd, const u16* __restrict__ wo, u16* __restrict__ hb) {
    int tid = threadIdx.x; asm volatile("" : "+v"(tid));
    const int lane = tid & 63, fr = lane & 15, g = lane >> 4;
    const int wm = w >> 1, wn = w & 1;
    const __amdgpu_buffer_rsrc_t rh = __builtin_amdgcn_make_buffer_rsrc((void*)hb, 0, (int)(NKB_H * BLK_ELEMS * 2), 0x00020000);
    auto hs = [&](int m, int part) {
        const int row = 256 * mt + 64 * wm + 16 * m + fr;
        SCHED();
        if (part < 2) {
            const int pp = part;
            unsigned v[2][2];
#pragma unroll
            for (int hh = 0; hh < 2; ++hh) {
                const int gt = 2 * pp + hh;
                float hv[4];
#pragma unroll
                for (int e = 0; e < 4; ++e) hv[e] = fmaxf(acc[2 * gt][m][e], 0.f) * acc[2 * gt + 1][m][e];
                v[hh][0] = pk_bf16(hv[0], hv[1]); v[hh][1] = pk_bf16(hv[2], hv[3]);
            }
            auto r0 = __builtin_amdgcn_permlane16_swap(v[0][0], v[1][0], false, false);
            auto r1 = __builtin_amdgcn_permlane16_swap(v[0][1], v[1][1], false, false);
            u32x4 o; o[0] = r0[0]; o[1] = r1[0]; o[2] = r0[1]; o[3] = r1[1];
            const int blk = 4 * j + 2 * wn + pp, chunk = ((g & 1) << 1) | (g >> 1);
            __builtin_amdgcn_raw_buffer_store_b128(o, rh, (unsigned)(blk * (int)(BLK_ELEMS * 2) + blk_off(row, chunk)), 0, HST_AUX);
        } else {
            float hv[4];
#pragma unroll
            for (int e = 0; e < 4; ++e) {
                const unsigned mine = __float_as_uint(acc[8][m][e]);
                auto r = __builtin_amdgcn_permlane32_swap(mine, mine, false, false);
                hv[e] = fmaxf(__uint_as_float(r[0]), 0.f) * __uint_as_float(r[1]);
            }
            u32x2 v; v[0] = pk_bf16(hv[0], hv[1]); v[1] = pk_bf16(hv[2], hv[3]);
            const int blk = 128 + (j >> 1), chunk = (j & 1) * 2 + wn;
            const unsigned off = g < 2 ? (unsigned)(blk * (int)(BLK_ELEMS * 2) + blk_off(row, chunk) + g * 8) : 0xfffffff0u;
            __builtin_amdgcn_raw_buffer_store_b64(v, rh, off, 0, HST_AUX);
        }
        SCHED();
    };
    hs(0, 0);
    BAR();
    STAMP(1, 2);
    LAS char* qt = lds;
    {
#pragma unroll
        for (int u = 0; u < 2; ++u) {
            const int nn = 16 * (2 * wn + (u ^ (wm & 1))) + 4 * g;
            const int h0 = nn / 18, h1 = (nn + 2) / 18;
            const int o0 = h0 * 48 + (nn - h0 * 18) * 2, o1 = h1 * 48 + (nn + 2 - h1 * 18) * 2;
#pragma unroll
            for (int m = 0; m < 4; ++m) {
                LAS char* rp = qt + (16 + 64 * wm + 16 * m + fr) * QS;
                if (h0 < 3) *(LAS unsigned*)(rp + o0) = pk_bf16(acc[9 + u][m][0], acc[9 + u][m][1]);
                if (h1 < 3) *(LAS unsigned*)(rp + o1) = pk_bf16(acc[9 + u][m][2], acc[9 + u][m][3]);
            }
            if (u == 0 && (w == 0 || w == 2 || w == 1)) {
                LAS char* rp = qt + fr * QS;
                if (h0 < 3) *(LAS unsigned*)(rp + o0) = pk_bf16(ach[0], ach[1]);
                if (h1 < 3) *(LAS unsigned*)(rp + o1) = pk_bf16(ach[2], ach[3]);
            }
            if (u == 0) hs(0, 1);
        }
    }
    asm volatile("s_waitcnt lgkmcnt(0)" ::: "memory");
    BAR();
    STAMP(1, 3);
    {
        const int s = tid >> 1, half = tid & 1;
        const int lrow = 16 + s, grow = 256 * mt + s;
        auto ld18 = [&](LAS const char* p, unsigned (&v)[9]) {
            const u32x4 a = *(LAS const u32x4*)p, c = *(LAS const u32x4*)(p + 16);
            v[0] = a[0]; v[1] = a[1]; v[2] = a[2]; v[3] = a[3]; v[4] = c[0]; v[5] = c[1]; v[6] = c[2]; v[7] = c[3];
            v[8] = *(LAS const unsigned*)(p + 32);
        };
        hs(0, 2);
        unsigned qv[9];
        ld18(qt + lrow * QS + 96, qv);
        float lg[4];
#pragma unroll
        for (int ww = 0; ww < 4; ++ww) {
            const int wi = 4 * half + ww;
            unsigned kv[9];
            ld18(qt + (lrow - 2 * wi) * QS, kv);
            float a = 0.f;
#pragma unroll
            for (int i = 0; i < 9; ++i) a = __builtin_amdgcn_fdot2_f32_bf16(__builtin_bit_cast(bf16x2_t, qv[i]), __builtin_bit_cast(bf16x2_t, kv[i]), a, false);
            a = rbf(rbf(a) * 0.23570226039551584f);
            lg[ww] = (grow - 2 * wi >= 0) ? a : -1e9f;
            if (ww == 0) hs(1, 0);
            if (ww == 2) hs(1, 1);
        }
        float mx = fmaxf(fmaxf(lg[0], lg[1]), fmaxf(lg[2], lg[3]));
        mx = fmaxf(mx, __shfl_xor(mx, 1));
        float ex[4], sum = 0.f;
#pragma unroll
        for (int ww = 0; ww < 4; ++ww) { ex[ww] = __expf(lg[ww] - mx); sum += ex[ww]; }
        sum += __shfl_xor(sum, 1);
        const float inv = 1.0f / sum;
        hs(1, 2);
        float o[18];
#pragma unroll
        for (int i = 0; i < 18; ++i) o[i] = 0.f;
#pragma unroll
        for (int ww = 0; ww < 4; ++ww) {
            const int wi = 4 * half + ww;
            const float pw = (grow - 2 * wi >= 0) ? rbf(ex[ww] * inv) : 0.f;
            unsigned vv[9];
            ld18(qt + (lrow - 2 * wi) * QS + 48, vv);
#pragma unroll
            for (int i = 0; i < 9; ++i) { o[2 * i] += pw * bf_lo(vv[i]); o[2 * i + 1] += pw * bf_hi(vv[i]); }
            if (ww < 3) hs(2, ww);
        }
        hs(3, 0);
#pragma unroll
        for (int ii = 0; ii < 5; ++ii) {
            const float s0 = half ? o[2 * ii] : o[(10 + 2 * ii) % 18], s1 = half ? o[2 * ii + 1] : o[(11 + 2 * ii) % 18];
            const float r0 = __shfl_xor(s0, 1), r1 = __shfl_xor(s1, 1);
            const float m0 = half ? o[(10 + 2 * ii) % 18] : o[2 * ii], m1 = half ? o[(11 + 2 * ii) % 18] : o[2 * ii + 1];
            const int i = half * 5 + ii;
            if (i < 9) {
                const int col = FF + 18 * j + 2 * i;
                __builtin_amdgcn_raw_buffer_store_b32(pk_bf16(m0 + r0, m1 + r1), rh, (unsigned)((col >> 5) * (int)(BLK_ELEMS * 2) + blk_off(grow, (col >> 3) & 3) + (col & 7) * 2), 0, 16);
            }
            if (ii == 2) hs(3, 1);
        }
        hs(3, 2);
    }
    STAMP(1, 15);
}

__global__ __launch_bounds__(512, 2) void k1_gateup(const u16* __restrict__ xnb, const u16* __restrict__ Wg,
                                                    const u16* __restrict__ Wu, const u16* __restrict__ wqh, const u16* __restrict__ gx,
                                                    const u16* __restrict__ Wd, const u16* __restrict__ wo, u16* __restrict__ hb, unsigned* __restrict__ cnt,
                                                    const u16* __restrict__ dx, u16* __restrict__ part, const u16* __restrict__ x, u16* __restrict__ out) {
    extern __shared__ __attribute__((aligned(16))) char smem[];
    LAS char* lds = (LAS char*)smem;
    const int tid = threadIdx.x, lane = tid & 63;
    const int w = __builtin_amdgcn_readfirstlane(tid >> 6);
    const int b = blockIdx.x, xcd = b & 7, idx = b >> 3;
    const int j = 4 * xcd + (idx & 3), mt = idx >> 2;
    const int wm = w >> 1, wn = w & 1;
    const unsigned lds0 = (unsigned)(size_t)lds;
    const int fr = lane & 15, g = lane >> 4, q4 = (lane >> 2) & 3, p4 = lane & 3;

    f32x4 acc[11][4], ach;
    {
        const unsigned srcA = (unsigned)((256 * mt + 32 * w) * 32 + lane * 8) * 2u;
        const int koct = w >> 1, mat = w & 1;
        const u16* W = mat ? Wu : Wg;
        unsigned srcB, srcX;
        const u16* baseX; int strideX; unsigned dX;
        {
            const int r = lane >> 3, swz = ((r >> 1) & 1) | ((koct & 1) << 1), c = (lane & 7) ^ (swz << 1);
            srcB = (unsigned)((8 * koct + r) * FF + 128 * j + 8 * c) * 2u;
            if (w < 2) {
                const int ko = lane >> 4, re = ((lane >> 1) & 7) ^ ((ko & 1) << 2), ce = (lane & 1) ^ w;
                srcX = (unsigned)(((w * 32 + j) * 576 + 8 * ko + re) * 16 + 8 * ce) * 2u;
                baseX = gx; strideX = 32 * 16; dX = lds0 + RE + w * 1024;
            } else if (w < 6) {
                const int ko = w - 2, swq = ((r >> 1) & 1) | ((ko & 1) << 1), cq = (lane & 7) ^ (swq << 1);
                srcX = (unsigned)((j * 576 + 8 * ko + r) * 64 + 8 * cq) * 2u;
                baseX = wqh; strideX = 32 * 64; dX = lds0 + RQ + ko * 1024;
            } else {
                const int hr = mt > 0 ? 256 * mt - 16 : 0;
                srcX = (unsigned)(hr * 64 + lane * 16);
                baseX = xnb; strideX = (int)BLK_ELEMS; dX = lds0 + RH;
            }
        }
        const int slotX = w < 2 ? 2048 : (w < 6 ? 4096 : 1024);
        const unsigned dA = lds0 + RA + (2 * w) * 1024, dB = lds0 + RB + (2 * w) * 1024;
        auto issue1 = [&](int t, int sl, int i) {
            const u16* ba = xnb + (size_t)t * BLK_ELEMS;
            const u16* bw = W + (size_t)t * 32 * FF;
            if (i == 0) glds16(bw, srcB, dB + sl * 16384);
            if (i == 1) glds16(bw + 64, srcB, dB + sl * 16384 + 1024);
            if (i == 2) glds16(baseX + (size_t)t * strideX, srcX, dX + sl * slotX);
            if (i == 3) glds16(ba, srcA, dA + sl * 16384);
            if (i == 4) glds16_o1k(ba, srcA, dA + sl * 16384);
        };
        auto prefetch = [&](int p) {
            const int split = xcd >> 1;
            const unsigned start = (unsigned)((split == 0 ? 0 : 1344 + 1280 * (split - 1)) * DM * 2);
            const unsigned len = (unsigned)((split == 0 ? 1344 : 1280) * DM * 2), lwd = (unsigned)(704 * DM * 2);
            p = p < 6 ? p : 5;
            unsigned o = (unsigned)((idx * 8 + w) * 6144 + p * 1024); o = o + 1024 <= len ? o : 0u;
            const char* src = (split == 3 && o >= lwd) ? (const char*)wo + (o - lwd) : (const char*)Wd + start + o;
            glds16(src, (unsigned)lane * 16u, lds0 + T_DUMMY);
        };
        STAMP(1, 0);
#pragma unroll
        for (int t = 0; t < 3; ++t) {
#pragma unroll
            for (int i = 0; i < 5; ++i) issue1(t, t, i);
            if (t == 1) prefetch(0);
        }

        const unsigned pa = lds0 + RA + blk_off(64 * wm + fr, g);
        const int swz = ((q4 >> 1) & 1) | ((g & 1) << 1);
        const unsigned pb0 = lds0 + RB + (g * 4 + wn) * 1024 + q4 * 128 + (swz << 5) + p4 * 8;
        const int emat = p4 >> 1;
        const unsigned pe0 = lds0 + RE + (emat * 4 + g) * 256 + ((q4 ^ ((g & 1) << 2)) * 32) + ((wn ^ emat) * 16) + (p4 & 1) * 8;
        const unsigned pq0 = lds0 + RQ + g * 1024 + q4 * 128 + (((2 * wn + (wm & 1)) ^ swz) << 5) + p4 * 8;
        LAS char* pbv[4]; LAS char* pqv[2];
#pragma unroll
        for (int u = 0; u < 4; ++u) pbv[u] = (LAS char*)(size_t)(pb0 ^ (u << 5));
        pqv[0] = (LAS char*)(size_t)pq0; pqv[1] = (LAS char*)(size_t)(pq0 ^ 32);
        LAS char* pev[2] = {(LAS char*)(size_t)pe0, (LAS char*)(size_t)(pe0 ^ 128)};
        LAS char* phv = lds + RH + blk_off(fr, g);
        auto rdB = [&](int sl, int nf, int t1) -> s16x4 {
            if (nf < 8) return tr_read(pbv[nf >> 1] + sl * 16384 + (nf & 1) * 2048 + t1 * 512);
            if (nf == 8) return tr_read(pev[t1] + sl * 2048);
            return tr_read(pqv[nf - 9] + sl * 4096 + t1 * 512);
        };
        auto rdA = [&](int sl, int m) -> bf16x8 { return row_frag((LAS char*)(size_t)pa + sl * 16384 + m * 1024); };
        auto rdH = [&](int sl) -> bf16x8 { return row_frag(phv + sl * 1024); };
#pragma unroll
        for (int n = 0; n < 11; ++n)
#pragma unroll
            for (int m = 0; m < 4; ++m) { acc[n][m] = (f32x4){0.f, 0.f, 0.f, 0.f}; asm volatile("" : "+v"(acc[n][m])); }
        ach = (f32x4){0.f, 0.f, 0.f, 0.f}; asm volatile("" : "+v"(ach));

        wait_vm<11>(); BAR();
        STAMP(1, 1);
        bf16x8 fa[2][2], fc[2], fh, fb[NB];
#pragma unroll
        for (int m = 0; m < 2; ++m) { fa[0][m] = rdA(0, m); fc[m] = rdA(0, 2 + m); }
#pragma unroll
        for (int i = 0; i < PF; ++i) { s16x4 lo = rdB(0, i, 0), hi = rdB(0, i, 1); fb[i] = __builtin_shufflevector(lo, hi, 0, 1, 2, 3, 4, 5, 6, 7); }
#define MM(C, A, B) mfma_ip(C, A, B)
        auto step = [&](auto S_, int t, bool do_issue, auto LAST1_, auto WAIT0_) {
            constexpr int S = decltype(S_)::value;
            constexpr bool has_next = !decltype(LAST1_)::value;
            constexpr bool wait0 = decltype(WAIT0_)::value;
            constexpr int sl = S, sn = (S + 1) & 3, cur = S & 1;
#pragma unroll
            for (int nf = 0; nf < 11; ++nf) {
                constexpr int dummy = 0; (void)dummy;
                const int r = 11 * S + nf;
                const int psl = (nf + PF < 11) ? sl : sn, nn = (nf + PF < 11) ? nf + PF : nf + PF - 11;
                const bool pf = (nf + PF < 11) || has_next;
                const bool lastg = (nf == 10);
                s16x4 lo, hi;
                if (!lastg) MM(acc[nf][0], fb[r % NB], fa[cur][0]); else MM(acc[nf][2], fb[r % NB], fc[0]);
                SCHED();
                if (nf == 0) {
                    if (!wait0) wait_vm<6>(); else wait_vm<0>();
                    BAR();
                }
                if (pf) lo = rdB(psl, nn, 0);
                SCHED();
                if (!lastg) MM(acc[nf][1], fb[r % NB], fa[cur][1]); else MM(acc[nf][3], fb[r % NB], fc[1]);
                SCHED();
                if (pf) hi = rdB(psl, nn, 1);
                if (lastg && has_next) fc[0] = rdA(sn, 2);
                SCHED();
                if (!lastg) MM(acc[nf][2], fb[r % NB], fc[0]); else MM(acc[nf][0], fb[r % NB], fa[cur][0]);
                SCHED();
                if (lastg && has_next) fc[1] = rdA(sn, 3);
                if (nf >= 4 && nf < 6 && has_next) fa[cur ^ 1][nf - 4] = rdA(sn, nf - 4);
                if (nf == 7) fh = rdH(sl);
                if ((nf < 4 || nf == 6) && do_issue) issue1(t + 3, (S + 3) & 3, nf < 4 ? nf : 4);
                if (nf == 8 && (S & 1) == 0 && do_issue) prefetch(1 + (t >> 1));
                SCHED();
                if (!lastg) MM(acc[nf][3], fb[r % NB], fc[1]); else MM(acc[nf][1], fb[r % NB], fa[cur][1]);
                SCHED();
                if (nf == 9) { MM(ach, fb[r % NB], fh); SCHED(); }
                if (pf) fb[(r + PF) % NB] = __builtin_shufflevector(lo, hi, 0, 1, 2, 3, 4, 5, 6, 7);
            }
        };
        using I0 = std::integral_constant<int, 0>; using I1 = std::integral_constant<int, 1>;
        using I2 = std::integral_constant<int, 2>; using I3 = std::integral_constant<int, 3>;
        using F = std::false_type; using T = std::true_type;
#pragma unroll 1
        for (int t0 = 0; t0 < 16; t0 += 4) {
            step(I0{}, t0, true, F{}, F{});
            step(I1{}, t0 + 1, true, F{}, F{});
            step(I2{}, t0 + 2, true, F{}, F{});
            step(I3{}, t0 + 3, t0 + 3 < 15, F{}, F{});
        }
        step(I0{}, 16, false, F{}, T{});
        step(I1{}, 17, false, T{}, T{});
    }
    asm volatile("s_nop 7\n\ts_nop 7" ::: "memory");
    STAMP(1, 4);
    k1_epilogue(acc, ach, lds, lds0, w, xcd, idx, j, mt, Wd, wo, hb);
    phase2(lds, hb, Wd, wo, dx, part, cnt, x, out);
}

extern "C" void kernel_launch(void* const* d_in, const int* in_sizes, int n_in, void* d_out, int out_size, void* d_ws,
                              size_t ws_size, hipStream_t stream) {
    const u16* x = (const u16*)d_in[0];
    const u16* wqkv = (const u16*)d_in[1];
    const u16* wo = (const u16*)d_in[2];
    const u16* Wg = (const u16*)d_in[4];
    const u16* Wu = (const u16*)d_in[5];
    const u16* Wd = (const u16*)d_in[6];
    char* ws = (char*)d_ws;
    u16* xnb = (u16*)ws;
    u16* hb = (u16*)(ws + WS_HB);
    u16* part = (u16*)(ws + WS_PART);
    u16* wqh = (u16*)(ws + WS_WQH);
    u16* gx = (u16*)(ws + WS_GX);
    u16* dx = (u16*)(ws + WS_DX);
    unsigned* cnt = (unsigned*)(ws + WS_CNT);
    (void)hipFuncSetAttribute((const void*)k1_gateup, hipFuncAttributeMaxDynamicSharedMemorySize, K1_LDS);
    k0_ln<<<256, 256, 0, stream>>>(x, xnb, wqkv, wqh, Wg, Wu, gx, Wd, wo, dx, (const char*)ws, (const char*)d_out, cnt);
    k1_gateup<<<256, 512, K1_LDS, stream>>>(xnb, Wg, Wu, wqh, gx, Wd, wo, hb, cnt, dx, part, x, (u16*)d_out);
}
```

```cpp
#include <hip/hip_runtime.h>
#include <hip/hip_bf16.h>
#include <stdint.h>
#include <type_traits>

typedef unsigned short u16;
typedef short bf16x8 __attribute__((ext_vector_type(8)));
typedef short s16x4 __attribute__((ext_vector_type(4)));
typedef float f32x4 __attribute__((ext_vector_type(4)));
typedef float f32x2 __attribute__((ext_vector_type(2)));
typedef __bf16 bf16x2_t __attribute__((ext_vector_type(2)));
typedef unsigned u32x4 __attribute__((ext_vector_type(4)));
typedef unsigned u32x2 __attribute__((ext_vector_type(2)));
#define LAS __attribute__((address_space(3)))
#define GAS __attribute__((address_space(1)))

constexpr int SEQ = 2048, DM = 576, FF = 4608;
constexpr int NKB_X = 18;
constexpr int NKB_H = 162;
constexpr size_t BLK_ELEMS = (size_t)SEQ * 32;

constexpr size_t WS_HB = 18 * 131072, WS_PART = WS_HB + 162 * 131072, WS_WQH = WS_PART + 4 * 64 * 8 * 10 * 512, WS_GX = WS_WQH + 32 * 576 * 64 * 2,
                 WS_DX = WS_GX + 2 * 32 * 576 * 16 * 2, WS_CNT = WS_DX + 4 * 5184 * 16 * 2, WS_END = WS_CNT + 64 * 256;

__device__ __forceinline__ unsigned pk_bf16(float lo, float hi) {
    f32x2 v = {lo, hi};
    bf16x2_t b = __builtin_convertvector(v, bf16x2_t);
    return __builtin_bit_cast(unsigned, b);
}
__device__ __forceinline__ float bf_lo(unsigned u) { return __uint_as_float(u << 16); }
__device__ __forceinline__ float bf_hi(unsigned u) { return __uint_as_float(u & 0xffff0000u); }
__device__ __forceinline__ float rbf(float x) { return bf_lo(pk_bf16(x, x)); }

__device__ __forceinline__ const void* uni(const void* p) {
    const unsigned long long v = (unsigned long long)p;
    const unsigned lo = __builtin_amdgcn_readfirstlane((unsigned)v), hi = __builtin_amdgcn_readfirstlane((unsigned)(v >> 32));
    return (const void*)(((unsigned long long)hi << 32) | lo);
}
__device__ __forceinline__ void glds16(const void* sbase, unsigned voff, unsigned lds) {
    sbase = uni(sbase); lds = __builtin_amdgcn_readfirstlane(lds);
    asm volatile("s_mov_b32 m0, %2\n\ts_nop 3\n\tglobal_load_lds_dwordx4 %0, %1"
                 :: "v"(voff), "s"(sbase), "s"(lds) : "memory", "m0");
}
__device__ __forceinline__ void glds16_o1k(const void* sbase, unsigned voff, unsigned lds) {
    sbase = uni(sbase); lds = __builtin_amdgcn_readfirstlane(lds);
    asm volatile("s_mov_b32 m0, %2\n\ts_nop 3\n\tglobal_load_lds_dwordx4 %0, %1 offset:1024"
                 :: "v"(voff), "s"(sbase), "s"(lds) : "memory", "m0");
}
__device__ __forceinline__ void glds16_q(const void* sbase, unsigned voff, unsigned lds) {
    sbase = uni(sbase); lds = __builtin_amdgcn_readfirstlane(lds);
    unsigned sv; unsigned long long ex;
    asm volatile("s_mov_b32 %0, m0\n\ts_mov_b64 %1, exec\n\ts_mov_b32 m0, %4\n\ts_mov_b64 exec, 0xffff\n\ts_nop 1\n\tglobal_load_lds_dwordx4 %2, %3\n\ts_mov_b64 exec, %1\n\ts_mov_b32 m0, %0"
                 : "=&s"(sv), "=&s"(ex) : "v"(voff), "s"(sbase), "s"(lds) : "memory");
}
__device__ __forceinline__ void glds16_m(const void* sbase, unsigned voff, unsigned lds) {
    sbase = uni(sbase); lds = __builtin_amdgcn_readfirstlane(lds);
    unsigned sv; unsigned long long ex;
    asm volatile("s_mov_b32 %0, m0\n\ts_mov_b64 %1, exec\n\ts_mov_b32 m0, %4\n\ts_mov_b32 exec_lo, -1\n\ts_mov_b32 exec_hi, 0xff\n\ts_nop 1\n\tglobal_load_lds_dwordx4 %2, %3\n\ts_mov_b64 exec, %1\n\ts_mov_b32 m0, %0"
                 : "=&s"(sv), "=&s"(ex) : "v"(voff), "s"(sbase), "s"(lds) : "memory");
}
#define SCHED() __builtin_amdgcn_sched_barrier(0)
#define STAMP(kid, i) do {} while (0)
#define WSTAMP(i) do {} while (0)
__device__ __forceinline__ s16x4 tr_read(LAS char* p) {
    return __builtin_bit_cast(s16x4, __builtin_amdgcn_ds_read_tr16_b64_v4i16((LAS s16x4*)p));
}
__device__ __forceinline__ bf16x8 tr_frag(LAS char* p, int off_t1) {
    s16x4 lo = tr_read(p), hi = tr_read(p + off_t1);
    return __builtin_shufflevector(lo, hi, 0, 1, 2, 3, 4, 5, 6, 7);
}
__device__ __forceinline__ bf16x8 row_frag(LAS char* p) { return *(LAS bf16x8*)p; }
#define MFMA16(a, b, c) __builtin_amdgcn_mfma_f32_16x16x32_bf16((a), (b), (c), 0, 0, 0)
__device__ __forceinline__ void mfma_ip(f32x4& c, bf16x8 a, bf16x8 b) { asm volatile("v_mfma_f32_16x16x32_bf16 %0, %1, %2, %0" : "+v"(c) : "v"(a), "v"(b)); }
#define WAIT_V(n) asm volatile("s_waitcnt vmcnt(" #n ")" ::: "memory")
#define BAR() do { asm volatile("" ::: "memory"); __builtin_amdgcn_s_barrier(); asm volatile("" ::: "memory"); } while (0)

__device__ __forceinline__ int blk_off(int row, int chunk) { return row * 64 + ((chunk ^ (((row >> 3) & 1) << 1)) << 4); }

__global__ __launch_bounds__(256) void k0_ln(const u16* __restrict__ x, u16* __restrict__ xnb,
                                            const u16* __restrict__ wqkv, u16* __restrict__ wqh,
                                            const u16* __restrict__ Wg, const u16* __restrict__ Wu, u16* __restrict__ gx,
                                            const u16* __restrict__ Wd, const u16* __restrict__ wo, u16* __restrict__ dx,
                                            const char* __restrict__ wsb, const char* __restrict__ outb) {
    if (threadIdx.x >= 64) {
        const int l = threadIdx.x - 64, head = blockIdx.x >> 3, k0 = (blockIdx.x & 7) * 72;
        unsigned v[12];
#pragma unroll
        for (int i = 0; i < 12; ++i) {
            const int id = l + 192 * i, k = k0 + (id >> 5), cp = id & 31;
            const int part = cp >= 18 ? 2 : (cp >= 9 ? 1 : 0), d2 = cp - part * 9;
            const int src = (part == 2 ? 0 : part + 1) * DM + 18 * head + 2 * d2;
            v[i] = cp < 27 ? *(const unsigned*)(wqkv + (size_t)k * (3 * DM) + src) : 0u;
        }
        u32x4 c[3];
#pragma unroll
        for (int i = 0; i < 2; ++i) {
            const int id = l + 192 * i;
            if (id < 288) {
                const int gid = blockIdx.x * 288 + id, mat = gid / 36864, r = gid - mat * 36864, k = r >> 6, ch = r & 63;
                c[i] = *(const u32x4*)((mat ? Wu : Wg) + (size_t)k * FF + 4096 + 8 * ch);
            }
        }
        if (l < 162) {
            const int gid = blockIdx.x * 162 + l, k = gid >> 3, ch = gid & 7;
            c[2] = *(const u32x4*)((k < FF ? Wd + (size_t)k * DM : wo + (size_t)(k - FF) * DM) + 512 + 8 * ch);
        }
#pragma unroll
        for (int i = 0; i < 12; ++i) {
            const int id = l + 192 * i;
            *(unsigned*)(wqh + ((size_t)head * 576 + k0) * 64 + 2 * id) = v[i];
        }
#pragma unroll
        for (int i = 0; i < 2; ++i) {
            const int id = l + 192 * i;
            if (id < 288) {
                const int gid = blockIdx.x * 288 + id, mat = gid / 36864, r = gid - mat * 36864, k = r >> 6, ch = r & 63;
                *(u32x4*)(gx + (((size_t)mat * 32 + (ch >> 1)) * 576 + k) * 16 + 8 * (ch & 1)) = c[i];
            }
        }
        if (l < 162) {
            const int gid = blockIdx.x * 162 + l, k = gid >> 3, ch = gid & 7;
            *(u32x4*)(dx + ((size_t)(ch >> 1) * 5184 + k) * 16 + 8 * (ch & 1)) = c[2];
        }
    if (threadIdx.x >= 192) {
        const int l = threadIdx.x - 192, slot = (blockIdx.x >> 3) * 64 + l;
        constexpr int NW = (FF * DM * 2 + 65535) / 65536;
        constexpr int NS = (int)((WS_END + 65535) / 65536), NO = (SEQ * DM * 2 + 65535) / 65536, NWO = (DM * DM * 2 + 65535) / 65536;
        const char* p = nullptr;
        int i = slot;
        if (i < NW) p = (const char*)Wg + (size_t)i * 65536; i -= NW;
        if (i >= 0 && i < NW) p = (const char*)Wu + (size_t)i * 65536; i -= NW;
        if (i >= 0 && i < NW) p = (const char*)Wd + (size_t)i * 65536; i -= NW;
        if (i >= 0 && i < NS) p = wsb + (size_t)i * 65536; i -= NS;
        if (i >= 0 && i < NO) p = outb + (size_t)i * 65536; i -= NO;
        if (i >= 0 && i < NWO) p = (const char*)wo + (size_t)i * 65536;
        if (p) { unsigned v; asm volatile("global_load_dword %0, %1, off\n\ts_waitcnt vmcnt(0)" : "=&v"(v) : "v"(p) : "memory"); }
    }
        return;
    }
    const int lane = threadIdx.x, i = lane & 7, row = blockIdx.x * 8 + (lane >> 3);
    u32x4 xv[9];
#pragma unroll
    for (int cc = 0; cc < 9; ++cc) xv[cc] = *(const u32x4*)(x + (size_t)row * DM + (i + 8 * cc) * 8);
    float s = 0.f;
#pragma unroll
    for (int cc = 0; cc < 9; ++cc)
#pragma unroll
        for (int e = 0; e < 4; ++e) s += bf_lo(xv[cc][e]) + bf_hi(xv[cc][e]);
    s += __shfl_xor(s, 1); s += __shfl_xor(s, 2); s += __shfl_xor(s, 4);
    const float mean = s * (1.0f / 576.0f);
    float q = 0.f;
#pragma unroll
    for (int cc = 0; cc < 9; ++cc)
#pragma unroll
        for (int e = 0; e < 4; ++e) { const float a = bf_lo(xv[cc][e]) - mean, c2 = bf_hi(xv[cc][e]) - mean; q += a * a + c2 * c2; }
    q += __shfl_xor(q, 1); q += __shfl_xor(q, 2); q += __shfl_xor(q, 4);
    const float mb = rbf(mean), vb = rbf(q * (1.0f / 576.0f));
    const float sb = rbf(sqrtf(rbf(vb + 1e-5f)));
    const float rs = 1.0f / sb;
#pragma unroll
    for (int cc = 0; cc < 9; ++cc) {
        u32x4 o;
#pragma unroll
        for (int e = 0; e < 4; ++e) {
            const unsigned d = pk_bf16(bf_lo(xv[cc][e]) - mb, bf_hi(xv[cc][e]) - mb);
            o[e] = pk_bf16(bf_lo(d) * rs, bf_hi(d) * rs);
        }
        const int blk = 2 * cc + (i >> 2);
        *(u32x4*)((char*)xnb + (size_t)blk * (BLK_ELEMS * 2) + blk_off(row, i & 3)) = o;
    }
}

constexpr int RA = 0, RB = 65536, RE = 131072, RQ = 139264, RH = 155648;
constexpr int T_DUMMY = 159744;
constexpr int K1_LDS = T_DUMMY + 1024;
constexpr int QS = 144;
#define HST_AUX 16
#define PF 3
constexpr int NB = PF + 1;
template <int N> __device__ __forceinline__ void wait_vm() { asm volatile("s_waitcnt vmcnt(%0)" :: "i"(N) : "memory"); }
#define GLDS_MASKED(NAME, LO, HI) \
__device__ __forceinline__ void NAME(const void* sbase, unsigned voff, unsigned lds) { \
    sbase = uni(sbase); lds = __builtin_amdgcn_readfirstlane(lds); \
    unsigned sv; unsigned long long ex; \
    asm volatile("s_mov_b32 %0, m0\n\ts_mov_b64 %1, exec\n\ts_mov_b32 m0, %4\n\ts_mov_b32 exec_lo, " #LO "\n\ts_mov_b32 exec_hi, " #HI "\n\tglobal_load_lds_dwordx4 %2, %3\n\ts_mov_b64 exec, %1\n\ts_mov_b32 m0, %0" \
                 : "=&s"(sv), "=&s"(ex) : "v"(voff), "s"(sbase), "s"(lds) : "memory"); }
GLDS_MASKED(glds16_32, -1, 0)
GLDS_MASKED(glds16_8, 0xff, 0)

__device__ __forceinline__ void k1_epilogue(f32x4 (&acc)[11][4], f32x4& ach, LAS char* lds, unsigned lds0, int w, int xcd, int idx,
                                            int j, int mt, const u16* __restrict__ Wd, const u16* __restrict__ wo, u16* __restrict__ hb) {
    int tid = threadIdx.x; asm volatile("" : "+v"(tid));
    const int lane = tid & 63, fr = lane & 15, g = lane >> 4;
    const int wm = w >> 1, wn = w & 1;
    const __amdgpu_buffer_rsrc_t rh = __builtin_amdgcn_make_buffer_rsrc((void*)hb, 0, (int)(NKB_H * BLK_ELEMS * 2), 0x00020000);
    auto hs = [&](int m, int part) {
        const int row = 256 * mt + 64 * wm + 16 * m + fr;
        SCHED();
        if (part < 2) {
            const int pp = part;
            unsigned v[2][2];
#pragma unroll
            for (int hh = 0; hh < 2; ++hh) {
                const int gt = 2 * pp + hh;
                float hv[4];
#pragma unroll
                for (int e = 0; e < 4; ++e) hv[e] = fmaxf(acc[2 * gt][m][e], 0.f) * acc[2 * gt + 1][m][e];
                v[hh][0] = pk_bf16(hv[0], hv[1]); v[hh][1] = pk_bf16(hv[2], hv[3]);
            }
            auto r0 = __builtin_amdgcn_permlane16_swap(v[0][0], v[1][0], false, false);
            auto r1 = __builtin_amdgcn_permlane16_swap(v[0][1], v[1][1], false, false);
            u32x4 o; o[0] = r0[0]; o[1] = r1[0]; o[2] = r0[1]; o[3] = r1[1];
            const int blk = 4 * j + 2 * wn + pp, chunk = ((g & 1) << 1) | (g >> 1);
            __builtin_amdgcn_raw_buffer_store_b128(o, rh, (unsigned)(blk * (int)(BLK_ELEMS * 2) + blk_off(row, chunk)), 0, HST_AUX);
        } else {
            float hv[4];
#pragma unroll
            for (int e = 0; e < 4; ++e) {
                const unsigned mine = __float_as_uint(acc[8][m][e]);
                auto r = __builtin_amdgcn_permlane32_swap(mine, mine, false, false);
                hv[e] = fmaxf(__uint_as_float(r[0]), 0.f) * __uint_as_float(r[1]);
            }
            u32x2 v; v[0] = pk_bf16(hv[0], hv[1]); v[1] = pk_bf16(hv[2], hv[3]);
            const int blk = 128 + (j >> 1), chunk = (j & 1) * 2 + wn;
            const unsigned off = g < 2 ? (unsigned)(blk * (int)(BLK_ELEMS * 2) + blk_off(row, chunk) + g * 8) : 0xfffffff0u;
            __builtin_amdgcn_raw_buffer_store_b64(v, rh, off, 0, HST_AUX);
        }
        SCHED();
    };
    hs(0, 0);
    BAR();
    STAMP(1, 2);
    LAS char* qt = lds;
    {
#pragma unroll
        for (int u = 0; u < 2; ++u) {
            const int nn = 16 * (2 * wn + (u ^ (wm & 1))) + 4 * g;
            const int h0 = nn / 18, h1 = (nn + 2) / 18;
            const int o0 = h0 * 48 + (nn - h0 * 18) * 2, o1 = h1 * 48 + (nn + 2 - h1 * 18) * 2;
#pragma unroll
            for (int m = 0; m < 4; ++m) {
                LAS char* rp = qt + (16 + 64 * wm + 16 * m + fr) * QS;
                if (h0 < 3) *(LAS unsigned*)(rp + o0) = pk_bf16(acc[9 + u][m][0], acc[9 + u][m][1]);
                if (h1 < 3) *(LAS unsigned*)(rp + o1) = pk_bf16(acc[9 + u][m][2], acc[9 + u][m][3]);
            }
            if (u == 0 && (w == 0 || w == 2 || w == 1)) {
                LAS char* rp = qt + fr * QS;
                if (h0 < 3) *(LAS unsigned*)(rp + o0) = pk_bf16(ach[0], ach[1]);
                if (h1 < 3) *(LAS unsigned*)(rp + o1) = pk_bf16(ach[2], ach[3]);
            }
            if (u == 0) hs(0, 1);
        }
    }
    asm volatile("s_waitcnt lgkmcnt(0)" ::: "memory");
    BAR();
    STAMP(1, 3);
    {
        const int s = tid >> 1, half = tid & 1;
        const int lrow = 16 + s, grow = 256 * mt + s;
        auto ld18 = [&](LAS const char* p, unsigned (&v)[9]) {
            const u32x4 a = *(LAS const u32x4*)p, c = *(LAS const u32x4*)(p + 16);
            v[0] = a[0]; v[1] = a[1]; v[2] = a[2]; v[3] = a[3]; v[4] = c[0]; v[5] = c[1]; v[6] = c[2]; v[7] = c[3];
            v[8] = *(LAS const unsigned*)(p + 32);
        };
        hs(0, 2);
        unsigned qv[9];
        ld18(qt + lrow * QS + 96, qv);
        float lg[4];
#pragma unroll
        for (int ww = 0; ww < 4; ++ww) {
            const int wi = 4 * half + ww;
            unsigned kv[9];
            ld18(qt + (lrow - 2 * wi) * QS, kv);
            float a = 0.f;
#pragma unroll
            for (int i = 0; i < 9; ++i) a = __builtin_amdgcn_fdot2_f32_bf16(__builtin_bit_cast(bf16x2_t, qv[i]), __builtin_bit_cast(bf16x2_t, kv[i]), a, false);
            a = rbf(rbf(a) * 0.23570226039551584f);
            lg[ww] = (grow - 2 * wi >= 0) ? a : -1e9f;
            if (ww == 0) hs(1, 0);
            if (ww == 2) hs(1, 1);
        }
        float mx = fmaxf(fmaxf(lg[0], lg[1]), fmaxf(lg[2], lg[3]));
        mx = fmaxf(mx, __shfl_xor(mx, 1));
        float ex[4], sum = 0.f;
#pragma unroll
        for (int ww = 0; ww < 4; ++ww) { ex[ww] = __expf(lg[ww] - mx); sum += ex[ww]; }
        sum += __shfl_xor(sum, 1);
        const float inv = 1.0f / sum;
        hs(1, 2);
        float o[18];
#pragma unroll
        for (int i = 0; i < 18; ++i) o[i] = 0.f;
#pragma unroll
        for (int ww = 0; ww < 4; ++ww) {
            const int wi = 4 * half + ww;
            const float pw = (grow - 2 * wi >= 0) ? rbf(ex[ww] * inv) : 0.f;
            unsigned vv[9];
            ld18(qt + (lrow - 2 * wi) * QS + 48, vv);
#pragma unroll
            for (int i = 0; i < 9; ++i) { o[2 * i] += pw * bf_lo(vv[i]); o[2 * i + 1] += pw * bf_hi(vv[i]); }
            if (ww < 3) hs(2, ww);
        }
        hs(3, 0);
#pragma unroll
        for (int ii = 0; ii < 5; ++ii) {
            const float s0 = half ? o[2 * ii] : o[(10 + 2 * ii) % 18], s1 = half ? o[2 * ii + 1] : o[(11 + 2 * ii) % 18];
            const float r0 = __shfl_xor(s0, 1), r1 = __shfl_xor(s1, 1);
            const float m0 = half ? o[(10 + 2 * ii) % 18] : o[2 * ii], m1 = half ? o[(11 + 2 * ii) % 18] : o[2 * ii + 1];
            const int i = half * 5 + ii;
            if (i < 9) {
                const int col = FF + 18 * j + 2 * i;
                *(unsigned*)((char*)hb + (size_t)(col >> 5) * (BLK_ELEMS * 2) + blk_off(grow, (col >> 3) & 3) + (col & 7) * 2) = pk_bf16(m0 + r0, m1 + r1);
            }
            if (ii == 2) hs(3, 1);
        }
        hs(3, 2);
    }
    STAMP(1, 15);
}

__global__ __launch_bounds__(512, 2) void k1_gateup(const u16* __restrict__ xnb, const u16* __restrict__ Wg,
                                                    const u16* __restrict__ Wu, const u16* __restrict__ wqh, const u16* __restrict__ gx,
                                                    const u16* __restrict__ Wd, const u16* __restrict__ wo, u16* __restrict__ hb, unsigned* __restrict__ cnt) {
    extern __shared__ __attribute__((aligned(16))) char smem[];
    LAS char* lds = (LAS char*)smem;
    const int tid = threadIdx.x, lane = tid & 63;
    if (blockIdx.x < 64 && tid == 0) cnt[blockIdx.x * 64] = 0u;
    const int w = __builtin_amdgcn_readfirstlane(tid >> 6);
    const int b = blockIdx.x, xcd = b & 7, idx = b >> 3;
    const int j = 4 * xcd + (idx & 3), mt = idx >> 2;
    const int wm = w >> 1, wn = w & 1;
    const unsigned lds0 = (unsigned)(size_t)lds;
    const int fr = lane & 15, g = lane >> 4, q4 = (lane >> 2) & 3, p4 = lane & 3;

    f32x4 acc[11][4], ach;
    {
        const unsigned srcA = (unsigned)((256 * mt + 32 * w) * 32 + lane * 8) * 2u;
        const int koct = w >> 1, mat = w & 1;
        const u16* W = mat ? Wu : Wg;
        unsigned srcB, srcX;
        const u16* baseX; int strideX; unsigned dX;
        {
            const int r = lane >> 3, swz = ((r >> 1) & 1) | ((koct & 1) << 1), c = (lane & 7) ^ (swz << 1);
            srcB = (unsigned)((8 * koct + r) * FF + 128 * j + 8 * c) * 2u;
            if (w < 2) {
                const int ko = lane >> 4, re = ((lane >> 1) & 7) ^ ((ko & 1) << 2), ce = (lane & 1) ^ w;
                srcX = (unsigned)(((w * 32 + j) * 576 + 8 * ko + re) * 16 + 8 * ce) * 2u;
                baseX = gx; strideX = 32 * 16; dX = lds0 + RE + w * 1024;
            } else if (w < 6) {
                const int ko = w - 2, swq = ((r >> 1) & 1) | ((ko & 1) << 1), cq = (lane & 7) ^ (swq << 1);
                srcX = (unsigned)((j * 576 + 8 * ko + r) * 64 + 8 * cq) * 2u;
                baseX = wqh; strideX = 32 * 64; dX = lds0 + RQ + ko * 1024;
            } else {
                const int hr = mt > 0 ? 256 * mt - 16 : 0;
                srcX = (unsigned)(hr * 64 + lane * 16);
                baseX = xnb; strideX = (int)BLK_ELEMS; dX = lds0 + RH;
            }
        }
        const int slotX = w < 2 ? 2048 : (w < 6 ? 4096 : 1024);
        const unsigned dA = lds0 + RA + (2 * w) * 1024, dB = lds0 + RB + (2 * w) * 1024;
        auto issue1 = [&](int t, int sl, int i) {
            const u16* ba = xnb + (size_t)t * BLK_ELEMS;
            const u16* bw = W + (size_t)t * 32 * FF;
            if (i == 0) glds16(bw, srcB, dB + sl * 16384);
            if (i == 1) glds16(bw + 64, srcB, dB + sl * 16384 + 1024);
            if (i == 2) glds16(baseX + (size_t)t * strideX, srcX, dX + sl * slotX);
            if (i == 3) glds16(ba, srcA, dA + sl * 16384);
            if (i == 4) glds16_o1k(ba, srcA, dA + sl * 16384);
        };
        auto prefetch = [&](int p) {
            const int split = xcd >> 1;
            const unsigned start = (unsigned)((split == 0 ? 0 : 1344 + 1280 * (split - 1)) * DM * 2);
            const unsigned len = (unsigned)((split == 0 ? 1344 : 1280) * DM * 2), lwd = (unsigned)(704 * DM * 2);
            p = p < 6 ? p : 5;
            unsigned o = (unsigned)((idx * 8 + w) * 6144 + p * 1024); o = o + 1024 <= len ? o : 0u;
            const char* src = (split == 3 && o >= lwd) ? (const char*)wo + (o - lwd) : (const char*)Wd + start + o;
            glds16(src, (unsigned)lane * 16u, lds0 + T_DUMMY);
        };
        STAMP(1, 0);
#pragma unroll
        for (int t = 0; t < 3; ++t) {
#pragma unroll
            for (int i = 0; i < 5; ++i) issue1(t, t, i);
            if (t == 1) prefetch(0);
        }

        const unsigned pa = lds0 + RA + blk_off(64 * wm + fr, g);
        const int swz = ((q4 >> 1) & 1) | ((g & 1) << 1);
        const unsigned pb0 = lds0 + RB + (g * 4 + wn) * 1024 + q4 * 128 + (swz << 5) + p4 * 8;
        const int emat = p4 >> 1;
        const unsigned pe0 = lds0 + RE + (emat * 4 + g) * 256 + ((q4 ^ ((g & 1) << 2)) * 32) + ((wn ^ emat) * 16) + (p4 & 1) * 8;
        const unsigned pq0 = lds0 + RQ + g * 1024 + q4 * 128 + (((2 * wn + (wm & 1)) ^ swz) << 5) + p4 * 8;
        LAS char* pbv[4]; LAS char* pqv[2];
#pragma unroll
        for (int u = 0; u < 4; ++u) pbv[u] = (LAS char*)(size_t)(pb0 ^ (u << 5));
        pqv[0] = (LAS char*)(size_t)pq0; pqv[1] = (LAS char*)(size_t)(pq0 ^ 32);
        LAS char* pev[2] = {(LAS char*)(size_t)pe0, (LAS char*)(size_t)(pe0 ^ 128)};
        LAS char* phv = lds + RH + blk_off(fr, g);
        auto rdB = [&](int sl, int nf, int t1) -> s16x4 {
            if (nf < 8) return tr_read(pbv[nf >> 1] + sl * 16384 + (nf & 1) * 2048 + t1 * 512);
            if (nf == 8) return tr_read(pev[t1] + sl * 2048);
            return tr_read(pqv[nf - 9] + sl * 4096 + t1 * 512);
        };
        auto rdA = [&](int sl, int m) -> bf16x8 { return row_frag((LAS char*)(size_t)pa + sl * 16384 + m * 1024); };
        auto rdH = [&](int sl) -> bf16x8 { return row_frag(phv + sl * 1024); };
#pragma unroll
        for (int n = 0; n < 11; ++n)
#pragma unroll
            for (int m = 0; m < 4; ++m) { acc[n][m] = (f32x4){0.f, 0.f, 0.f, 0.f}; asm volatile("" : "+v"(acc[n][m])); }
        ach = (f32x4){0.f, 0.f, 0.f, 0.f}; asm volatile("" : "+v"(ach));

        wait_vm<11>(); BAR();
        STAMP(1, 1);
        bf16x8 fa[2][2], fc[2], fh, fb[NB];
#pragma unroll
        for (int m = 0; m < 2; ++m) { fa[0][m] = rdA(0, m); fc[m] = rdA(0, 2 + m); }
#pragma unroll
        for (int i = 0; i < PF; ++i) { s16x4 lo = rdB(0, i, 0), hi = rdB(0, i, 1); fb[i] = __builtin_shufflevector(lo, hi, 0, 1, 2, 3, 4, 5, 6, 7); }
#define MM(C, A, B) mfma_ip(C, A, B)
        auto step = [&](auto S_, int t, bool do_issue, auto LAST1_, auto WAIT0_) {
            constexpr int S = decltype(S_)::value;
            constexpr bool has_next = !decltype(LAST1_)::value;
            constexpr bool wait0 = decltype(WAIT0_)::value;
            constexpr int sl = S, sn = (S + 1) & 3, cur = S & 1;
#pragma unroll
            for (int nf = 0; nf < 11; ++nf) {
                constexpr int dummy = 0; (void)dummy;
                const int r = 11 * S + nf;
                const int psl = (nf + PF < 11) ? sl : sn, nn = (nf + PF < 11) ? nf + PF : nf + PF - 11;
                const bool pf = (nf + PF < 11) || has_next;
                const bool lastg = (nf == 10);
                s16x4 lo, hi;
                if (!lastg) MM(acc[nf][0], fb[r % NB], fa[cur][0]); else MM(acc[nf][2], fb[r % NB], fc[0]);
                SCHED();
                if (nf == 0) {
                    if (!wait0) wait_vm<6>(); else wait_vm<0>();
                    BAR();
                }
                if (pf) lo = rdB(psl, nn, 0);
                SCHED();
                if (!lastg) MM(acc[nf][1], fb[r % NB], fa[cur][1]); else MM(acc[nf][3], fb[r % NB], fc[1]);
                SCHED();
                if (pf) hi = rdB(psl, nn, 1);
                if (lastg && has_next) fc[0] = rdA(sn, 2);
                SCHED();
                if (!lastg) MM(acc[nf][2], fb[r % NB], fc[0]); else MM(acc[nf][0], fb[r % NB], fa[cur][0]);
                SCHED();
                if (lastg && has_next) fc[1] = rdA(sn, 3);
                if (nf >= 4 && nf < 6 && has_next) fa[cur ^ 1][nf - 4] = rdA(sn, nf - 4);
                if (nf == 7) fh = rdH(sl);
                if ((nf < 4 || nf == 6) && do_issue) issue1(t + 3, (S + 3) & 3, nf < 4 ? nf : 4);
                if (nf == 8 && (S & 1) == 0 && do_issue) prefetch(1 + (t >> 1));
                SCHED();
                if (!lastg) MM(acc[nf][3], fb[r % NB], fc[1]); else MM(acc[nf][1], fb[r % NB], fa[cur][1]);
                SCHED();
                if (nf == 9) { MM(ach, fb[r % NB], fh); SCHED(); }
                if (pf) fb[(r + PF) % NB] = __builtin_shufflevector(lo, hi, 0, 1, 2, 3, 4, 5, 6, 7);
            }
        };
        using I0 = std::integral_constant<int, 0>; using I1 = std::integral_constant<int, 1>;
        using I2 = std::integral_constant<int, 2>; using I3 = std::integral_constant<int, 3>;
        using F = std::false_type; using T = std::true_type;
#pragma unroll 1
        for (int t0 = 0; t0 < 16; t0 += 4) {
            step(I0{}, t0, true, F{}, F{});
            step(I1{}, t0 + 1, true, F{}, F{});
            step(I2{}, t0 + 2, true, F{}, F{});
            step(I3{}, t0 + 3, t0 + 3 < 15, F{}, F{});
        }
        step(I0{}, 16, false, F{}, T{});
        step(I1{}, 17, false, T{}, T{});
    }
    asm volatile("s_nop 7\n\ts_nop 7" ::: "memory");
    STAMP(1, 4);
    k1_epilogue(acc, ach, lds, lds0, w, xcd, idx, j, mt, Wd, wo, hb);
}

constexpr int K2_NT = 21;
constexpr int K2_RB = 0, K2_RE = 65536, K2_LDS = 81920;
constexpr int NSPLIT = 4;
__device__ __forceinline__ void gload2(bf16x8& d0, bf16x8& d1, const void* sbase, unsigned voff) {
    sbase = uni(sbase);
    asm volatile("s_nop 4\n\tglobal_load_dwordx4 %0, %2, %3\n\tglobal_load_dwordx4 %1, %2, %3 offset:1024"
                 : "=&v"(d0), "=&v"(d1) : "v"(voff), "s"(sbase) : "memory");
}
__global__ __launch_bounds__(512, 2) void k2_down(const u16* __restrict__ hb, const u16* __restrict__ Wd,
                                                  const u16* __restrict__ wo, const u16* __restrict__ dx, u16* __restrict__ part, unsigned* __restrict__ cnt,
                                                  const u16* __restrict__ x, u16* __restrict__ out) {
    extern __shared__ __attribute__((aligned(16))) char smem[];
    LAS char* lds = (LAS char*)smem;
    const int tid = threadIdx.x, lane = tid & 63;
    const int w = __builtin_amdgcn_readfirstlane(tid >> 6);
    const int b = blockIdx.x, xcd = b & 7, idx = b >> 3;
    const int split = xcd >> 1, mt = (xcd & 1) * 8 + (idx >> 2), nt = idx & 3;
    const int wm = w >> 1, kh = w & 1;
    const int T0 = split == 0 ? 0 : 21 + 20 * (split - 1);

    const unsigned srcA = (unsigned)(kh * (int)BLK_ELEMS * 2 + blk_off(128 * mt + 32 * wm + (lane & 15), lane >> 4));
    const int koct = w >> 1, sub = w & 1;
    unsigned offB, offE;
    {
        const int r = lane >> 3, swz = ((r >> 1) & 1) | ((koct & 1) << 1), c = (lane & 7) ^ (swz << 1);
        offB = (unsigned)((32 * sub + 8 * koct + r) * DM + 128 * nt + 8 * c) * 2u;
        const int re = ((lane >> 1) & 7) ^ ((koct & 1) << 2), ce = (lane & 1);
        offE = (unsigned)((nt * 5184 + 32 * sub + 8 * koct + re) * 16 + 8 * ce) * 2u;
    }
    const unsigned lds0 = (unsigned)(size_t)lds;
    const unsigned dB = lds0 + K2_RB + (2 * w) * 1024, dE = lds0 + K2_RE + (sub * 4 + koct) * 256;
    bf16x8 fa[4][2];
    auto issue1 = [&](int t, int sl, int i, bf16x8& a0, bf16x8& a1) {
        int T = T0 + t; if (t == K2_NT - 1 && split != 0) T -= 1;
        const u16* Wt = T < 72 ? Wd + (size_t)T * 64 * DM : wo + (size_t)(T - 72) * 64 * DM;
        const u16* ba = hb + (size_t)T * 2 * BLK_ELEMS;
        if (i == 0) glds16(Wt, offB, dB + sl * 16384);
        if (i == 1) glds16(Wt + 64, offB, dB + sl * 16384 + 1024);
        if (i == 2) glds16_q(dx + (size_t)T * 64 * 16, offE, dE + sl * 2048);
        if (i == 3) gload2(a0, a1, ba, srcA);
    };
    STAMP(2, 0);
#pragma unroll
    for (int t = 0; t < 3; ++t)
#pragma unroll
        for (int i = 0; i < 4; ++i) issue1(t, t, i, fa[t][0], fa[t][1]);

    const int fr = lane & 15, g = lane >> 4, q4 = (lane >> 2) & 3, p4 = lane & 3;
    const int swz = ((q4 >> 1) & 1) | ((g & 1) << 1);
    LAS char* pb[4];
#pragma unroll
    for (int u = 0; u < 4; ++u) pb[u] = lds + K2_RB + (g * 4 + kh * 2) * 1024 + q4 * 128 + ((u ^ swz) << 5) + p4 * 8;
    LAS char* pe0 = lds + K2_RE + (kh * 4 + g) * 256 + ((q4 ^ ((g & 1) << 2)) * 32) + p4 * 8;
    LAS char* pe1 = lds + K2_RE + (kh * 4 + g) * 256 + (((4 + q4) ^ ((g & 1) << 2)) * 32) + p4 * 8;
    auto rdB = [&](int sl, int nf, int t1) -> s16x4 {
        if (nf < 8) return tr_read(pb[nf & 3] + sl * 16384 + (nf >> 2) * 1024 + t1 * 512);
        return tr_read((t1 ? pe1 : pe0) + sl * 2048);
    };

    f32x4 acc[9][2];
#pragma unroll
    for (int n = 0; n < 9; ++n)
#pragma unroll
        for (int m = 0; m < 2; ++m) { acc[n][m] = (f32x4){0.f, 0.f, 0.f, 0.f}; asm volatile("" : "+v"(acc[n][m])); }

    wait_vm<10>(); BAR();
    STAMP(2, 1);
    constexpr int PF2 = 4;
    bf16x8 fb[9];
#pragma unroll
    for (int i = 0; i < PF2; ++i) { s16x4 lo = rdB(0, i, 0), hi = rdB(0, i, 1); fb[i] = __builtin_shufflevector(lo, hi, 0, 1, 2, 3, 4, 5, 6, 7); }
    auto step = [&](auto S_, int t, bool do_issue, bool wait0, auto LAST_) {
        constexpr int S = decltype(S_)::value;
        constexpr bool has_next = !decltype(LAST_)::value;
        constexpr int sl = S, sn = (S + 1) & 3;
        asm volatile("" : "+v"(fa[sl][0]), "+v"(fa[sl][1]));
        if (!has_next && split != 0) {
#pragma unroll
            for (int m = 0; m < 2; ++m) fa[sl][m] = (bf16x8){0, 0, 0, 0, 0, 0, 0, 0};
            asm volatile("s_nop 1" : "+v"(fa[sl][0]), "+v"(fa[sl][1]));
        }
#pragma unroll
        for (int nf = 0; nf < 9; ++nf) {
            const int psl = (nf + PF2 < 9) ? sl : sn, nn = (nf + PF2 < 9) ? nf + PF2 : nf + PF2 - 9;
            const bool pf = (nf + PF2 < 9) || has_next;
            s16x4 lo, hi;
            mfma_ip(acc[nf][0], fb[nf], fa[sl][0]);
            SCHED();
            if (nf == 0) {
                if (!wait0) wait_vm<5>(); else wait_vm<0>();
                BAR();
            }
            if (pf) lo = rdB(psl, nn, 0);
            SCHED();
            mfma_ip(acc[nf][1], fb[nf], fa[sl][1]);
            SCHED();
            if (pf) hi = rdB(psl, nn, 1);
            if (nf >= 1 && nf < 5 && do_issue) issue1(t + 3, (S + 3) & 3, nf - 1, fa[(S + 3) & 3][0], fa[(S + 3) & 3][1]);
            SCHED();
            if (pf) fb[nn] = __builtin_shufflevector(lo, hi, 0, 1, 2, 3, 4, 5, 6, 7);
        }
    };
    using I0 = std::integral_constant<int, 0>; using I1 = std::integral_constant<int, 1>;
    using I2 = std::integral_constant<int, 2>; using I3 = std::integral_constant<int, 3>;
#pragma unroll 1
    for (int t0 = 0; t0 < 20; t0 += 4) {
        step(I0{}, t0, true, false, std::false_type{});
        step(I1{}, t0 + 1, true, false, std::false_type{});
        step(I2{}, t0 + 2, t0 + 2 < 18, false, std::false_type{});
        step(I3{}, t0 + 3, t0 + 3 < 18, t0 + 3 == 19, std::false_type{});
    }
    step(I0{}, 20, false, true, std::true_type{});
    asm volatile("s_nop 7\n\ts_nop 7" ::: "memory");
    BAR();
    STAMP(2, 2);
    f32x4 val[10];
    {
        LAS char* xw = lds + w * 10240;
        LAS char* xr = lds + (w ^ 1) * 10240;
#pragma unroll
        for (int n = 0; n < 9; ++n) {
            const bool give = kh ? (n < 5) : (n >= 5);
            if (give) {
#pragma unroll
                for (int m = 0; m < 2; ++m) *(LAS f32x4*)(xw + ((kh ? n : n - 5) * 2 + m) * 1024 + lane * 16) = acc[n][m];
            }
        }
        asm volatile("s_waitcnt lgkmcnt(0)" ::: "memory");
        BAR();
#pragma unroll
        for (int i = 0; i < 10; ++i) {
            const f32x4 mine = kh ? acc[(5 + (i >> 1)) < 9 ? 5 + (i >> 1) : 8][i & 1] : acc[i >> 1][i & 1];
            val[i] = mine + *(LAS f32x4*)(xr + i * 1024 + lane * 16);
        }
    }
    {
        const int nit = kh ? 8 : 10;
        const int grp = mt * 4 + nt;
        const __amdgpu_buffer_rsrc_t rp = __builtin_amdgcn_make_buffer_rsrc((void*)part, 0, NSPLIT * 64 * 8 * 10 * 512, 0x00020000);
        const unsigned slot = (unsigned)((grp * 8 + w) * 10 * 512 + lane * 8);
        constexpr unsigned SPL = 64 * 8 * 10 * 512;
        u32x2 xv[3]; unsigned xoff[3];
#pragma unroll
        for (int q = 0; q < 3; ++q) {
            int i = split + 4 * q; i = i < nit ? i : split;
            const int n = kh ? 5 + (i >> 1) : (i >> 1), m = i & 1;
            const int row = 128 * mt + 32 * wm + 16 * m + fr;
            const int col = (n < 8 ? 128 * nt + 16 * n : 512 + 16 * nt) + 4 * g;
            xoff[q] = (unsigned)(row * DM + col);
            xv[q] = *(const u32x2*)(x + xoff[q]);
        }
#pragma unroll
        for (int i = 0; i < 10; ++i) {
            if (i < nit && (i & 3) != split) {
                u32x2 v; v[0] = pk_bf16(val[i][0], val[i][1]); v[1] = pk_bf16(val[i][2], val[i][3]);
                __builtin_amdgcn_raw_buffer_store_b64(v, rp, slot + split * SPL + i * 512, 0, 16);
            }
        }
        STAMP(2, 3);
        asm volatile("s_waitcnt vmcnt(0)" ::: "memory");
        BAR();
        STAMP(2, 4);
        unsigned* c = cnt + grp * 64;
        if (tid == 0) {
            __hip_atomic_fetch_add(c, 1u, __ATOMIC_RELAXED, __HIP_MEMORY_SCOPE_AGENT);
            unsigned spins = 0;
            while (__hip_atomic_load(c, __ATOMIC_RELAXED, __HIP_MEMORY_SCOPE_AGENT) < (unsigned)NSPLIT) {
                __builtin_amdgcn_s_sleep(1);
                if (++spins > (1u << 22)) break;
            }
        }
        BAR();
        STAMP(2, 5);
        u32x2 pv[3][3];
#pragma unroll
        for (int q = 0; q < 3; ++q) {
            const int i = split + 4 * q;
#pragma unroll
            for (int s2 = 1; s2 < NSPLIT; ++s2) {
                const int so = (split + s2) & 3;
                const unsigned off = i < nit ? slot + so * SPL + i * 512 : 0xfffffff0u;
                pv[q][s2 - 1] = __builtin_amdgcn_raw_buffer_load_b64(rp, off, 0, 16);
            }
        }
#pragma unroll
        for (int q = 0; q < 3; ++q) {
            const int i = split + 4 * q;
            if (i < nit) {
                f32x4 o = val[0];
#pragma unroll
                for (int ii = 0; ii < 10; ++ii) if (ii == i) o = val[ii];
#pragma unroll
                for (int s2 = 0; s2 < 3; ++s2) { o[0] += bf_lo(pv[q][s2][0]); o[1] += bf_hi(pv[q][s2][0]); o[2] += bf_lo(pv[q][s2][1]); o[3] += bf_hi(pv[q][s2][1]); }
                u32x2 v; v[0] = pk_bf16(o[0] + bf_lo(xv[q][0]), o[1] + bf_hi(xv[q][0])); v[1] = pk_bf16(o[2] + bf_lo(xv[q][1]), o[3] + bf_hi(xv[q][1]));
                *(u32x2*)(out + xoff[q]) = v;
            }
        }
    }
    STAMP(2, 15);
}

extern "C" void kernel_launch(void* const* d_in, const int* in_sizes, int n_in, void* d_out, int out_size, void* d_ws,
                              size_t ws_size, hipStream_t stream) {
    const u16* x = (const u16*)d_in[0];
    const u16* wqkv = (const u16*)d_in[1];
    const u16* wo = (const u16*)d_in[2];
    const u16* Wg = (const u16*)d_in[4];
    const u16* Wu = (const u16*)d_in[5];
    const u16* Wd = (const u16*)d_in[6];
    char* ws = (char*)d_ws;
    u16* xnb = (u16*)ws;
    u16* hb = (u16*)(ws + WS_HB);
    u16* part = (u16*)(ws + WS_PART);
    u16* wqh = (u16*)(ws + WS_WQH);
    u16* gx = (u16*)(ws + WS_GX);
    u16* dx = (u16*)(ws + WS_DX);
    unsigned* cnt = (unsigned*)(ws + WS_CNT);
    (void)hipFuncSetAttribute((const void*)k1_gateup, hipFuncAttributeMaxDynamicSharedMemorySize, K1_LDS);
    (void)hipFuncSetAttribute((const void*)k2_down, hipFuncAttributeMaxDynamicSharedMemorySize, K2_LDS);
    k0_ln<<<256, 256, 0, stream>>>(x, xnb, wqkv, wqh, Wg, Wu, gx, Wd, wo, dx, (const char*)ws, (const char*)d_out);
    k1_gateup<<<256, 512, K1_LDS, stream>>>(xnb, Wg, Wu, wqh, gx, Wd, wo, hb, cnt);
    k2_down<<<256, 512, K2_LDS, stream>>>(hb, Wd, wo, dx, part, cnt, x, (u16*)d_out);
}
```

```cpp
#include <hip/hip_runtime.h>
#include <hip/hip_bf16.h>
#include <stdint.h>
#include <stdio.h>
#include <type_traits>

typedef unsigned short u16;
typedef short bf16x8 __attribute__((ext_vector_type(8)));
typedef short s16x4 __attribute__((ext_vector_type(4)));
typedef float f32x4 __attribute__((ext_vector_type(4)));
typedef float f32x2 __attribute__((ext_vector_type(2)));
typedef __bf16 bf16x2_t __attribute__((ext_vector_type(2)));
typedef unsigned u32x4 __attribute__((ext_vector_type(4)));
typedef unsigned u32x2 __attribute__((ext_vector_type(2)));
#define LAS __attribute__((address_space(3)))
#define GAS __attribute__((address_space(1)))

constexpr int SEQ = 2048, DM = 576, FF = 4608;
constexpr int NKB_X = 18;
constexpr int NKB_H = 162;
constexpr size_t BLK_ELEMS = (size_t)SEQ * 32;

constexpr size_t WS_HB = 18 * 131072, WS_PART = WS_HB + 162 * 131072, WS_WQH = WS_PART + 4 * 64 * 8 * 10 * 512, WS_GX = WS_WQH + 32 * 576 * 64 * 2,
                 WS_DX = WS_GX + 2 * 32 * 576 * 16 * 2, WS_CNT = WS_DX + 4 * 5184 * 16 * 2, WS_END = WS_CNT + 64 * 256;

__device__ __forceinline__ unsigned pk_bf16(float lo, float hi) {
    f32x2 v = {lo, hi};
    bf16x2_t b = __builtin_convertvector(v, bf16x2_t);
    return __builtin_bit_cast(unsigned, b);
}
__device__ __forceinline__ float bf_lo(unsigned u) { return __uint_as_float(u << 16); }
__device__ __forceinline__ float bf_hi(unsigned u) { return __uint_as_float(u & 0xffff0000u); }
__device__ __forceinline__ float rbf(float x) { return bf_lo(pk_bf16(x, x)); }

__device__ __forceinline__ const void* uni(const void* p) {
    const unsigned long long v = (unsigned long long)p;
    const unsigned lo = __builtin_amdgcn_readfirstlane((unsigned)v), hi = __builtin_amdgcn_readfirstlane((unsigned)(v >> 32));
    return (const void*)(((unsigned long long)hi << 32) | lo);
}
__device__ __forceinline__ void glds16(const void* sbase, unsigned voff, unsigned lds) {
    sbase = uni(sbase); lds = __builtin_amdgcn_readfirstlane(lds);
    asm volatile("s_mov_b32 m0, %2\n\ts_nop 3\n\tglobal_load_lds_dwordx4 %0, %1"
                 :: "v"(voff), "s"(sbase), "s"(lds) : "memory", "m0");
}
__device__ __forceinline__ void glds16_o1k(const void* sbase, unsigned voff, unsigned lds) {
    sbase = uni(sbase); lds = __builtin_amdgcn_readfirstlane(lds);
    asm volatile("s_mov_b32 m0, %2\n\ts_nop 3\n\tglobal_load_lds_dwordx4 %0, %1 offset:1024"
                 :: "v"(voff), "s"(sbase), "s"(lds) : "memory", "m0");
}
__device__ __forceinline__ void glds16_q(const void* sbase, unsigned voff, unsigned lds) {
    sbase = uni(sbase); lds = __builtin_amdgcn_readfirstlane(lds);
    unsigned sv; unsigned long long ex;
    asm volatile("s_mov_b32 %0, m0\n\ts_mov_b64 %1, exec\n\ts_mov_b32 m0, %4\n\ts_mov_b64 exec, 0xffff\n\ts_nop 1\n\tglobal_load_lds_dwordx4 %2, %3\n\ts_mov_b64 exec, %1\n\ts_mov_b32 m0, %0"
                 : "=&s"(sv), "=&s"(ex) : "v"(voff), "s"(sbase), "s"(lds) : "memory");
}
__device__ __forceinline__ void glds16_m(const void* sbase, unsigned voff, unsigned lds) {
    sbase = uni(sbase); lds = __builtin_amdgcn_readfirstlane(lds);
    unsigned sv; unsigned long long ex;
    asm volatile("s_mov_b32 %0, m0\n\ts_mov_b64 %1, exec\n\ts_mov_b32 m0, %4\n\ts_mov_b32 exec_lo, -1\n\ts_mov_b32 exec_hi, 0xff\n\ts_nop 1\n\tglobal_load_lds_dwordx4 %2, %3\n\ts_mov_b64 exec, %1\n\ts_mov_b32 m0, %0"
                 : "=&s"(sv), "=&s"(ex) : "v"(voff), "s"(sbase), "s"(lds) : "memory");
}
template <int VX, int VOFF, int LOFF>
__device__ __forceinline__ void glds16_so(const void* sbase, unsigned voff, unsigned lds) {
    sbase = uni(sbase); lds = __builtin_amdgcn_readfirstlane(lds);
    unsigned t;
    asm volatile("v_xad_u32 %0, %1, %4, %5\n\ts_add_u32 m0, %3, %6\n\ts_nop 3\n\tglobal_load_lds_dwordx4 %0, %2"
                 : "=&v"(t) : "v"(voff), "s"(sbase), "s"(lds), "i"(VX), "s"(VOFF), "i"(LOFF) : "memory", "m0", "scc");
}
typedef __amdgpu_buffer_rsrc_t rsrc_t;
__device__ __forceinline__ rsrc_t mk_rsrc(const void* p, int bytes) { return __builtin_amdgcn_make_buffer_rsrc((void*)uni(p), 0, bytes, 0x00020000); }
__device__ __forceinline__ void blds16(rsrc_t r, unsigned voff, unsigned soff, unsigned lds) {
    soff = __builtin_amdgcn_readfirstlane(soff); lds = __builtin_amdgcn_readfirstlane(lds);
    asm volatile("s_mov_b32 m0, %3\n\ts_nop 3\n\tbuffer_load_dwordx4 %0, %1, %2 offen lds" :: "v"(voff), "s"(r), "s"(soff), "s"(lds) : "memory", "m0");
}
__device__ __forceinline__ void blds4(rsrc_t r, unsigned voff, unsigned soff, unsigned lds) {
    soff = __builtin_amdgcn_readfirstlane(soff); lds = __builtin_amdgcn_readfirstlane(lds);
    asm volatile("s_mov_b32 m0, %3\n\ts_nop 3\n\tbuffer_load_dword %0, %1, %2 offen lds" :: "v"(voff), "s"(r), "s"(soff), "s"(lds) : "memory", "m0");
}
__device__ __forceinline__ void glds4(const void* sbase, unsigned voff, unsigned lds) {
    sbase = uni(sbase); lds = __builtin_amdgcn_readfirstlane(lds);
    asm volatile("s_mov_b32 m0, %2\n\ts_nop 3\n\tglobal_load_lds_dword %0, %1" :: "v"(voff), "s"(sbase), "s"(lds) : "memory", "m0");
}
__device__ __forceinline__ unsigned vxor(unsigned v, int imm) { unsigned r; asm volatile("v_xor_b32 %0, %1, %2" : "=v"(r) : "i"(imm), "v"(v)); return r; }
__device__ __forceinline__ float dot2(unsigned a, unsigned b, float c) {
    return __builtin_amdgcn_fdot2_f32_bf16(__builtin_bit_cast(bf16x2_t, a), __builtin_bit_cast(bf16x2_t, b), c, false);
}
__device__ __forceinline__ void dot2_ip(float& c, unsigned a, unsigned b) { asm volatile("v_dot2_f32_bf16 %0, %1, %2, %0" : "+v"(c) : "v"(a), "v"(b)); }
__device__ __forceinline__ void dot2_ips(float& c, unsigned a, unsigned b) { asm volatile("v_dot2_f32_bf16 %0, %1, %2, %0" : "+v"(c) : "v"(a), "s"(b)); }
#define SCHED() __builtin_amdgcn_sched_barrier(0)
#define STAMP(kid, i) do {} while (0)
#define WSTAMP(i) do {} while (0)
__device__ __forceinline__ s16x4 tr_read(LAS char* p) {
    return __builtin_bit_cast(s16x4, __builtin_amdgcn_ds_read_tr16_b64_v4i16((LAS s16x4*)p));
}
__device__ __forceinline__ bf16x8 tr_frag(LAS char* p, int off_t1) {
    s16x4 lo = tr_read(p), hi = tr_read(p + off_t1);
    return __builtin_shufflevector(lo, hi, 0, 1, 2, 3, 4, 5, 6, 7);
}
__device__ __forceinline__ bf16x8 row_frag(LAS char* p) { return *(LAS bf16x8*)p; }
#define MFMA16(a, b, c) __builtin_amdgcn_mfma_f32_16x16x32_bf16((a), (b), (c), 0, 0, 0)
__device__ __forceinline__ void mfma_ip(f32x4& c, bf16x8 a, bf16x8 b) { asm volatile("v_mfma_f32_16x16x32_bf16 %0, %1, %2, %0" : "+v"(c) : "v"(a), "v"(b)); }
#define WAIT_V(n) asm volatile("s_waitcnt vmcnt(" #n ")" ::: "memory")
#define BAR() do { asm volatile("" ::: "memory"); __builtin_amdgcn_s_barrier(); asm volatile("" ::: "memory"); } while (0)

__device__ __forceinline__ int blk_off(int row, int chunk) { return row * 64 + ((chunk ^ (((row >> 3) & 1) << 1)) << 4); }

constexpr int RA = 0, RB = 65536, RE = 131072, RQ = 139264, RH = 155648;
constexpr int T_DUMMY = 159744;
constexpr int T_DX = 160768;
constexpr int K1_LDS = 163840;
constexpr int ST_ROW = 65536, ST_COL = ST_ROW + 272 * 8;
constexpr int QS = 144;
#define HST_AUX 16
#define PF 3
constexpr int NB = PF + 1;
template <int N> __device__ __forceinline__ void wait_vm() { asm volatile("s_waitcnt vmcnt(%0)" :: "i"(N) : "memory"); }
#define GLDS_MASKED(NAME, LO, HI) \
__device__ __forceinline__ void NAME(const void* sbase, unsigned voff, unsigned lds) { \
    sbase = uni(sbase); lds = __builtin_amdgcn_readfirstlane(lds); \
    unsigned sv; unsigned long long ex; \
    asm volatile("s_mov_b32 %0, m0\n\ts_mov_b64 %1, exec\n\ts_mov_b32 m0, %4\n\ts_mov_b32 exec_lo, " #LO "\n\ts_mov_b32 exec_hi, " #HI "\n\tglobal_load_lds_dwordx4 %2, %3\n\ts_mov_b64 exec, %1\n\ts_mov_b32 m0, %0" \
                 : "=&s"(sv), "=&s"(ex) : "v"(voff), "s"(sbase), "s"(lds) : "memory"); }
GLDS_MASKED(glds16_32, -1, 0)
GLDS_MASKED(glds16_8, 0xff, 0)

__device__ __forceinline__ void k1_epilogue(f32x4 (&acc)[11][4], f32x4& ach, float (&rs1)[2], float (&rs2)[2], float hs1, float hs2, float (&cs)[3],
                                            LAS char* lds, int w, int b, int j, int mt, u16* __restrict__ hb, u16* __restrict__ dx) {
    int tid = threadIdx.x; asm volatile("" : "+v"(tid));
    const int lane = tid & 63, fr = lane & 15, g = lane >> 4;
    const int wm = w >> 1, wn = w & 1;
    const __amdgpu_buffer_rsrc_t rh = __builtin_amdgcn_make_buffer_rsrc((void*)hb, 0, (int)(NKB_H * BLK_ELEMS * 2), 0x00020000);
    BAR();
    STAMP(1, 2);
    LAS f32x2* srow = (LAS f32x2*)(lds + ST_ROW);
    LAS float* scol = (LAS float*)(lds + ST_COL);
    {
#pragma unroll
        for (int mm = 0; mm < 2; ++mm) {
            float a = rs1[mm], c = rs2[mm];
            a += __shfl_xor(a, 16); a += __shfl_xor(a, 32); c += __shfl_xor(c, 16); c += __shfl_xor(c, 32);
            if (g == 0) srow[16 + 64 * wm + 16 * (2 * wn + mm) + fr] = (f32x2){a, c};
        }
        float a = hs1, c = hs2;
        a += __shfl_xor(a, 16); a += __shfl_xor(a, 32); c += __shfl_xor(c, 16); c += __shfl_xor(c, 32);
        if (w == 0 && g == 0) srow[fr] = (f32x2){a, c};
#pragma unroll
        for (int q = 0; q < 3; ++q) {
            float v = cs[q];
            v += __shfl_xor(v, 16); v += __shfl_xor(v, 32);
            if (g == 0 && (q < 2 || wm == 0)) scol[(wn * 11 + (q < 2 ? 2 * wm + q : 8)) * 16 + fr] = v;
        }
        if (wm < 2 && fr == 0) *(LAS f32x4*)(scol + (wn * 11 + 9 + wm) * 16 + 4 * g) = ach;
    }
    asm volatile("s_waitcnt lgkmcnt(0)" ::: "memory");
    BAR();
    auto rowstat = [&](int lrow, float& mu, float& rs) {
        const f32x2 s = srow[lrow];
        const float mean = s[0] * (1.0f / 576.0f);
        const float var = fmaxf(s[1] * (1.0f / 576.0f) - mean * mean, 0.f);
        mu = rbf(mean);
        rs = 1.0f / rbf(sqrtf(rbf(rbf(var) + 1e-5f)));
    };
    float mu[4], rs[4];
#pragma unroll
    for (int m = 0; m < 4; ++m) rowstat(16 + 64 * wm + 16 * (m ^ (2 * wn)) + fr, mu[m], rs[m]);
    auto csum = [&](int grp) -> f32x4 { return *(LAS const f32x4*)(scol + (wn * 11 + grp) * 16 + 4 * g); };
    auto hs = [&](int m, int part) {
        const int row = 256 * mt + 64 * wm + 16 * (m ^ (2 * wn)) + fr;
        const float r2 = rs[m] * rs[m];
        SCHED();
        if (part < 2) {
            const int pp = part;
            unsigned v[2][2];
#pragma unroll
            for (int hh = 0; hh < 2; ++hh) {
                const int gt = 2 * pp + hh;
                const f32x4 cg = csum(2 * (gt ^ wm)), cu = csum(2 * (gt ^ wm) + 1);
                float hv[4];
#pragma unroll
                for (int e = 0; e < 4; ++e) hv[e] = fmaxf(acc[2 * gt][m][e] - mu[m] * cg[e], 0.f) * r2 * (acc[2 * gt + 1][m][e] - mu[m] * cu[e]);
                v[hh][0] = pk_bf16(hv[0], hv[1]); v[hh][1] = pk_bf16(hv[2], hv[3]);
            }
            auto r0 = __builtin_amdgcn_permlane16_swap(v[0][0], v[1][0], false, false);
            auto r1 = __builtin_amdgcn_permlane16_swap(v[0][1], v[1][1], false, false);
            u32x4 o; o[0] = r0[0]; o[1] = r1[0]; o[2] = r0[1]; o[3] = r1[1];
            const int blk = 4 * j + 2 * wn + (pp ^ (wm >> 1)), chunk = (((g & 1) ^ (wm & 1)) << 1) | (g >> 1);
            __builtin_amdgcn_raw_buffer_store_b128(o, rh, (unsigned)(blk * (int)(BLK_ELEMS * 2) + blk_off(row, chunk)), 0, HST_AUX);
        } else {
            const f32x4 cm = csum(8);
            float hv[4];
#pragma unroll
            for (int e = 0; e < 4; ++e) {
                const unsigned mine = __float_as_uint(acc[8][m][e] - mu[m] * cm[e]);
                auto r = __builtin_amdgcn_permlane32_swap(mine, mine, false, false);
                hv[e] = fmaxf(__uint_as_float(r[0]), 0.f) * r2 * __uint_as_float(r[1]);
            }
            u32x2 v; v[0] = pk_bf16(hv[0], hv[1]); v[1] = pk_bf16(hv[2], hv[3]);
            const int blk = 128 + (j >> 1), chunk = (j & 1) * 2 + wn;
            const unsigned off = g < 2 ? (unsigned)(blk * (int)(BLK_ELEMS * 2) + blk_off(row, chunk) + g * 8) : 0xfffffff0u;
            __builtin_amdgcn_raw_buffer_store_b64(v, rh, off, 0, HST_AUX);
        }
        SCHED();
    };
    hs(0, 0);
    LAS char* qt = lds;
    {
#pragma unroll
        for (int u = 0; u < 2; ++u) {
            const int cb = 2 * wn + (u ^ (wm & 1));
            const f32x4 cq = csum(9 + (u ^ (wm & 1)));
            const int nn = 16 * cb + 4 * g;
            const int h0 = nn / 18, h1 = (nn + 2) / 18;
            const int o0 = h0 * 48 + (nn - h0 * 18) * 2, o1 = h1 * 48 + (nn + 2 - h1 * 18) * 2;
#pragma unroll
            for (int m = 0; m < 4; ++m) {
                LAS char* rp = qt + (16 + 64 * wm + 16 * (m ^ (2 * wn)) + fr) * QS;
                const f32x4 v = (acc[9 + u][m] - mu[m] * cq) * rs[m];
                if (h0 < 3) *(LAS unsigned*)(rp + o0) = pk_bf16(v[0], v[1]);
                if (h1 < 3) *(LAS unsigned*)(rp + o1) = pk_bf16(v[2], v[3]);
            }
            if (u == 0 && (w == 0 || w == 2 || w == 1)) {
                float hmu, hrs; rowstat(fr, hmu, hrs);
                LAS char* rp = qt + fr * QS;
                const f32x4 v = (ach - hmu * cq) * hrs;
                if (h0 < 3) *(LAS unsigned*)(rp + o0) = pk_bf16(v[0], v[1]);
                if (h1 < 3) *(LAS unsigned*)(rp + o1) = pk_bf16(v[2], v[3]);
            }
            if (u == 0) hs(0, 1);
        }
    }
    if (tid < 162) {
        const int gid = b * 162 + tid, k = gid >> 3, ch = gid & 7;
        *(u32x4*)(dx + ((size_t)(ch >> 1) * 5184 + k) * 16 + 8 * (ch & 1)) = *(LAS const u32x4*)(lds + T_DX + tid * 16);
    }
    asm volatile("s_waitcnt lgkmcnt(0)" ::: "memory");
    BAR();
    STAMP(1, 3);
    {
        const int s = tid >> 1, half = tid & 1;
        const int lrow = 16 + s, grow = 256 * mt + s;
        auto ld18 = [&](LAS const char* p, unsigned (&v)[9]) {
            const u32x4 a = *(LAS const u32x4*)p, c = *(LAS const u32x4*)(p + 16);
            v[0] = a[0]; v[1] = a[1]; v[2] = a[2]; v[3] = a[3]; v[4] = c[0]; v[5] = c[1]; v[6] = c[2]; v[7] = c[3];
            v[8] = *(LAS const unsigned*)(p + 32);
        };
        hs(0, 2);
        unsigned qv[9];
        ld18(qt + lrow * QS + 96, qv);
        float lg[4];
#pragma unroll
        for (int ww = 0; ww < 4; ++ww) {
            const int wi = 4 * half + ww;
            unsigned kv[9];
            ld18(qt + (lrow - 2 * wi) * QS, kv);
            float a = 0.f;
#pragma unroll
            for (int i = 0; i < 9; ++i) a = dot2(qv[i], kv[i], a);
            a = rbf(rbf(a) * 0.23570226039551584f);
            lg[ww] = (grow - 2 * wi >= 0) ? a : -1e9f;
            if (ww == 0) hs(1, 0);
            if (ww == 2) hs(1, 1);
        }
        float mx = fmaxf(fmaxf(lg[0], lg[1]), fmaxf(lg[2], lg[3]));
        mx = fmaxf(mx, __shfl_xor(mx, 1));
        float ex[4], sum = 0.f;
#pragma unroll
        for (int ww = 0; ww < 4; ++ww) { ex[ww] = __expf(lg[ww] - mx); sum += ex[ww]; }
        sum += __shfl_xor(sum, 1);
        const float inv = 1.0f / sum;
        hs(1, 2);
        float o[18];
#pragma unroll
        for (int i = 0; i < 18; ++i) o[i] = 0.f;
#pragma unroll
        for (int ww = 0; ww < 4; ++ww) {
            const int wi = 4 * half + ww;
            const float pw = (grow - 2 * wi >= 0) ? rbf(ex[ww] * inv) : 0.f;
            unsigned vv[9];
            ld18(qt + (lrow - 2 * wi) * QS + 48, vv);
#pragma unroll
            for (int i = 0; i < 9; ++i) { o[2 * i] += pw * bf_lo(vv[i]); o[2 * i + 1] += pw * bf_hi(vv[i]); }
            if (ww < 3) hs(2, ww);
        }
        hs(3, 0);
#pragma unroll
        for (int ii = 0; ii < 5; ++ii) {
            const float s0 = half ? o[2 * ii] : o[(10 + 2 * ii) % 18], s1 = half ? o[2 * ii + 1] : o[(11 + 2 * ii) % 18];
            const float r0 = __shfl_xor(s0, 1), r1 = __shfl_xor(s1, 1);
            const float m0 = half ? o[(10 + 2 * ii) % 18] : o[2 * ii], m1 = half ? o[(11 + 2 * ii) % 18] : o[2 * ii + 1];
            const int i = half * 5 + ii;
            if (i < 9) {
                const int col = FF + 18 * j + 2 * i;
                *(unsigned*)((char*)hb + (size_t)(col >> 5) * (BLK_ELEMS * 2) + blk_off(grow, (col >> 3) & 3) + (col & 7) * 2) = pk_bf16(m0 + r0, m1 + r1);
            }
            if (ii == 2) hs(3, 1);
        }
        hs(3, 2);
    }
    STAMP(1, 15);
}

__global__ __launch_bounds__(512, 2) void k1_gateup(const u16* __restrict__ x, const u16* __restrict__ Wg,
                                                    const u16* __restrict__ Wu, const u16* __restrict__ wqkv,
                                                    const u16* __restrict__ Wd, const u16* __restrict__ wo, u16* __restrict__ hb,
                                                    unsigned* __restrict__ cnt, u16* __restrict__ dx) {
    extern __shared__ __attribute__((aligned(16))) char smem[];
    LAS char* lds = (LAS char*)smem;
    const int tid = threadIdx.x, lane = tid & 63;
    if (blockIdx.x < 64 && tid == 0) cnt[blockIdx.x * 64] = 0u;
    const int w = __builtin_amdgcn_readfirstlane(tid >> 6);
    const int b = blockIdx.x, xcd = b & 7, idx = b >> 3;
    const int j = 4 * xcd + (idx & 3), mt = idx >> 2;
    const int wm = w >> 1, wn = w & 1;
    const unsigned lds0 = (unsigned)(size_t)lds;
    const int fr = lane & 15, g = lane >> 4, q4 = (lane >> 2) & 3, p4 = lane & 3;

    f32x4 acc[11][4], ach;
    float rs1[2], rs2[2], hs1 = 0.f, hs2 = 0.f, cs[3];
    {
        const int koct = w >> 1, mat = w & 1;
        const u16* W = mat ? Wu : Wg;
        unsigned srcA, srcB, srcE, srcQ;
        {
            const int r = lane >> 3;
            srcA = (unsigned)(r * DM * 2 + (((lane & 7) ^ (r >> 1)) << 4));
            const int swz = ((r >> 1) & 1) | ((koct & 1) << 1), c = (lane & 7) ^ (swz << 1);
            srcB = (unsigned)((8 * koct + r) * FF + 128 * j + 8 * c) * 2u;
            const int ko = lane >> 4, re = ((lane >> 1) & 7) ^ ((ko & 1) << 2), ce = (lane & 1) ^ mat;
            srcE = (unsigned)((8 * ko + re) * FF + 4096 + 16 * j + 8 * ce) * 2u;
            {
                const int rq = 8 * koct + 2 * mat + (lane >> 5), pq = lane & 31;
                const int swq = ((rq >> 1) & 1) | (((rq >> 3) & 1) << 1);
                int d = (((pq >> 3) ^ swq) << 3) | (pq & 7); d = d < 27 ? d : 0;
                const int part = d >= 18 ? 2 : (d >= 9 ? 1 : 0), d2 = d - 9 * part;
                srcQ = (unsigned)(rq * (3 * DM) + (part == 2 ? 0 : part + 1) * DM + 18 * j + 2 * d2) * 2u;
            }
        }
        const unsigned dA = lds0 + RA + (32 * w) * 128, dB = lds0 + RB + (2 * w) * 1024;
        const int hr = mt > 0 ? 256 * mt - 16 : 0;
        const rsrc_t rX = mk_rsrc(x, SEQ * DM * 2), rW = mk_rsrc(W, DM * FF * 2), rQ = mk_rsrc(wqkv, DM * 3 * DM * 2);
        const rsrc_t rD = mk_rsrc(Wd, FF * DM * 2), rO = mk_rsrc(wo, DM * DM * 2);
        const unsigned oA = (unsigned)((256 * mt + 32 * w) * DM * 2), oH = (unsigned)((hr + 8 * ((w - 2) & 1)) * DM * 2);
        auto issueA = [&](int P, int i) {
            const unsigned d = dA + (P & 1) * 32768;
            if (i < 4) blds16(rX, (i & 1) ? vxor(srcA, 64) : srcA, oA + 128 * P + i * (8 * DM * 2), d + i * 1024);
            if (i == 4) {
                if (w == 2) blds16(rX, srcA, oH + 128 * P, lds0 + RH + (P & 1) * 2048);
                if (w == 3) blds16(rX, vxor(srcA, 64), oH + 128 * P, lds0 + RH + (P & 1) * 2048 + 1024);
            }
        };
        auto issueB = [&](int t, int sl, int i) {
            const unsigned ow = (unsigned)t * (32 * FF * 2), oq = (unsigned)t * (32 * 3 * DM * 2);
            if (i == 0) blds16(rW, srcB, ow, dB + sl * 16384);
            if (i == 1) blds16(rW, srcB, ow + 128, dB + sl * 16384 + 1024);
            if (i == 2) blds4(rQ, srcQ, oq, lds0 + RQ + sl * 4096 + koct * 1024 + mat * 256);
            if (i == 3) blds4(rQ, srcQ, oq + 4 * (3 * DM * 2), lds0 + RQ + sl * 4096 + koct * 1024 + mat * 256 + 512);
            if (i == 4 && w < 2) blds16(rW, srcE, ow, lds0 + RE + sl * 2048 + w * 1024);
        };
        auto prefetch = [&](int p) {
            const int split = xcd >> 1;
            const int r0 = split == 0 ? 0 : 1344 + 1280 * (split - 1), nro = split == 0 ? 168 : 160;
            p = p < 6 ? p : 5;
            int id = (idx * 8 + w) * 6 + p; id = id < nro * 9 ? id : 0;
            const int ro = id / 9, cb = id - ro * 9, row = r0 + 8 * ro;
            if (row < FF) blds16(rD, srcA, (unsigned)(row * DM * 2 + 128 * cb), lds0 + T_DUMMY);
            else blds16(rO, srcA, (unsigned)((row - FF) * DM * 2 + 128 * cb), lds0 + T_DUMMY);
        };
        auto waitE = [&](auto N_) { constexpr int N = decltype(N_)::value; if (w < 2) wait_vm<N + 1>(); else wait_vm<N>(); };
        STAMP(1, 0);
        {
            int l = w * 64 + lane; l = l < 162 ? l : 0;
            const int gid = b * 162 + l, k = gid >> 3, ch = gid & 7;
            const u16* src = (k < FF ? Wd + (size_t)k * DM : wo + (size_t)(k - FF) * DM) + 512 + 8 * ch;
            const unsigned d = __builtin_amdgcn_readfirstlane(w < 3 ? lds0 + T_DX + w * 1024 : lds0 + T_DUMMY);
            asm volatile("s_mov_b32 m0, %1\n\ts_nop 3\n\tglobal_load_lds_dwordx4 %0, off" :: "v"(src), "s"(d) : "memory", "m0");
        }
#pragma unroll
        for (int i = 0; i < 5; ++i) issueA(0, i);
#pragma unroll
        for (int t = 0; t < 3; ++t) {
#pragma unroll
            for (int i = 0; i < 5; ++i) issueB(t, t, i);
            if (t == 1) prefetch(0);
        }

        const unsigned pa0 = lds0 + RA + (64 * wm + fr) * 128 + ((g ^ (fr >> 1)) << 4) + wn * 4096;
        const int hdelta = RH - RA - 64 * wm * 128 - wn * 4096;
        const int swz = ((q4 >> 1) & 1) | ((g & 1) << 1);
        const unsigned pb0 = lds0 + RB + (g * 4 + wn) * 1024 + q4 * 128 + (swz << 5) + p4 * 8;
        const int emat = p4 >> 1;
        const unsigned pe0 = lds0 + RE + (emat * 4 + g) * 256 + ((q4 ^ ((g & 1) << 2)) * 32) + ((wn ^ emat) * 16) + (p4 & 1) * 8;
        const unsigned pq0 = lds0 + RQ + g * 1024 + q4 * 128 + (((2 * wn + (wm & 1)) ^ swz) << 5) + p4 * 8;
        LAS char* pbv[4];
#pragma unroll
        for (int u = 0; u < 4; ++u) pbv[u] = (LAS char*)(size_t)(pb0 ^ ((u ^ wm) << 5));
        auto rdB = [&](int sl, int nf, int t1) -> s16x4 {
            if (nf < 8) return tr_read(pbv[nf >> 1] + sl * 16384 + (nf & 1) * 2048 + t1 * 512);
            if (nf == 8) return tr_read((LAS char*)(size_t)(t1 ? vxor(pe0, 128) : pe0) + sl * 2048);
            return tr_read((LAS char*)(size_t)(nf == 10 ? vxor(pq0, 32) : pq0) + sl * 4096 + t1 * 512);
        };
        auto rdA = [&](int S, int m) -> bf16x8 {
            const int xo = ((S & 1) ? 64 : 0) | (m < 2 ? 0 : 4096);
            return row_frag((LAS char*)(size_t)(xo ? vxor(pa0, xo) : pa0) + ((S >> 1) & 1) * 32768 + (m & 1) * 2048);
        };
        auto rdH = [&](int S) -> bf16x8 {
            unsigned r; asm volatile("v_add_u32 %0, %1, %2" : "=v"(r) : "s"(hdelta), "v"((S & 1) ? vxor(pa0, 64) : pa0));
            return row_frag((LAS char*)(size_t)r + ((S >> 1) & 1) * 2048);
        };
#pragma unroll
        for (int n = 0; n < 11; ++n)
#pragma unroll
            for (int m = 0; m < 4; ++m) { acc[n][m] = (f32x4){0.f, 0.f, 0.f, 0.f}; asm volatile("" : "+v"(acc[n][m])); }
        ach = (f32x4){0.f, 0.f, 0.f, 0.f}; asm volatile("" : "+v"(ach));
        rs1[0] = rs1[1] = rs2[0] = rs2[1] = 0.f; cs[0] = cs[1] = cs[2] = 0.f;
        constexpr unsigned ONES = 0x3f803f80u;

        if (w < 2) wait_vm<11>(); else wait_vm<9>();
        BAR();
        STAMP(1, 1);
        bf16x8 fa[4], fh, fb[NB];
#pragma unroll
        for (int m = 0; m < 4; ++m) fa[m] = rdA(0, m);
#pragma unroll
        for (int i = 0; i < PF; ++i) { s16x4 lo = rdB(0, i, 0), hi = rdB(0, i, 1); fb[i] = __builtin_shufflevector(lo, hi, 0, 1, 2, 3, 4, 5, 6, 7); }
#define MM(C, A, B) mfma_ip(C, A, B)
        auto step = [&](auto S_, int t, bool do_issue, auto LAST1_, auto WAIT0_) {
            constexpr int S = decltype(S_)::value;
            constexpr bool has_next = !decltype(LAST1_)::value;
            constexpr bool wait0 = decltype(WAIT0_)::value;
            constexpr int sl = S, sn = (S + 1) & 3;
            constexpr bool even = (S & 1) == 0;
#pragma unroll
            for (int nf = 0; nf < 11; ++nf) {
                const int r = 11 * S + nf;
                const int psl = (nf + PF < 11) ? sl : sn, nn = (nf + PF < 11) ? nf + PF : nf + PF - 11;
                const bool pf = (nf + PF < 11) || has_next;
                const bool lastg = (nf == 10);
                s16x4 lo, hi;
                MM(acc[nf][0], fb[r % NB], fa[0]);
                SCHED();
                if (nf == 0) {
                    if (wait0) wait_vm<0>(); else waitE(std::integral_constant<int, 5>{});
                    BAR();
                }
                if (pf) lo = rdB(psl, nn, 0);
                if (lastg && has_next) fa[0] = rdA(sn, 0);
                SCHED();
                MM(acc[nf][1], fb[r % NB], fa[1]);
                SCHED();
                if (pf) hi = rdB(psl, nn, 1);
                if (lastg && has_next) fa[1] = rdA(sn, 1);
                SCHED();
                MM(acc[nf][2], fb[r % NB], fa[2]);
                SCHED();
                if (lastg && has_next) fa[2] = rdA(sn, 2);
                if (nf == 7) fh = rdH(S);
                if (do_issue) {
                    if (even) { if (nf < 5) issueA((t >> 1) + 1, nf); else if (nf < 10) issueB(t + 3, (S + 3) & 3, nf - 5); else prefetch(1 + (t >> 1)); }
                    else if (nf < 5) issueB(t + 3, (S + 3) & 3, nf);
                }
                if (nf < 2 || nf == 8) {
                    const u32x4 f = __builtin_bit_cast(u32x4, fb[r % NB]);
#pragma unroll
                    for (int e = 0; e < 4; ++e) dot2_ips(cs[nf < 2 ? nf : 2], f[e], ONES);
                }
                if (nf >= 2 && nf < 6) {
                    const int mm = (nf - 2) >> 1;
                    const u32x4 f = __builtin_bit_cast(u32x4, fa[mm]);
#pragma unroll
                    for (int e = 0; e < 4; ++e) { if (nf & 1) dot2_ips(rs1[mm], f[e], ONES); else dot2_ip(rs2[mm], f[e], f[e]); }
                }
                if (nf == 9) {
                    u32x4 f = __builtin_bit_cast(u32x4, fh);
#pragma unroll
                    for (int e = 0; e < 4; ++e) { dot2_ips(hs1, f[e], ONES); dot2_ip(hs2, f[e], f[e]); f[e] = fr == 0 ? ONES : f[e]; }
                    fh = __builtin_bit_cast(bf16x8, f);
                    asm volatile("s_nop 1" : "+v"(fh));
                }
                SCHED();
                MM(acc[nf][3], fb[r % NB], fa[3]);
                SCHED();
                if (lastg && has_next) fa[3] = rdA(sn, 3);
                if (nf == 9) { MM(ach, fb[r % NB], fh); SCHED(); }
                if (pf) fb[(r + PF) % NB] = __builtin_shufflevector(lo, hi, 0, 1, 2, 3, 4, 5, 6, 7);
            }
        };
        using I0 = std::integral_constant<int, 0>; using I1 = std::integral_constant<int, 1>;
        using I2 = std::integral_constant<int, 2>; using I3 = std::integral_constant<int, 3>;
        using F = std::false_type; using T = std::true_type;
#pragma unroll 1
        for (int t0 = 0; t0 < 16; t0 += 4) {
            step(I0{}, t0, true, F{}, F{});
            step(I1{}, t0 + 1, true, F{}, F{});
            step(I2{}, t0 + 2, true, F{}, F{});
            step(I3{}, t0 + 3, t0 + 3 < 15, F{}, F{});
        }
        step(I0{}, 16, false, F{}, T{});
        step(I1{}, 17, false, T{}, T{});
    }
    asm volatile("s_nop 7\n\ts_nop 7" ::: "memory");
    STAMP(1, 4);
    k1_epilogue(acc, ach, rs1, rs2, hs1, hs2, cs, lds, w, b, j, mt, hb, dx);
}

constexpr int K2_NT = 21;
constexpr int K2_RB = 0, K2_RE = 65536, K2_LDS = 81920;
constexpr int NSPLIT = 4;
__device__ __forceinline__ void gload2(bf16x8& d0, bf16x8& d1, const void* sbase, unsigned voff) {
    sbase = uni(sbase);
    asm volatile("s_nop 4\n\tglobal_load_dwordx4 %0, %2, %3\n\tglobal_load_dwordx4 %1, %2, %3 offset:1024"
                 : "=&v"(d0), "=&v"(d1) : "v"(voff), "s"(sbase) : "memory");
}
__global__ __launch_bounds__(512, 2) void k2_down(const u16* __restrict__ hb, const u16* __restrict__ Wd,
                                                  const u16* __restrict__ wo, const u16* __restrict__ dx, u16* __restrict__ part, unsigned* __restrict__ cnt,
                                                  const u16* __restrict__ x, u16* __restrict__ out) {
    extern __shared__ __attribute__((aligned(16))) char smem[];
    LAS char* lds = (LAS char*)smem;
    const int tid = threadIdx.x, lane = tid & 63;
    const int w = __builtin_amdgcn_readfirstlane(tid >> 6);
    const int b = blockIdx.x, xcd = b & 7, idx = b >> 3;
    const int split = xcd >> 1, mt = (xcd & 1) * 8 + (idx >> 2), nt = idx & 3;
    const int wm = w >> 1, kh = w & 1;
    const int T0 = split == 0 ? 0 : 21 + 20 * (split - 1);

    const unsigned srcA = (unsigned)(kh * (int)BLK_ELEMS * 2 + blk_off(128 * mt + 32 * wm + (lane & 15), lane >> 4));
    const int koct = w >> 1, sub = w & 1;
    unsigned offB, offE;
    {
        const int r = lane >> 3, swz = ((r >> 1) & 1) | ((koct & 1) << 1), c = (lane & 7) ^ (swz << 1);
        offB = (unsigned)((32 * sub + 8 * koct + r) * DM + 128 * nt + 8 * c) * 2u;
        const int re = ((lane >> 1) & 7) ^ ((koct & 1) << 2), ce = (lane & 1);
        offE = (unsigned)((nt * 5184 + 32 * sub + 8 * koct + re) * 16 + 8 * ce) * 2u;
    }
    const unsigned lds0 = (unsigned)(size_t)lds;
    const unsigned dB = lds0 + K2_RB + (2 * w) * 1024, dE = lds0 + K2_RE + (sub * 4 + koct) * 256;
    bf16x8 fa[4][2];
    auto issue1 = [&](int t, int sl, int i, bf16x8& a0, bf16x8& a1) {
        int T = T0 + t; if (t == K2_NT - 1 && split != 0) T -= 1;
        const u16* Wt = T < 72 ? Wd + (size_t)T * 64 * DM : wo + (size_t)(T - 72) * 64 * DM;
        const u16* ba = hb + (size_t)T * 2 * BLK_ELEMS;
        if (i == 0) glds16(Wt, offB, dB + sl * 16384);
        if (i == 1) glds16(Wt + 64, offB, dB + sl * 16384 + 1024);
        if (i == 2) glds16_q(dx + (size_t)T * 64 * 16, offE, dE + sl * 2048);
        if (i == 3) gload2(a0, a1, ba, srcA);
    };
    STAMP(2, 0);
#pragma unroll
    for (int t = 0; t < 3; ++t)
#pragma unroll
        for (int i = 0; i < 4; ++i) issue1(t, t, i, fa[t][0], fa[t][1]);

    const int fr = lane & 15, g = lane >> 4, q4 = (lane >> 2) & 3, p4 = lane & 3;
    const int swz = ((q4 >> 1) & 1) | ((g & 1) << 1);
    LAS char* pb[4];
#pragma unroll
    for (int u = 0; u < 4; ++u) pb[u] = lds + K2_RB + (g * 4 + kh * 2) * 1024 + q4 * 128 + ((u ^ swz) << 5) + p4 * 8;
    LAS char* pe0 = lds + K2_RE + (kh * 4 + g) * 256 + ((q4 ^ ((g & 1) << 2)) * 32) + p4 * 8;
    LAS char* pe1 = lds + K2_RE + (kh * 4 + g) * 256 + (((4 + q4) ^ ((g & 1) << 2)) * 32) + p4 * 8;
    auto rdB = [&](int sl, int nf, int t1) -> s16x4 {
        if (nf < 8) return tr_read(pb[nf & 3] + sl * 16384 + (nf >> 2) * 1024 + t1 * 512);
        return tr_read((t1 ? pe1 : pe0) + sl * 2048);
    };

    f32x4 acc[9][2];
#pragma unroll
    for (int n = 0; n < 9; ++n)
#pragma unroll
        for (int m = 0; m < 2; ++m) { acc[n][m] = (f32x4){0.f, 0.f, 0.f, 0.f}; asm volatile("" : "+v"(acc[n][m])); }

    wait_vm<10>(); BAR();
    STAMP(2, 1);
    constexpr int PF2 = 4;
    bf16x8 fb[9];
#pragma unroll
    for (int i = 0; i < PF2; ++i) { s16x4 lo = rdB(0, i, 0), hi = rdB(0, i, 1); fb[i] = __builtin_shufflevector(lo, hi, 0, 1, 2, 3, 4, 5, 6, 7); }
    auto step = [&](auto S_, int t, bool do_issue, bool wait0, auto LAST_) {
        constexpr int S = decltype(S_)::value;
        constexpr bool has_next = !decltype(LAST_)::value;
        constexpr int sl = S, sn = (S + 1) & 3;
        asm volatile("" : "+v"(fa[sl][0]), "+v"(fa[sl][1]));
        if (!has_next && split != 0) {
#pragma unroll
            for (int m = 0; m < 2; ++m) fa[sl][m] = (bf16x8){0, 0, 0, 0, 0, 0, 0, 0};
            asm volatile("s_nop 1" : "+v"(fa[sl][0]), "+v"(fa[sl][1]));
        }
#pragma unroll
        for (int nf = 0; nf < 9; ++nf) {
            const int psl = (nf + PF2 < 9) ? sl : sn, nn = (nf + PF2 < 9) ? nf + PF2 : nf + PF2 - 9;
            const bool pf = (nf + PF2 < 9) || has_next;
            s16x4 lo, hi;
            mfma_ip(acc[nf][0], fb[nf], fa[sl][0]);
            SCHED();
            if (nf == 0) {
                if (!wait0) wait_vm<5>(); else wait_vm<0>();
                BAR();
            }
            if (pf) lo = rdB(psl, nn, 0);
            SCHED();
            mfma_ip(acc[nf][1], fb[nf], fa[sl][1]);
            SCHED();
            if (pf) hi = rdB(psl, nn, 1);
            if (nf >= 1 && nf < 5 && do_issue) issue1(t + 3, (S + 3) & 3, nf - 1, fa[(S + 3) & 3][0], fa[(S + 3) & 3][1]);
            SCHED();
            if (pf) fb[nn] = __builtin_shufflevector(lo, hi, 0, 1, 2, 3, 4, 5, 6, 7);
        }
    };
    using I0 = std::integral_constant<int, 0>; using I1 = std::integral_constant<int, 1>;
    using I2 = std::integral_constant<int, 2>; using I3 = std::integral_constant<int, 3>;
#pragma unroll 1
    for (int t0 = 0; t0 < 20; t0 += 4) {
        step(I0{}, t0, true, false, std::false_type{});
        step(I1{}, t0 + 1, true, false, std::false_type{});
        step(I2{}, t0 + 2, t0 + 2 < 18, false, std::false_type{});
        step(I3{}, t0 + 3, t0 + 3 < 18, t0 + 3 == 19, std::false_type{});
    }
    step(I0{}, 20, false, true, std::true_type{});
    asm volatile("s_nop 7\n\ts_nop 7" ::: "memory");
    BAR();
    STAMP(2, 2);
    f32x4 val[10];
    {
        LAS char* xw = lds + w * 10240;
        LAS char* xr = lds + (w ^ 1) * 10240;
#pragma unroll
        for (int n = 0; n < 9; ++n) {
            const bool give = kh ? (n < 5) : (n >= 5);
            if (give) {
#pragma unroll
                for (int m = 0; m < 2; ++m) *(LAS f32x4*)(xw + ((kh ? n : n - 5) * 2 + m) * 1024 + lane * 16) = acc[n][m];
            }
        }
        asm volatile("s_waitcnt lgkmcnt(0)" ::: "memory");
        BAR();
#pragma unroll
        for (int i = 0; i < 10; ++i) {
            const f32x4 mine = kh ? acc[(5 + (i >> 1)) < 9 ? 5 + (i >> 1) : 8][i & 1] : acc[i >> 1][i & 1];
            val[i] = mine + *(LAS f32x4*)(xr + i * 1024 + lane * 16);
        }
    }
    {
        const int nit = kh ? 8 : 10;
        const int grp = mt * 4 + nt;
        const __amdgpu_buffer_rsrc_t rp = __builtin_amdgcn_make_buffer_rsrc((void*)part, 0, NSPLIT * 64 * 8 * 10 * 512, 0x00020000);
        const unsigned slot = (unsigned)((grp * 8 + w) * 10 * 512 + lane * 8);
        constexpr unsigned SPL = 64 * 8 * 10 * 512;
        u32x2 xv[3]; unsigned xoff[3];
#pragma unroll
        for (int q = 0; q < 3; ++q) {
            int i = split + 4 * q; i = i < nit ? i : split;
            const int n = kh ? 5 + (i >> 1) : (i >> 1), m = i & 1;
            const int row = 128 * mt + 32 * wm + 16 * m + fr;
            const int col = (n < 8 ? 128 * nt + 16 * n : 512 + 16 * nt) + 4 * g;
            xoff[q] = (unsigned)(row * DM + col);
            xv[q] = *(const u32x2*)(x + xoff[q]);
        }
#pragma unroll
        for (int i = 0; i < 10; ++i) {
            if (i < nit && (i & 3) != split) {
                u32x2 v; v[0] = pk_bf16(val[i][0], val[i][1]); v[1] = pk_bf16(val[i][2], val[i][3]);
                __builtin_amdgcn_raw_buffer_store_b64(v, rp, slot + split * SPL + i * 512, 0, 16);
            }
        }
        STAMP(2, 3);
        asm volatile("s_waitcnt vmcnt(0)" ::: "memory");
        BAR();
        STAMP(2, 4);
        unsigned* c = cnt + grp * 64;
        if (tid == 0) {
            __hip_atomic_fetch_add(c, 1u, __ATOMIC_RELAXED, __HIP_MEMORY_SCOPE_AGENT);
            unsigned spins = 0;
            while (__hip_atomic_load(c, __ATOMIC_RELAXED, __HIP_MEMORY_SCOPE_AGENT) < (unsigned)NSPLIT) {
                __builtin_amdgcn_s_sleep(1);
                if (++spins > (1u << 22)) break;
            }
        }
        BAR();
        STAMP(2, 5);
        u32x2 pv[3][3];
#pragma unroll
        for (int q = 0; q < 3; ++q) {
            const int i = split + 4 * q;
#pragma unroll
            for (int s2 = 1; s2 < NSPLIT; ++s2) {
                const int so = (split + s2) & 3;
                const unsigned off = i < nit ? slot + so * SPL + i * 512 : 0xfffffff0u;
                pv[q][s2 - 1] = __builtin_amdgcn_raw_buffer_load_b64(rp, off, 0, 16);
            }
        }
#pragma unroll
        for (int q = 0; q < 3; ++q) {
            const int i = split + 4 * q;
            if (i < nit) {
                f32x4 o = val[0];
#pragma unroll
                for (int ii = 0; ii < 10; ++ii) if (ii == i) o = val[ii];
#pragma unroll
                for (int s2 = 0; s2 < 3; ++s2) { o[0] += bf_lo(pv[q][s2][0]); o[1] += bf_hi(pv[q][s2][0]); o[2] += bf_lo(pv[q][s2][1]); o[3] += bf_hi(pv[q][s2][1]); }
                u32x2 v; v[0] = pk_bf16(o[0] + bf_lo(xv[q][0]), o[1] + bf_hi(xv[q][0])); v[1] = pk_bf16(o[2] + bf_lo(xv[q][1]), o[3] + bf_hi(xv[q][1]));
                *(u32x2*)(out + xoff[q]) = v;
            }
        }
    }
    STAMP(2, 15);
}

extern "C" void kernel_launch(void* const* d_in, const int* in_sizes, int n_in, void* d_out, int out_size, void* d_ws,
                              size_t ws_size, hipStream_t stream) {
    const u16* x = (const u16*)d_in[0];
    const u16* wqkv = (const u16*)d_in[1];
    const u16* wo = (const u16*)d_in[2];
    const u16* Wg = (const u16*)d_in[4];
    const u16* Wu = (const u16*)d_in[5];
    const u16* Wd = (const u16*)d_in[6];
    char* ws = (char*)d_ws;
    u16* hb = (u16*)(ws + WS_HB);
    u16* part = (u16*)(ws + WS_PART);
    u16* dx = (u16*)(ws + WS_DX);
    unsigned* cnt = (unsigned*)(ws + WS_CNT);
    (void)hipFuncSetAttribute((const void*)k1_gateup, hipFuncAttributeMaxDynamicSharedMemorySize, K1_LDS);
    (void)hipFuncSetAttribute((const void*)k2_down, hipFuncAttributeMaxDynamicSharedMemorySize, K2_LDS);
    k1_gateup<<<256, 512, K1_LDS, stream>>>(x, Wg, Wu, wqkv, Wd, wo, hb, cnt, dx);
    k2_down<<<256, 512, K2_LDS, stream>>>(hb, Wd, wo, dx, part, cnt, x, (u16*)d_out);
}
```

```cpp
#define NO_WPF
#include <hip/hip_runtime.h>
#include <hip/hip_bf16.h>
#include <stdint.h>
#include <type_traits>

typedef unsigned short u16;
typedef short bf16x8 __attribute__((ext_vector_type(8)));
typedef short s16x4 __attribute__((ext_vector_type(4)));
typedef float f32x4 __attribute__((ext_vector_type(4)));
typedef float f32x2 __attribute__((ext_vector_type(2)));
typedef __bf16 bf16x2_t __attribute__((ext_vector_type(2)));
typedef unsigned u32x4 __attribute__((ext_vector_type(4)));
typedef unsigned u32x2 __attribute__((ext_vector_type(2)));
#define LAS __attribute__((address_space(3)))
#define GAS __attribute__((address_space(1)))

constexpr int SEQ = 2048, DM = 576, FF = 4608;
constexpr int NKB_X = 18;
constexpr int NKB_H = 162;
constexpr size_t BLK_ELEMS = (size_t)SEQ * 32;

constexpr size_t WS_HB = 18 * 131072, WS_PART = WS_HB + 162 * 131072, WS_WQH = WS_PART + 4 * 64 * 8 * 10 * 512, WS_GX = WS_WQH + 32 * 576 * 64 * 2,
                 WS_DX = WS_GX + 2 * 32 * 576 * 16 * 2, WS_CNT = WS_DX + 4 * 5184 * 16 * 2, WS_END = WS_CNT + 64 * 256;

__device__ __forceinline__ unsigned pk_bf16(float lo, float hi) {
    f32x2 v = {lo, hi};
    bf16x2_t b = __builtin_convertvector(v, bf16x2_t);
    return __builtin_bit_cast(unsigned, b);
}
__device__ __forceinline__ float bf_lo(unsigned u) { return __uint_as_float(u << 16); }
__device__ __forceinline__ float bf_hi(unsigned u) { return __uint_as_float(u & 0xffff0000u); }
__device__ __forceinline__ float rbf(float x) { return bf_lo(pk_bf16(x, x)); }

__device__ __forceinline__ const void* uni(const void* p) {
    const unsigned long long v = (unsigned long long)p;
    const unsigned lo = __builtin_amdgcn_readfirstlane((unsigned)v), hi = __builtin_amdgcn_readfirstlane((unsigned)(v >> 32));
    return (const void*)(((unsigned long long)hi << 32) | lo);
}
__device__ __forceinline__ void glds16(const void* sbase, unsigned voff, unsigned lds) {
    sbase = uni(sbase); lds = __builtin_amdgcn_readfirstlane(lds);
    asm volatile("s_mov_b32 m0, %2\n\ts_nop 3\n\tglobal_load_lds_dwordx4 %0, %1"
                 :: "v"(voff), "s"(sbase), "s"(lds) : "memory", "m0");
}
__device__ __forceinline__ void glds16_o1k(const void* sbase, unsigned voff, unsigned lds) {
    sbase = uni(sbase); lds = __builtin_amdgcn_readfirstlane(lds);
    asm volatile("s_mov_b32 m0, %2\n\ts_nop 3\n\tglobal_load_lds_dwordx4 %0, %1 offset:1024"
                 :: "v"(voff), "s"(sbase), "s"(lds) : "memory", "m0");
}
__device__ __forceinline__ void glds16_q(const void* sbase, unsigned voff, unsigned lds) {
    sbase = uni(sbase); lds = __builtin_amdgcn_readfirstlane(lds);
    unsigned sv; unsigned long long ex;
    asm volatile("s_mov_b32 %0, m0\n\ts_mov_b64 %1, exec\n\ts_mov_b32 m0, %4\n\ts_mov_b64 exec, 0xffff\n\ts_nop 1\n\tglobal_load_lds_dwordx4 %2, %3\n\ts_mov_b64 exec, %1\n\ts_mov_b32 m0, %0"
                 : "=&s"(sv), "=&s"(ex) : "v"(voff), "s"(sbase), "s"(lds) : "memory");
}
__device__ __forceinline__ void glds16_m(const void* sbase, unsigned voff, unsigned lds) {
    sbase = uni(sbase); lds = __builtin_amdgcn_readfirstlane(lds);
    unsigned sv; unsigned long long ex;
    asm volatile("s_mov_b32 %0, m0\n\ts_mov_b64 %1, exec\n\ts_mov_b32 m0, %4\n\ts_mov_b32 exec_lo, -1\n\ts_mov_b32 exec_hi, 0xff\n\ts_nop 1\n\tglobal_load_lds_dwordx4 %2, %3\n\ts_mov_b64 exec, %1\n\ts_mov_b32 m0, %0"
                 : "=&s"(sv), "=&s"(ex) : "v"(voff), "s"(sbase), "s"(lds) : "memory");
}
#define SCHED() __builtin_amdgcn_sched_barrier(0)
#define STAMP(kid, i) do {} while (0)
#define WSTAMP(i) do {} while (0)
__device__ __forceinline__ s16x4 tr_read(LAS char* p) {
    return __builtin_bit_cast(s16x4, __builtin_amdgcn_ds_read_tr16_b64_v4i16((LAS s16x4*)p));
}
__device__ __forceinline__ bf16x8 tr_frag(LAS char* p, int off_t1) {
    s16x4 lo = tr_read(p), hi = tr_read(p + off_t1);
    return __builtin_shufflevector(lo, hi, 0, 1, 2, 3, 4, 5, 6, 7);
}
__device__ __forceinline__ bf16x8 row_frag(LAS char* p) { return *(LAS bf16x8*)p; }
#define MFMA16(a, b, c) __builtin_amdgcn_mfma_f32_16x16x32_bf16((a), (b), (c), 0, 0, 0)
__device__ __forceinline__ void mfma_ip(f32x4& c, bf16x8 a, bf16x8 b) { asm volatile("v_mfma_f32_16x16x32_bf16 %0, %1, %2, %0" : "+v"(c) : "v"(a), "v"(b)); }
#define WAIT_V(n) asm volatile("s_waitcnt vmcnt(" #n ")" ::: "memory")
#define BAR() do { asm volatile("" ::: "memory"); __builtin_amdgcn_s_barrier(); asm volatile("" ::: "memory"); } while (0)

__device__ __forceinline__ int blk_off(int row, int chunk) { return row * 64 + ((chunk ^ (((row >> 3) & 1) << 1)) << 4); }

__global__ __launch_bounds__(256) void k0_ln(const u16* __restrict__ x, u16* __restrict__ xnb,
                                            const u16* __restrict__ wqkv, u16* __restrict__ wqh,
                                            const u16* __restrict__ Wg, const u16* __restrict__ Wu, u16* __restrict__ gx,
                                            const u16* __restrict__ Wd, const u16* __restrict__ wo, u16* __restrict__ dx,
                                            const char* __restrict__ wsb, const char* __restrict__ outb) {
    if (threadIdx.x >= 64) {
        const int l = threadIdx.x - 64, head = blockIdx.x >> 3, k0 = (blockIdx.x & 7) * 72;
        unsigned v[12];
#pragma unroll
        for (int i = 0; i < 12; ++i) {
            const int id = l + 192 * i, k = k0 + (id >> 5), cp = id & 31;
            const int part = cp >= 18 ? 2 : (cp >= 9 ? 1 : 0), d2 = cp - part * 9;
            const int src = (part == 2 ? 0 : part + 1) * DM + 18 * head + 2 * d2;
            v[i] = cp < 27 ? *(const unsigned*)(wqkv + (size_t)k * (3 * DM) + src) : 0u;
        }
        u32x4 c[3];
#pragma unroll
        for (int i = 0; i < 2; ++i) {
            const int id = l + 192 * i;
            if (id < 288) {
                const int gid = blockIdx.x * 288 + id, mat = gid / 36864, r = gid - mat * 36864, k = r >> 6, ch = r & 63;
                c[i] = *(const u32x4*)((mat ? Wu : Wg) + (size_t)k * FF + 4096 + 8 * ch);
            }
        }
        if (l < 162) {
            const int gid = blockIdx.x * 162 + l, k = gid >> 3, ch = gid & 7;
            c[2] = *(const u32x4*)((k < FF ? Wd + (size_t)k * DM : wo + (size_t)(k - FF) * DM) + 512 + 8 * ch);
        }
#pragma unroll
        for (int i = 0; i < 12; ++i) {
            const int id = l + 192 * i;
            *(unsigned*)(wqh + ((size_t)head * 576 + k0) * 64 + 2 * id) = v[i];
        }
#pragma unroll
        for (int i = 0; i < 2; ++i) {
            const int id = l + 192 * i;
            if (id < 288) {
                const int gid = blockIdx.x * 288 + id, mat = gid / 36864, r = gid - mat * 36864, k = r >> 6, ch = r & 63;
                *(u32x4*)(gx + (((size_t)mat * 32 + (ch >> 1)) * 576 + k) * 16 + 8 * (ch & 1)) = c[i];
            }
        }
        if (l < 162) {
            const int gid = blockIdx.x * 162 + l, k = gid >> 3, ch = gid & 7;
            *(u32x4*)(dx + ((size_t)(ch >> 1) * 5184 + k) * 16 + 8 * (ch & 1)) = c[2];
        }
        return;
    }
    const int lane = threadIdx.x, i = lane & 7, row = blockIdx.x * 8 + (lane >> 3);
    u32x4 xv[9];
#pragma unroll
    for (int cc = 0; cc < 9; ++cc) xv[cc] = *(const u32x4*)(x + (size_t)row * DM + (i + 8 * cc) * 8);
    float s = 0.f;
#pragma unroll
    for (int cc = 0; cc < 9; ++cc)
#pragma unroll
        for (int e = 0; e < 4; ++e) s += bf_lo(xv[cc][e]) + bf_hi(xv[cc][e]);
    s += __shfl_xor(s, 1); s += __shfl_xor(s, 2); s += __shfl_xor(s, 4);
    const float mean = s * (1.0f / 576.0f);
    float q = 0.f;
#pragma unroll
    for (int cc = 0; cc < 9; ++cc)
#pragma unroll
        for (int e = 0; e < 4; ++e) { const float a = bf_lo(xv[cc][e]) - mean, c2 = bf_hi(xv[cc][e]) - mean; q += a * a + c2 * c2; }
    q += __shfl_xor(q, 1); q += __shfl_xor(q, 2); q += __shfl_xor(q, 4);
    const float mb = rbf(mean), vb = rbf(q * (1.0f / 576.0f));
    const float sb = rbf(sqrtf(rbf(vb + 1e-5f)));
    const float rs = 1.0f / sb;
#pragma unroll
    for (int cc = 0; cc < 9; ++cc) {
        u32x4 o;
#pragma unroll
        for (int e = 0; e < 4; ++e) {
            const unsigned d = pk_bf16(bf_lo(xv[cc][e]) - mb, bf_hi(xv[cc][e]) - mb);
            o[e] = pk_bf16(bf_lo(d) * rs, bf_hi(d) * rs);
        }
        const int blk = 2 * cc + (i >> 2);
        *(u32x4*)((char*)xnb + (size_t)blk * (BLK_ELEMS * 2) + blk_off(row, i & 3)) = o;
    }
}

constexpr int RA = 0, RB = 65536, RE = 131072, RQ = 139264, RH = 155648;
constexpr int T_DUMMY = 159744;
constexpr int K1_LDS = T_DUMMY + 1024;
constexpr int QS = 144;
#define HST_AUX 16
#define PF 3
constexpr int NB = PF + 1;
template <int N> __device__ __forceinline__ void wait_vm() { asm volatile("s_waitcnt vmcnt(%0)" :: "i"(N) : "memory"); }
#define GLDS_MASKED(NAME, LO, HI) \
__device__ __forceinline__ void NAME(const void* sbase, unsigned voff, unsigned lds) { \
    sbase = uni(sbase); lds = __builtin_amdgcn_readfirstlane(lds); \
    unsigned sv; unsigned long long ex; \
    asm volatile("s_mov_b32 %0, m0\n\ts_mov_b64 %1, exec\n\ts_mov_b32 m0, %4\n\ts_mov_b32 exec_lo, " #LO "\n\ts_mov_b32 exec_hi, " #HI "\n\tglobal_load_lds_dwordx4 %2, %3\n\ts_mov_b64 exec, %1\n\ts_mov_b32 m0, %0" \
                 : "=&s"(sv), "=&s"(ex) : "v"(voff), "s"(sbase), "s"(lds) : "memory"); }
GLDS_MASKED(glds16_32, -1, 0)
GLDS_MASKED(glds16_8, 0xff, 0)

__device__ __forceinline__ void k1_epilogue(f32x4 (&acc)[11][4], f32x4& ach, LAS char* lds, unsigned lds0, int w, int xcd, int idx,
                                            int j, int mt, const u16* __restrict__ Wd, const u16* __restrict__ wo, u16* __restrict__ hb) {
    int tid = threadIdx.x; asm volatile("" : "+v"(tid));
    const int lane = tid & 63, fr = lane & 15, g = lane >> 4;
    const int wm = w >> 1, wn = w & 1;
    const __amdgpu_buffer_rsrc_t rh = __builtin_amdgcn_make_buffer_rsrc((void*)hb, 0, (int)(NKB_H * BLK_ELEMS * 2), 0x00020000);
    auto hs = [&](int m, int part) {
        const int row = 256 * mt + 64 * wm + 16 * m + fr;
        SCHED();
        if (part < 2) {
            const int pp = part;
            unsigned v[2][2];
#pragma unroll
            for (int hh = 0; hh < 2; ++hh) {
                const int gt = 2 * pp + hh;
                float hv[4];
#pragma unroll
                for (int e = 0; e < 4; ++e) hv[e] = fmaxf(acc[2 * gt][m][e], 0.f) * acc[2 * gt + 1][m][e];
                v[hh][0] = pk_bf16(hv[0], hv[1]); v[hh][1] = pk_bf16(hv[2], hv[3]);
            }
            auto r0 = __builtin_amdgcn_permlane16_swap(v[0][0], v[1][0], false, false);
            auto r1 = __builtin_amdgcn_permlane16_swap(v[0][1], v[1][1], false, false);
            u32x4 o; o[0] = r0[0]; o[1] = r1[0]; o[2] = r0[1]; o[3] = r1[1];
            const int blk = 4 * j + 2 * wn + pp, chunk = ((g & 1) << 1) | (g >> 1);
            __builtin_amdgcn_raw_buffer_store_b128(o, rh, (unsigned)(blk * (int)(BLK_ELEMS * 2) + blk_off(row, chunk)), 0, HST_AUX);
        } else {
            float hv[4];
#pragma unroll
            for (int e = 0; e < 4; ++e) {
                const unsigned mine = __float_as_uint(acc[8][m][e]);
                auto r = __builtin_amdgcn_permlane32_swap(mine, mine, false, false);
                hv[e] = fmaxf(__uint_as_float(r[0]), 0.f) * __uint_as_float(r[1]);
            }
            u32x2 v; v[0] = pk_bf16(hv[0], hv[1]); v[1] = pk_bf16(hv[2], hv[3]);
            const int blk = 128 + (j >> 1), chunk = (j & 1) * 2 + wn;
            const unsigned off = g < 2 ? (unsigned)(blk * (int)(BLK_ELEMS * 2) + blk_off(row, chunk) + g * 8) : 0xfffffff0u;
            __builtin_amdgcn_raw_buffer_store_b64(v, rh, off, 0, HST_AUX);
        }
        SCHED();
    };
    hs(0, 0);
    BAR();
    STAMP(1, 2);
    LAS char* qt = lds;
    {
#pragma unroll
        for (int u = 0; u < 2; ++u) {
            const int nn = 16 * (2 * wn + (u ^ (wm & 1))) + 4 * g;
            const int h0 = nn / 18, h1 = (nn + 2) / 18;
            const int o0 = h0 * 48 + (nn - h0 * 18) * 2, o1 = h1 * 48 + (nn + 2 - h1 * 18) * 2;
#pragma unroll
            for (int m = 0; m < 4; ++m) {
                LAS char* rp = qt + (16 + 64 * wm + 16 * m + fr) * QS;
                if (h0 < 3) *(LAS unsigned*)(rp + o0) = pk_bf16(acc[9 + u][m][0], acc[9 + u][m][1]);
                if (h1 < 3) *(LAS unsigned*)(rp + o1) = pk_bf16(acc[9 + u][m][2], acc[9 + u][m][3]);
            }
            if (u == 0 && (w == 0 || w == 2 || w == 1)) {
                LAS char* rp = qt + fr * QS;
                if (h0 < 3) *(LAS unsigned*)(rp + o0) = pk_bf16(ach[0], ach[1]);
                if (h1 < 3) *(LAS unsigned*)(rp + o1) = pk_bf16(ach[2], ach[3]);
            }
            if (u == 0) hs(0, 1);
        }
    }
    asm volatile("s_waitcnt lgkmcnt(0)" ::: "memory");
    BAR();
    STAMP(1, 3);
    {
        const int s = tid >> 1, half = tid & 1;
        const int lrow = 16 + s, grow = 256 * mt + s;
        auto ld18 = [&](LAS const char* p, unsigned (&v)[9]) {
            const u32x4 a = *(LAS const u32x4*)p, c = *(LAS const u32x4*)(p + 16);
            v[0] = a[0]; v[1] = a[1]; v[2] = a[2]; v[3] = a[3]; v[4] = c[0]; v[5] = c[1]; v[6] = c[2]; v[7] = c[3];
            v[8] = *(LAS const unsigned*)(p + 32);
        };
        hs(0, 2);
        unsigned qv[9];
        ld18(qt + lrow * QS + 96, qv);
        float lg[4];
#pragma unroll
        for (int ww = 0; ww < 4; ++ww) {
            const int wi = 4 * half + ww;
            unsigned kv[9];
            ld18(qt + (lrow - 2 * wi) * QS, kv);
            float a = 0.f;
#pragma unroll
            for (int i = 0; i < 9; ++i) a = __builtin_amdgcn_fdot2_f32_bf16(__builtin_bit_cast(bf16x2_t, qv[i]), __builtin_bit_cast(bf16x2_t, kv[i]), a, false);
            a = rbf(rbf(a) * 0.23570226039551584f);
            lg[ww] = (grow - 2 * wi >= 0) ? a : -1e9f;
            if (ww == 0) hs(1, 0);
            if (ww == 2) hs(1, 1);
        }
        float mx = fmaxf(fmaxf(lg[0], lg[1]), fmaxf(lg[2], lg[3]));
        mx = fmaxf(mx, __shfl_xor(mx, 1));
        float ex[4], sum = 0.f;
#pragma unroll
        for (int ww = 0; ww < 4; ++ww) { ex[ww] = __expf(lg[ww] - mx); sum += ex[ww]; }
        sum += __shfl_xor(sum, 1);
        const float inv = 1.0f / sum;
        hs(1, 2);
        float o[18];
#pragma unroll
        for (int i = 0; i < 18; ++i) o[i] = 0.f;
#pragma unroll
        for (int ww = 0; ww < 4; ++ww) {
            const int wi = 4 * half + ww;
            const float pw = (grow - 2 * wi >= 0) ? rbf(ex[ww] * inv) : 0.f;
            unsigned vv[9];
            ld18(qt + (lrow - 2 * wi) * QS + 48, vv);
#pragma unroll
            for (int i = 0; i < 9; ++i) { o[2 * i] += pw * bf_lo(vv[i]); o[2 * i + 1] += pw * bf_hi(vv[i]); }
            if (ww < 3) hs(2, ww);
        }
        hs(3, 0);
#pragma unroll
        for (int ii = 0; ii < 5; ++ii) {
            const float s0 = half ? o[2 * ii] : o[(10 + 2 * ii) % 18], s1 = half ? o[2 * ii + 1] : o[(11 + 2 * ii) % 18];
            const float r0 = __shfl_xor(s0, 1), r1 = __shfl_xor(s1, 1);
            const float m0 = half ? o[(10 + 2 * ii) % 18] : o[2 * ii], m1 = half ? o[(11 + 2 * ii) % 18] : o[2 * ii + 1];
            const int i = half * 5 + ii;
            if (i < 9) {
                const int col = FF + 18 * j + 2 * i;
                *(unsigned*)((char*)hb + (size_t)(col >> 5) * (BLK_ELEMS * 2) + blk_off(grow, (col >> 3) & 3) + (col & 7) * 2) = pk_bf16(m0 + r0, m1 + r1);
            }
            if (ii == 2) hs(3, 1);
        }
        hs(3, 2);
    }
    STAMP(1, 15);
}

__global__ __launch_bounds__(512, 2) void k1_gateup(const u16* __restrict__ xnb, const u16* __restrict__ Wg,
                                                    const u16* __restrict__ Wu, const u16* __restrict__ wqh, const u16* __restrict__ gx,
                                                    const u16* __restrict__ Wd, const u16* __restrict__ wo, u16* __restrict__ hb, unsigned* __restrict__ cnt) {
    extern __shared__ __attribute__((aligned(16))) char smem[];
    LAS char* lds = (LAS char*)smem;
    const int tid = threadIdx.x, lane = tid & 63;
    if (blockIdx.x < 64 && tid == 0) cnt[blockIdx.x * 64] = 0u;
    const int w = __builtin_amdgcn_readfirstlane(tid >> 6);
    const int b = blockIdx.x, xcd = b & 7, idx = b >> 3;
    const int j = 4 * xcd + (idx & 3), mt = idx >> 2;
    const int wm = w >> 1, wn = w & 1;
    const unsigned lds0 = (unsigned)(size_t)lds;
    const int fr = lane & 15, g = lane >> 4, q4 = (lane >> 2) & 3, p4 = lane & 3;

    f32x4 acc[11][4], ach;
    {
        const unsigned srcA = (unsigned)((256 * mt + 32 * w) * 32 + lane * 8) * 2u;
        const int koct = w >> 1, mat = w & 1;
        const u16* W = mat ? Wu : Wg;
        unsigned srcB, srcX;
        const u16* baseX; int strideX; unsigned dX;
        {
            const int r = lane >> 3, swz = ((r >> 1) & 1) | ((koct & 1) << 1), c = (lane & 7) ^ (swz << 1);
            srcB = (unsigned)((8 * koct + r) * FF + 128 * j + 8 * c) * 2u;
            if (w < 2) {
                const int ko = lane >> 4, re = ((lane >> 1) & 7) ^ ((ko & 1) << 2), ce = (lane & 1) ^ w;
                srcX = (unsigned)(((w * 32 + j) * 576 + 8 * ko + re) * 16 + 8 * ce) * 2u;
                baseX = gx; strideX = 32 * 16; dX = lds0 + RE + w * 1024;
            } else if (w < 6) {
                const int ko = w - 2, swq = ((r >> 1) & 1) | ((ko & 1) << 1), cq = (lane & 7) ^ (swq << 1);
                srcX = (unsigned)((j * 576 + 8 * ko + r) * 64 + 8 * cq) * 2u;
                baseX = wqh; strideX = 32 * 64; dX = lds0 + RQ + ko * 1024;
            } else {
                const int hr = mt > 0 ? 256 * mt - 16 : 0;
                srcX = (unsigned)(hr * 64 + lane * 16);
                baseX = xnb; strideX = (int)BLK_ELEMS; dX = lds0 + RH;
            }
        }
        const int slotX = w < 2 ? 2048 : (w < 6 ? 4096 : 1024);
        const unsigned dA = lds0 + RA + (2 * w) * 1024, dB = lds0 + RB + (2 * w) * 1024;
        auto issue1 = [&](int t, int sl, int i) {
            const u16* ba = xnb + (size_t)t * BLK_ELEMS;
            const u16* bw = W + (size_t)t * 32 * FF;
            if (i == 0) glds16(bw, srcB, dB + sl * 16384);
            if (i == 1) glds16(bw + 64, srcB, dB + sl * 16384 + 1024);
            if (i == 2) glds16(baseX + (size_t)t * strideX, srcX, dX + sl * slotX);
            if (i == 3) glds16(ba, srcA, dA + sl * 16384);
            if (i == 4) glds16_o1k(ba, srcA, dA + sl * 16384);
        };
        auto prefetch = [&](int p) {
            const int split = xcd >> 1;
            const unsigned start = (unsigned)((split == 0 ? 0 : 1344 + 1280 * (split - 1)) * DM * 2);
            const unsigned len = (unsigned)((split == 0 ? 1344 : 1280) * DM * 2), lwd = (unsigned)(704 * DM * 2);
            p = p < 6 ? p : 5;
            unsigned o = (unsigned)((idx * 8 + w) * 6144 + p * 1024); o = o + 1024 <= len ? o : 0u;
            const char* src = (split == 3 && o >= lwd) ? (const char*)wo + (o - lwd) : (const char*)Wd + start + o;
            glds16(src, (unsigned)lane * 16u, lds0 + T_DUMMY);
        };
        STAMP(1, 0);
#pragma unroll
        for (int t = 0; t < 3; ++t) {
#pragma unroll
            for (int i = 0; i < 5; ++i) issue1(t, t, i);
            if (t == 1) prefetch(0);
        }

        const unsigned pa = lds0 + RA + blk_off(64 * wm + fr, g);
        const int swz = ((q4 >> 1) & 1) | ((g & 1) << 1);
        const unsigned pb0 = lds0 + RB + (g * 4 + wn) * 1024 + q4 * 128 + (swz << 5) + p4 * 8;
        const int emat = p4 >> 1;
        const unsigned pe0 = lds0 + RE + (emat * 4 + g) * 256 + ((q4 ^ ((g & 1) << 2)) * 32) + ((wn ^ emat) * 16) + (p4 & 1) * 8;
        const unsigned pq0 = lds0 + RQ + g * 1024 + q4 * 128 + (((2 * wn + (wm & 1)) ^ swz) << 5) + p4 * 8;
        LAS char* pbv[4]; LAS char* pqv[2];
#pragma unroll
        for (int u = 0; u < 4; ++u) pbv[u] = (LAS char*)(size_t)(pb0 ^ (u << 5));
        pqv[0] = (LAS char*)(size_t)pq0; pqv[1] = (LAS char*)(size_t)(pq0 ^ 32);
        LAS char* pev[2] = {(LAS char*)(size_t)pe0, (LAS char*)(size_t)(pe0 ^ 128)};
        LAS char* phv = lds + RH + blk_off(fr, g);
        auto rdB = [&](int sl, int nf, int t1) -> s16x4 {
            if (nf < 8) return tr_read(pbv[nf >> 1] + sl * 16384 + (nf & 1) * 2048 + t1 * 512);
            if (nf == 8) return tr_read(pev[t1] + sl * 2048);
            return tr_read(pqv[nf - 9] + sl * 4096 + t1 * 512);
        };
        auto rdA = [&](int sl, int m) -> bf16x8 { return row_frag((LAS char*)(size_t)pa + sl * 16384 + m * 1024); };
        auto rdH = [&](int sl) -> bf16x8 { return row_frag(phv + sl * 1024); };
#pragma unroll
        for (int n = 0; n < 11; ++n)
#pragma unroll
            for (int m = 0; m < 4; ++m) { acc[n][m] = (f32x4){0.f, 0.f, 0.f, 0.f}; asm volatile("" : "+v"(acc[n][m])); }
        ach = (f32x4){0.f, 0.f, 0.f, 0.f}; asm volatile("" : "+v"(ach));

        wait_vm<11>(); BAR();
        STAMP(1, 1);
        bf16x8 fa[2][2], fc[2], fh, fb[NB];
#pragma unroll
        for (int m = 0; m < 2; ++m) { fa[0][m] = rdA(0, m); fc[m] = rdA(0, 2 + m); }
#pragma unroll
        for (int i = 0; i < PF; ++i) { s16x4 lo = rdB(0, i, 0), hi = rdB(0, i, 1); fb[i] = __builtin_shufflevector(lo, hi, 0, 1, 2, 3, 4, 5, 6, 7); }
#define MM(C, A, B) mfma_ip(C, A, B)
        auto step = [&](auto S_, int t, bool do_issue, auto LAST1_, auto WAIT0_) {
            constexpr int S = decltype(S_)::value;
            constexpr bool has_next = !decltype(LAST1_)::value;
            constexpr bool wait0 = decltype(WAIT0_)::value;
            constexpr int sl = S, sn = (S + 1) & 3, cur = S & 1;
#pragma unroll
            for (int nf = 0; nf < 11; ++nf) {
                constexpr int dummy = 0; (void)dummy;
                const int r = 11 * S + nf;
                const int psl = (nf + PF < 11) ? sl : sn, nn = (nf + PF < 11) ? nf + PF : nf + PF - 11;
                const bool pf = (nf + PF < 11) || has_next;
                const bool lastg = (nf == 10);
                s16x4 lo, hi;
                if (!lastg) MM(acc[nf][0], fb[r % NB], fa[cur][0]); else MM(acc[nf][2], fb[r % NB], fc[0]);
                SCHED();
                if (nf == 0) {
                    if (!wait0) wait_vm<6>(); else wait_vm<0>();
                    BAR();
                }
                if (pf) lo = rdB(psl, nn, 0);
                SCHED();
                if (!lastg) MM(acc[nf][1], fb[r % NB], fa[cur][1]); else MM(acc[nf][3], fb[r % NB], fc[1]);
                SCHED();
                if (pf) hi = rdB(psl, nn, 1);
                if (lastg && has_next) fc[0] = rdA(sn, 2);
                SCHED();
                if (!lastg) MM(acc[nf][2], fb[r % NB], fc[0]); else MM(acc[nf][0], fb[r % NB], fa[cur][0]);
                SCHED();
                if (lastg && has_next) fc[1] = rdA(sn, 3);
                if (nf >= 4 && nf < 6 && has_next) fa[cur ^ 1][nf - 4] = rdA(sn, nf - 4);
                if (nf == 7) fh = rdH(sl);
                if ((nf < 4 || nf == 6) && do_issue) issue1(t + 3, (S + 3) & 3, nf < 4 ? nf : 4);
                if (nf == 8 && (S & 1) == 0 && do_issue) prefetch(1 + (t >> 1));
                SCHED();
                if (!lastg) MM(acc[nf][3], fb[r % NB], fc[1]); else MM(acc[nf][1], fb[r % NB], fa[cur][1]);
                SCHED();
                if (nf == 9) { MM(ach, fb[r % NB], fh); SCHED(); }
                if (pf) fb[(r + PF) % NB] = __builtin_shufflevector(lo, hi, 0, 1, 2, 3, 4, 5, 6, 7);
            }
        };
        using I0 = std::integral_constant<int, 0>; using I1 = std::integral_constant<int, 1>;
        using I2 = std::integral_constant<int, 2>; using I3 = std::integral_constant<int, 3>;
        using F = std::false_type; using T = std::true_type;
#pragma unroll 1
        for (int t0 = 0; t0 < 16; t0 += 4) {
            step(I0{}, t0, true, F{}, F{});
            step(I1{}, t0 + 1, true, F{}, F{});
            step(I2{}, t0 + 2, true, F{}, F{});
            step(I3{}, t0 + 3, t0 + 3 < 15, F{}, F{});
        }
        step(I0{}, 16, false, F{}, T{});
        step(I1{}, 17, false, T{}, T{});
    }
    asm volatile("s_nop 7\n\ts_nop 7" ::: "memory");
    STAMP(1, 4);
    k1_epilogue(acc, ach, lds, lds0, w, xcd, idx, j, mt, Wd, wo, hb);
}

constexpr int K2_NT = 21;
constexpr int K2_RB = 0, K2_RE = 65536, K2_LDS = 81920;
constexpr int NSPLIT = 4;
__device__ __forceinline__ void gload2(bf16x8& d0, bf16x8& d1, const void* sbase, unsigned voff) {
    sbase = uni(sbase);
    asm volatile("s_nop 4\n\tglobal_load_dwordx4 %0, %2, %3\n\tglobal_load_dwordx4 %1, %2, %3 offset:1024"
                 : "=&v"(d0), "=&v"(d1) : "v"(voff), "s"(sbase) : "memory");
}
__global__ __launch_bounds__(512, 2) void k2_down(const u16* __restrict__ hb, const u16* __restrict__ Wd,
                                                  const u16* __restrict__ wo, const u16* __restrict__ dx, u16* __restrict__ part, unsigned* __restrict__ cnt,
                                                  const u16* __restrict__ x, u16* __restrict__ out) {
    extern __shared__ __attribute__((aligned(16))) char smem[];
    LAS char* lds = (LAS char*)smem;
    const int tid = threadIdx.x, lane = tid & 63;
    const int w = __builtin_amdgcn_readfirstlane(tid >> 6);
    const int b = blockIdx.x, xcd = b & 7, idx = b >> 3;
    const int split = xcd >> 1, mt = (xcd & 1) * 8 + (idx >> 2), nt = idx & 3;
    const int wm = w >> 1, kh = w & 1;
    const int T0 = split == 0 ? 0 : 21 + 20 * (split - 1);

    const unsigned srcA = (unsigned)(kh * (int)BLK_ELEMS * 2 + blk_off(128 * mt + 32 * wm + (lane & 15), lane >> 4));
    const int koct = w >> 1, sub = w & 1;
    unsigned offB, offE;
    {
        const int r = lane >> 3, swz = ((r >> 1) & 1) | ((koct & 1) << 1), c = (lane & 7) ^ (swz << 1);
        offB = (unsigned)((32 * sub + 8 * koct + r) * DM + 128 * nt + 8 * c) * 2u;
        const int re = ((lane >> 1) & 7) ^ ((koct & 1) << 2), ce = (lane & 1);
        offE = (unsigned)((nt * 5184 + 32 * sub + 8 * koct + re) * 16 + 8 * ce) * 2u;
    }
    const unsigned lds0 = (unsigned)(size_t)lds;
    const unsigned dB = lds0 + K2_RB + (2 * w) * 1024, dE = lds0 + K2_RE + (sub * 4 + koct) * 256;
    bf16x8 fa[4][2];
    auto issue1 = [&](int t, int sl, int i, bf16x8& a0, bf16x8& a1) {
        int T = T0 + t; if (t == K2_NT - 1 && split != 0) T -= 1;
        const u16* Wt = T < 72 ? Wd + (size_t)T * 64 * DM : wo + (size_t)(T - 72) * 64 * DM;
        const u16* ba = hb + (size_t)T * 2 * BLK_ELEMS;
        if (i == 0) glds16(Wt, offB, dB + sl * 16384);
        if (i == 1) glds16(Wt + 64, offB, dB + sl * 16384 + 1024);
        if (i == 2) glds16_q(dx + (size_t)T * 64 * 16, offE, dE + sl * 2048);
        if (i == 3) gload2(a0, a1, ba, srcA);
    };
    STAMP(2, 0);
#pragma unroll
    for (int t = 0; t < 3; ++t)
#pragma unroll
        for (int i = 0; i < 4; ++i) issue1(t, t, i, fa[t][0], fa[t][1]);

    const int fr = lane & 15, g = lane >> 4, q4 = (lane >> 2) & 3, p4 = lane & 3;
    const int swz = ((q4 >> 1) & 1) | ((g & 1) << 1);
    LAS char* pb[4];
#pragma unroll
    for (int u = 0; u < 4; ++u) pb[u] = lds + K2_RB + (g * 4 + kh * 2) * 1024 + q4 * 128 + ((u ^ swz) << 5) + p4 * 8;
    LAS char* pe0 = lds + K2_RE + (kh * 4 + g) * 256 + ((q4 ^ ((g & 1) << 2)) * 32) + p4 * 8;
    LAS char* pe1 = lds + K2_RE + (kh * 4 + g) * 256 + (((4 + q4) ^ ((g & 1) << 2)) * 32) + p4 * 8;
    auto rdB = [&](int sl, int nf, int t1) -> s16x4 {
        if (nf < 8) return tr_read(pb[nf & 3] + sl * 16384 + (nf >> 2) * 1024 + t1 * 512);
        return tr_read((t1 ? pe1 : pe0) + sl * 2048);
    };

    f32x4 acc[9][2];
#pragma unroll
    for (int n = 0; n < 9; ++n)
#pragma unroll
        for (int m = 0; m < 2; ++m) { acc[n][m] = (f32x4){0.f, 0.f, 0.f, 0.f}; asm volatile("" : "+v"(acc[n][m])); }

    wait_vm<10>(); BAR();
    STAMP(2, 1);
    constexpr int PF2 = 4;
    bf16x8 fb[9];
#pragma unroll
    for (int i = 0; i < PF2; ++i) { s16x4 lo = rdB(0, i, 0), hi = rdB(0, i, 1); fb[i] = __builtin_shufflevector(lo, hi, 0, 1, 2, 3, 4, 5, 6, 7); }
    auto step = [&](auto S_, int t, bool do_issue, bool wait0, auto LAST_) {
        constexpr int S = decltype(S_)::value;
        constexpr bool has_next = !decltype(LAST_)::value;
        constexpr int sl = S, sn = (S + 1) & 3;
        asm volatile("" : "+v"(fa[sl][0]), "+v"(fa[sl][1]));
        if (!has_next && split != 0) {
#pragma unroll
            for (int m = 0; m < 2; ++m) fa[sl][m] = (bf16x8){0, 0, 0, 0, 0, 0, 0, 0};
            asm volatile("s_nop 1" : "+v"(fa[sl][0]), "+v"(fa[sl][1]));
        }
#pragma unroll
        for (int nf = 0; nf < 9; ++nf) {
            const int psl = (nf + PF2 < 9) ? sl : sn, nn = (nf + PF2 < 9) ? nf + PF2 : nf + PF2 - 9;
            const bool pf = (nf + PF2 < 9) || has_next;
            s16x4 lo, hi;
            mfma_ip(acc[nf][0], fb[nf], fa[sl][0]);
            SCHED();
            if (nf == 0) {
                if (!wait0) wait_vm<5>(); else wait_vm<0>();
                BAR();
            }
            if (pf) lo = rdB(psl, nn, 0);
            SCHED();
            mfma_ip(acc[nf][1], fb[nf], fa[sl][1]);
            SCHED();
            if (pf) hi = rdB(psl, nn, 1);
            if (nf >= 1 && nf < 5 && do_issue) issue1(t + 3, (S + 3) & 3, nf - 1, fa[(S + 3) & 3][0], fa[(S + 3) & 3][1]);
            SCHED();
            if (pf) fb[nn] = __builtin_shufflevector(lo, hi, 0, 1, 2, 3, 4, 5, 6, 7);
        }
    };
    using I0 = std::integral_constant<int, 0>; using I1 = std::integral_constant<int, 1>;
    using I2 = std::integral_constant<int, 2>; using I3 = std::integral_constant<int, 3>;
#pragma unroll 1
    for (int t0 = 0; t0 < 20; t0 += 4) {
        step(I0{}, t0, true, false, std::false_type{});
        step(I1{}, t0 + 1, true, false, std::false_type{});
        step(I2{}, t0 + 2, t0 + 2 < 18, false, std::false_type{});
        step(I3{}, t0 + 3, t0 + 3 < 18, t0 + 3 == 19, std::false_type{});
    }
    step(I0{}, 20, false, true, std::true_type{});
    asm volatile("s_nop 7\n\ts_nop 7" ::: "memory");
    BAR();
    STAMP(2, 2);
    f32x4 val[10];
    {
        LAS char* xw = lds + w * 10240;
        LAS char* xr = lds + (w ^ 1) * 10240;
#pragma unroll
        for (int n = 0; n < 9; ++n) {
            const bool give = kh ? (n < 5) : (n >= 5);
            if (give) {
#pragma unroll
                for (int m = 0; m < 2; ++m) *(LAS f32x4*)(xw + ((kh ? n : n - 5) * 2 + m) * 1024 + lane * 16) = acc[n][m];
            }
        }
        asm volatile("s_waitcnt lgkmcnt(0)" ::: "memory");
        BAR();
#pragma unroll
        for (int i = 0; i < 10; ++i) {
            const f32x4 mine = kh ? acc[(5 + (i >> 1)) < 9 ? 5 + (i >> 1) : 8][i & 1] : acc[i >> 1][i & 1];
            val[i] = mine + *(LAS f32x4*)(xr + i * 1024 + lane * 16);
        }
    }
    {
        const int nit = kh ? 8 : 10;
        const int grp = mt * 4 + nt;
        const __amdgpu_buffer_rsrc_t rp = __builtin_amdgcn_make_buffer_rsrc((void*)part, 0, NSPLIT * 64 * 8 * 10 * 512, 0x00020000);
        const unsigned slot = (unsigned)((grp * 8 + w) * 10 * 512 + lane * 8);
        constexpr unsigned SPL = 64 * 8 * 10 * 512;
        u32x2 xv[3]; unsigned xoff[3];
#pragma unroll
        for (int q = 0; q < 3; ++q) {
            int i = split + 4 * q; i = i < nit ? i : split;
            const int n = kh ? 5 + (i >> 1) : (i >> 1), m = i & 1;
            const int row = 128 * mt + 32 * wm + 16 * m + fr;
            const int col = (n < 8 ? 128 * nt + 16 * n : 512 + 16 * nt) + 4 * g;
            xoff[q] = (unsigned)(row * DM + col);
            xv[q] = *(const u32x2*)(x + xoff[q]);
        }
#pragma unroll
        for (int i = 0; i < 10; ++i) {
            if (i < nit && (i & 3) != split) {
                u32x2 v; v[0] = pk_bf16(val[i][0], val[i][1]); v[1] = pk_bf16(val[i][2], val[i][3]);
                __builtin_amdgcn_raw_buffer_store_b64(v, rp, slot + split * SPL + i * 512, 0, 16);
            }
        }
        STAMP(2, 3);
        asm volatile("s_waitcnt vmcnt(0)" ::: "memory");
        BAR();
        STAMP(2, 4);
        unsigned* c = cnt + grp * 64;
        if (tid == 0) {
            __hip_atomic_fetch_add(c, 1u, __ATOMIC_RELAXED, __HIP_MEMORY_SCOPE_AGENT);
            unsigned spins = 0;
            while (__hip_atomic_load(c, __ATOMIC_RELAXED, __HIP_MEMORY_SCOPE_AGENT) < (unsigned)NSPLIT) {
                __builtin_amdgcn_s_sleep(1);
                if (++spins > (1u << 22)) break;
            }
        }
        BAR();
        STAMP(2, 5);
        u32x2 pv[3][3];
#pragma unroll
        for (int q = 0; q < 3; ++q) {
            const int i = split + 4 * q;
#pragma unroll
            for (int s2 = 1; s2 < NSPLIT; ++s2) {
                const int so = (split + s2) & 3;
                const unsigned off = i < nit ? slot + so * SPL + i * 512 : 0xfffffff0u;
                pv[q][s2 - 1] = __builtin_amdgcn_raw_buffer_load_b64(rp, off, 0, 16);
            }
        }
#pragma unroll
        for (int q = 0; q < 3; ++q) {
            const int i = split + 4 * q;
            if (i < nit) {
                f32x4 o = val[0];
#pragma unroll
                for (int ii = 0; ii < 10; ++ii) if (ii == i) o = val[ii];
#pragma unroll
                for (int s2 = 0; s2 < 3; ++s2) { o[0] += bf_lo(pv[q][s2][0]); o[1] += bf_hi(pv[q][s2][0]); o[2] += bf_lo(pv[q][s2][1]); o[3] += bf_hi(pv[q][s2][1]); }
                u32x2 v; v[0] = pk_bf16(o[0] + bf_lo(xv[q][0]), o[1] + bf_hi(xv[q][0])); v[1] = pk_bf16(o[2] + bf_lo(xv[q][1]), o[3] + bf_hi(xv[q][1]));
                *(u32x2*)(out + xoff[q]) = v;
            }
        }
    }
    STAMP(2, 15);
}

extern "C" void kernel_launch(void* const* d_in, const int* in_sizes, int n_in, void* d_out, int out_size, void* d_ws,
                              size_t ws_size, hipStream_t stream) {
    const u16* x = (const u16*)d_in[0];
    const u16* wqkv = (const u16*)d_in[1];
    const u16* wo = (const u16*)d_in[2];
    const u16* Wg = (const u16*)d_in[4];
    const u16* Wu = (const u16*)d_in[5];
    const u16* Wd = (const u16*)d_in[6];
    char* ws = (char*)d_ws;
    u16* xnb = (u16*)ws;
    u16* hb = (u16*)(ws + WS_HB);
    u16* part = (u16*)(ws + WS_PART);
    u16* wqh = (u16*)(ws + WS_WQH);
    u16* gx = (u16*)(ws + WS_GX);
    u16* dx = (u16*)(ws + WS_DX);
    unsigned* cnt = (unsigned*)(ws + WS_CNT);
    (void)hipFuncSetAttribute((const void*)k1_gateup, hipFuncAttributeMaxDynamicSharedMemorySize, K1_LDS);
    (void)hipFuncSetAttribute((const void*)k2_down, hipFuncAttributeMaxDynamicSharedMemorySize, K2_LDS);
    k0_ln<<<256, 256, 0, stream>>>(x, xnb, wqkv, wqh, Wg, Wu, gx, Wd, wo, dx, (const char*)ws, (const char*)d_out);
    k1_gateup<<<256, 512, K1_LDS, stream>>>(xnb, Wg, Wu, wqh, gx, Wd, wo, hb, cnt);
    k2_down<<<256, 512, K2_LDS, stream>>>(hb, Wd, wo, dx, part, cnt, x, (u16*)d_out);
}
```

```cpp
#define NO_WPF
#include <hip/hip_runtime.h>
#include <hip/hip_bf16.h>
#include <stdint.h>
#include <type_traits>

typedef unsigned short u16;
typedef short bf16x8 __attribute__((ext_vector_type(8)));
typedef short s16x4 __attribute__((ext_vector_type(4)));
typedef float f32x4 __attribute__((ext_vector_type(4)));
typedef float f32x2 __attribute__((ext_vector_type(2)));
typedef __bf16 bf16x2_t __attribute__((ext_vector_type(2)));
typedef unsigned u32x4 __attribute__((ext_vector_type(4)));
typedef unsigned u32x2 __attribute__((ext_vector_type(2)));
#define LAS __attribute__((address_space(3)))
#define GAS __attribute__((address_space(1)))

constexpr int SEQ = 2048, DM = 576, FF = 4608;
constexpr int NKB_X = 18;
constexpr int NKB_H = 162;
constexpr size_t BLK_ELEMS = (size_t)SEQ * 32;

constexpr size_t WS_HB = 18 * 131072, WS_PART = WS_HB + 162 * 131072, WS_WQH = WS_PART + 8 * 32 * 8 * 18 * 512, WS_GX = WS_WQH + 32 * 576 * 64 * 2,
                 WS_DX = WS_GX + 2 * 32 * 576 * 16 * 2, WS_CNT = WS_DX + 4 * 5184 * 16 * 2, WS_END = WS_CNT + 64 * 256;

__device__ __forceinline__ unsigned pk_bf16(float lo, float hi) {
    f32x2 v = {lo, hi};
    bf16x2_t b = __builtin_convertvector(v, bf16x2_t);
    return __builtin_bit_cast(unsigned, b);
}
__device__ __forceinline__ float bf_lo(unsigned u) { return __uint_as_float(u << 16); }
__device__ __forceinline__ float bf_hi(unsigned u) { return __uint_as_float(u & 0xffff0000u); }
__device__ __forceinline__ float rbf(float x) { return bf_lo(pk_bf16(x, x)); }

__device__ __forceinline__ const void* uni(const void* p) {
    const unsigned long long v = (unsigned long long)p;
    const unsigned lo = __builtin_amdgcn_readfirstlane((unsigned)v), hi = __builtin_amdgcn_readfirstlane((unsigned)(v >> 32));
    return (const void*)(((unsigned long long)hi << 32) | lo);
}
__device__ __forceinline__ void glds16(const void* sbase, unsigned voff, unsigned lds) {
    sbase = uni(sbase); lds = __builtin_amdgcn_readfirstlane(lds);
    asm volatile("s_mov_b32 m0, %2\n\ts_nop 3\n\tglobal_load_lds_dwordx4 %0, %1"
                 :: "v"(voff), "s"(sbase), "s"(lds) : "memory", "m0");
}
__device__ __forceinline__ void glds16_o1k(const void* sbase, unsigned voff, unsigned lds) {
    sbase = uni(sbase); lds = __builtin_amdgcn_readfirstlane(lds);
    asm volatile("s_mov_b32 m0, %2\n\ts_nop 3\n\tglobal_load_lds_dwordx4 %0, %1 offset:1024"
                 :: "v"(voff), "s"(sbase), "s"(lds) : "memory", "m0");
}
__device__ __forceinline__ void glds16_q(const void* sbase, unsigned voff, unsigned lds) {
    sbase = uni(sbase); lds = __builtin_amdgcn_readfirstlane(lds);
    unsigned sv; unsigned long long ex;
    asm volatile("s_mov_b32 %0, m0\n\ts_mov_b64 %1, exec\n\ts_mov_b32 m0, %4\n\ts_mov_b64 exec, 0xffff\n\ts_nop 1\n\tglobal_load_lds_dwordx4 %2, %3\n\ts_mov_b64 exec, %1\n\ts_mov_b32 m0, %0"
                 : "=&s"(sv), "=&s"(ex) : "v"(voff), "s"(sbase), "s"(lds) : "memory");
}
__device__ __forceinline__ void glds16_m(const void* sbase, unsigned voff, unsigned lds) {
    sbase = uni(sbase); lds = __builtin_amdgcn_readfirstlane(lds);
    unsigned sv; unsigned long long ex;
    asm volatile("s_mov_b32 %0, m0\n\ts_mov_b64 %1, exec\n\ts_mov_b32 m0, %4\n\ts_mov_b32 exec_lo, -1\n\ts_mov_b32 exec_hi, 0xff\n\ts_nop 1\n\tglobal_load_lds_dwordx4 %2, %3\n\ts_mov_b64 exec, %1\n\ts_mov_b32 m0, %0"
                 : "=&s"(sv), "=&s"(ex) : "v"(voff), "s"(sbase), "s"(lds) : "memory");
}
#define SCHED() __builtin_amdgcn_sched_barrier(0)
#define STAMP(kid, i) do {} while (0)
#define WSTAMP(i) do {} while (0)
__device__ __forceinline__ s16x4 tr_read(LAS char* p) {
    return __builtin_bit_cast(s16x4, __builtin_amdgcn_ds_read_tr16_b64_v4i16((LAS s16x4*)p));
}
__device__ __forceinline__ bf16x8 tr_frag(LAS char* p, int off_t1) {
    s16x4 lo = tr_read(p), hi = tr_read(p + off_t1);
    return __builtin_shufflevector(lo, hi, 0, 1, 2, 3, 4, 5, 6, 7);
}
__device__ __forceinline__ bf16x8 row_frag(LAS char* p) { return *(LAS bf16x8*)p; }
#define MFMA16(a, b, c) __builtin_amdgcn_mfma_f32_16x16x32_bf16((a), (b), (c), 0, 0, 0)
__device__ __forceinline__ void mfma_ip(f32x4& c, bf16x8 a, bf16x8 b) { asm volatile("v_mfma_f32_16x16x32_bf16 %0, %1, %2, %0" : "+v"(c) : "v"(a), "v"(b)); }
#define WAIT_V(n) asm volatile("s_waitcnt vmcnt(" #n ")" ::: "memory")
#define BAR() do { asm volatile("" ::: "memory"); __builtin_amdgcn_s_barrier(); asm volatile("" ::: "memory"); } while (0)

__device__ __forceinline__ int blk_off(int row, int chunk) { return row * 64 + ((chunk ^ (((row >> 3) & 1) << 1)) << 4); }

__global__ __launch_bounds__(256) void k0_ln(const u16* __restrict__ x, u16* __restrict__ xnb,
                                            const u16* __restrict__ wqkv, u16* __restrict__ wqh,
                                            const u16* __restrict__ Wg, const u16* __restrict__ Wu, u16* __restrict__ gx,
                                            const u16* __restrict__ Wd, const u16* __restrict__ wo, u16* __restrict__ dx,
                                            const char* __restrict__ wsb, const char* __restrict__ outb) {
    if (threadIdx.x >= 64) {
        const int l = threadIdx.x - 64, head = blockIdx.x >> 3, k0 = (blockIdx.x & 7) * 72;
        unsigned v[12];
#pragma unroll
        for (int i = 0; i < 12; ++i) {
            const int id = l + 192 * i, k = k0 + (id >> 5), cp = id & 31;
            const int part = cp >= 18 ? 2 : (cp >= 9 ? 1 : 0), d2 = cp - part * 9;
            const int src = (part == 2 ? 0 : part + 1) * DM + 18 * head + 2 * d2;
            v[i] = cp < 27 ? *(const unsigned*)(wqkv + (size_t)k * (3 * DM) + src) : 0u;
        }
        u32x4 c[3];
#pragma unroll
        for (int i = 0; i < 2; ++i) {
            const int id = l + 192 * i;
            if (id < 288) {
                const int gid = blockIdx.x * 288 + id, mat = gid / 36864, r = gid - mat * 36864, k = r >> 6, ch = r & 63;
                c[i] = *(const u32x4*)((mat ? Wu : Wg) + (size_t)k * FF + 4096 + 8 * ch);
            }
        }
        if (l < 162) {
            const int gid = blockIdx.x * 162 + l, k = gid >> 3, ch = gid & 7;
            c[2] = *(const u32x4*)((k < FF ? Wd + (size_t)k * DM : wo + (size_t)(k - FF) * DM) + 512 + 8 * ch);
        }
#pragma unroll
        for (int i = 0; i < 12; ++i) {
            const int id = l + 192 * i;
            *(unsigned*)(wqh + ((size_t)head * 576 + k0) * 64 + 2 * id) = v[i];
        }
#pragma unroll
        for (int i = 0; i < 2; ++i) {
            const int id = l + 192 * i;
            if (id < 288) {
                const int gid = blockIdx.x * 288 + id, mat = gid / 36864, r = gid - mat * 36864, k = r >> 6, ch = r & 63;
                *(u32x4*)(gx + (((size_t)mat * 32 + (ch >> 1)) * 576 + k) * 16 + 8 * (ch & 1)) = c[i];
            }
        }
        if (l < 162) {
            const int gid = blockIdx.x * 162 + l, k = gid >> 3, ch = gid & 7;
            *(u32x4*)(dx + ((size_t)(ch >> 1) * 5184 + k) * 16 + 8 * (ch & 1)) = c[2];
        }
        return;
    }
    const int lane = threadIdx.x, i = lane & 7, row = blockIdx.x * 8 + (lane >> 3);
    u32x4 xv[9];
#pragma unroll
    for (int cc = 0; cc < 9; ++cc) xv[cc] = *(const u32x4*)(x + (size_t)row * DM + (i + 8 * cc) * 8);
    float s = 0.f;
#pragma unroll
    for (int cc = 0; cc < 9; ++cc)
#pragma unroll
        for (int e = 0; e < 4; ++e) s += bf_lo(xv[cc][e]) + bf_hi(xv[cc][e]);
    s += __shfl_xor(s, 1); s += __shfl_xor(s, 2); s += __shfl_xor(s, 4);
    const float mean = s * (1.0f / 576.0f);
    float q = 0.f;
#pragma unroll
    for (int cc = 0; cc < 9; ++cc)
#pragma unroll
        for (int e = 0; e < 4; ++e) { const float a = bf_lo(xv[cc][e]) - mean, c2 = bf_hi(xv[cc][e]) - mean; q += a * a + c2 * c2; }
    q += __shfl_xor(q, 1); q += __shfl_xor(q, 2); q += __shfl_xor(q, 4);
    const float mb = rbf(mean), vb = rbf(q * (1.0f / 576.0f));
    const float sb = rbf(sqrtf(rbf(vb + 1e-5f)));
    const float rs = 1.0f / sb;
#pragma unroll
    for (int cc = 0; cc < 9; ++cc) {
        u32x4 o;
#pragma unroll
        for (int e = 0; e < 4; ++e) {
            const unsigned d = pk_bf16(bf_lo(xv[cc][e]) - mb, bf_hi(xv[cc][e]) - mb);
            o[e] = pk_bf16(bf_lo(d) * rs, bf_hi(d) * rs);
        }
        const int blk = 2 * cc + (i >> 2);
        *(u32x4*)((char*)xnb + (size_t)blk * (BLK_ELEMS * 2) + blk_off(row, i & 3)) = o;
    }
}

constexpr int RA = 0, RB = 65536, RE = 131072, RQ = 139264, RH = 155648;
constexpr int T_DUMMY = 159744;
constexpr int K1_LDS = T_DUMMY + 1024;
constexpr int QS = 144;
#define HST_AUX 16
#define PF 3
constexpr int NB = PF + 1;
template <int N> __device__ __forceinline__ void wait_vm() { asm volatile("s_waitcnt vmcnt(%0)" :: "i"(N) : "memory"); }
#define GLDS_MASKED(NAME, LO, HI) \
__device__ __forceinline__ void NAME(const void* sbase, unsigned voff, unsigned lds) { \
    sbase = uni(sbase); lds = __builtin_amdgcn_readfirstlane(lds); \
    unsigned sv; unsigned long long ex; \
    asm volatile("s_mov_b32 %0, m0\n\ts_mov_b64 %1, exec\n\ts_mov_b32 m0, %4\n\ts_mov_b32 exec_lo, " #LO "\n\ts_mov_b32 exec_hi, " #HI "\n\tglobal_load_lds_dwordx4 %2, %3\n\ts_mov_b64 exec, %1\n\ts_mov_b32 m0, %0" \
                 : "=&s"(sv), "=&s"(ex) : "v"(voff), "s"(sbase), "s"(lds) : "memory"); }
GLDS_MASKED(glds16_32, -1, 0)
GLDS_MASKED(glds16_8, 0xff, 0)

__device__ __forceinline__ void k1_epilogue(f32x4 (&acc)[11][4], f32x4& ach, LAS char* lds, unsigned lds0, int w, int xcd, int idx,
                                            int j, int mt, const u16* __restrict__ Wd, const u16* __restrict__ wo, u16* __restrict__ hb) {
    int tid = threadIdx.x; asm volatile("" : "+v"(tid));
    const int lane = tid & 63, fr = lane & 15, g = lane >> 4;
    const int wm = w >> 1, wn = w & 1;
    const __amdgpu_buffer_rsrc_t rh = __builtin_amdgcn_make_buffer_rsrc((void*)hb, 0, (int)(NKB_H * BLK_ELEMS * 2), 0x00020000);
    auto hs = [&](int m, int part) {
        const int row = 256 * mt + 64 * wm + 16 * m + fr;
        SCHED();
        if (part < 2) {
            const int pp = part;
            unsigned v[2][2];
#pragma unroll
            for (int hh = 0; hh < 2; ++hh) {
                const int gt = 2 * pp + hh;
                float hv[4];
#pragma unroll
                for (int e = 0; e < 4; ++e) hv[e] = fmaxf(acc[2 * gt][m][e], 0.f) * acc[2 * gt + 1][m][e];
                v[hh][0] = pk_bf16(hv[0], hv[1]); v[hh][1] = pk_bf16(hv[2], hv[3]);
            }
            auto r0 = __builtin_amdgcn_permlane16_swap(v[0][0], v[1][0], false, false);
            auto r1 = __builtin_amdgcn_permlane16_swap(v[0][1], v[1][1], false, false);
            u32x4 o; o[0] = r0[0]; o[1] = r1[0]; o[2] = r0[1]; o[3] = r1[1];
            const int blk = 4 * j + 2 * wn + pp, chunk = ((g & 1) << 1) | (g >> 1);
            __builtin_amdgcn_raw_buffer_store_b128(o, rh, (unsigned)(blk * (int)(BLK_ELEMS * 2) + blk_off(row, chunk)), 0, HST_AUX);
        } else {
            float hv[4];
#pragma unroll
            for (int e = 0; e < 4; ++e) {
                const unsigned mine = __float_as_uint(acc[8][m][e]);
                auto r = __builtin_amdgcn_permlane32_swap(mine, mine, false, false);
                hv[e] = fmaxf(__uint_as_float(r[0]), 0.f) * __uint_as_float(r[1]);
            }
            u32x2 v; v[0] = pk_bf16(hv[0], hv[1]); v[1] = pk_bf16(hv[2], hv[3]);
            const int blk = 128 + (j >> 1), chunk = (j & 1) * 2 + wn;
            const unsigned off = g < 2 ? (unsigned)(blk * (int)(BLK_ELEMS * 2) + blk_off(row, chunk) + g * 8) : 0xfffffff0u;
            __builtin_amdgcn_raw_buffer_store_b64(v, rh, off, 0, HST_AUX);
        }
        SCHED();
    };
    hs(0, 0);
    BAR();
    STAMP(1, 2);
    LAS char* qt = lds;
    {
#pragma unroll
        for (int u = 0; u < 2; ++u) {
            const int nn = 16 * (2 * wn + (u ^ (wm & 1))) + 4 * g;
            const int h0 = nn / 18, h1 = (nn + 2) / 18;
            const int o0 = h0 * 48 + (nn - h0 * 18) * 2, o1 = h1 * 48 + (nn + 2 - h1 * 18) * 2;
#pragma unroll
            for (int m = 0; m < 4; ++m) {
                LAS char* rp = qt + (16 + 64 * wm + 16 * m + fr) * QS;
                if (h0 < 3) *(LAS unsigned*)(rp + o0) = pk_bf16(acc[9 + u][m][0], acc[9 + u][m][1]);
                if (h1 < 3) *(LAS unsigned*)(rp + o1) = pk_bf16(acc[9 + u][m][2], acc[9 + u][m][3]);
            }
            if (u == 0 && (w == 0 || w == 2 || w == 1)) {
                LAS char* rp = qt + fr * QS;
                if (h0 < 3) *(LAS unsigned*)(rp + o0) = pk_bf16(ach[0], ach[1]);
                if (h1 < 3) *(LAS unsigned*)(rp + o1) = pk_bf16(ach[2], ach[3]);
            }
            if (u == 0) hs(0, 1);
        }
    }
    asm volatile("s_waitcnt lgkmcnt(0)" ::: "memory");
    BAR();
    STAMP(1, 3);
    {
        const int s = tid >> 1, half = tid & 1;
        const int lrow = 16 + s, grow = 256 * mt + s;
        auto ld18 = [&](LAS const char* p, unsigned (&v)[9]) {
            const u32x4 a = *(LAS const u32x4*)p, c = *(LAS const u32x4*)(p + 16);
            v[0] = a[0]; v[1] = a[1]; v[2] = a[2]; v[3] = a[3]; v[4] = c[0]; v[5] = c[1]; v[6] = c[2]; v[7] = c[3];
            v[8] = *(LAS const unsigned*)(p + 32);
        };
        hs(0, 2);
        unsigned qv[9];
        ld18(qt + lrow * QS + 96, qv);
        float lg[4];
#pragma unroll
        for (int ww = 0; ww < 4; ++ww) {
            const int wi = 4 * half + ww;
            unsigned kv[9];
            ld18(qt + (lrow - 2 * wi) * QS, kv);
            float a = 0.f;
#pragma unroll
            for (int i = 0; i < 9; ++i) a = __builtin_amdgcn_fdot2_f32_bf16(__builtin_bit_cast(bf16x2_t, qv[i]), __builtin_bit_cast(bf16x2_t, kv[i]), a, false);
            a = rbf(rbf(a) * 0.23570226039551584f);
            lg[ww] = (grow - 2 * wi >= 0) ? a : -1e9f;
            if (ww == 0) hs(1, 0);
            if (ww == 2) hs(1, 1);
        }
        float mx = fmaxf(fmaxf(lg[0], lg[1]), fmaxf(lg[2], lg[3]));
        mx = fmaxf(mx, __shfl_xor(mx, 1));
        float ex[4], sum = 0.f;
#pragma unroll
        for (int ww = 0; ww < 4; ++ww) { ex[ww] = __expf(lg[ww] - mx); sum += ex[ww]; }
        sum += __shfl_xor(sum, 1);
        const float inv = 1.0f / sum;
        hs(1, 2);
        float o[18];
#pragma unroll
        for (int i = 0; i < 18; ++i) o[i] = 0.f;
#pragma unroll
        for (int ww = 0; ww < 4; ++ww) {
            const int wi = 4 * half + ww;
            const float pw = (grow - 2 * wi >= 0) ? rbf(ex[ww] * inv) : 0.f;
            unsigned vv[9];
            ld18(qt + (lrow - 2 * wi) * QS + 48, vv);
#pragma unroll
            for (int i = 0; i < 9; ++i) { o[2 * i] += pw * bf_lo(vv[i]); o[2 * i + 1] += pw * bf_hi(vv[i]); }
            if (ww < 3) hs(2, ww);
        }
        hs(3, 0);
#pragma unroll
        for (int ii = 0; ii < 5; ++ii) {
            const float s0 = half ? o[2 * ii] : o[(10 + 2 * ii) % 18], s1 = half ? o[2 * ii + 1] : o[(11 + 2 * ii) % 18];
            const float r0 = __shfl_xor(s0, 1), r1 = __shfl_xor(s1, 1);
            const float m0 = half ? o[(10 + 2 * ii) % 18] : o[2 * ii], m1 = half ? o[(11 + 2 * ii) % 18] : o[2 * ii + 1];
            const int i = half * 5 + ii;
            if (i < 9) {
                const int col = FF + 18 * j + 2 * i;
                *(unsigned*)((char*)hb + (size_t)(col >> 5) * (BLK_ELEMS * 2) + blk_off(grow, (col >> 3) & 3) + (col & 7) * 2) = pk_bf16(m0 + r0, m1 + r1);
            }
            if (ii == 2) hs(3, 1);
        }
        hs(3, 2);
    }
    STAMP(1, 15);
}

__global__ __launch_bounds__(512, 2) void k1_gateup(const u16* __restrict__ xnb, const u16* __restrict__ Wg,
                                                    const u16* __restrict__ Wu, const u16* __restrict__ wqh, const u16* __restrict__ gx,
                                                    const u16* __restrict__ Wd, const u16* __restrict__ wo, u16* __restrict__ hb, unsigned* __restrict__ cnt) {
    extern __shared__ __attribute__((aligned(16))) char smem[];
    LAS char* lds = (LAS char*)smem;
    const int tid = threadIdx.x, lane = tid & 63;
    if (blockIdx.x < 64 && tid == 0) cnt[blockIdx.x * 64] = 0u;
    const int w = __builtin_amdgcn_readfirstlane(tid >> 6);
    const int b = blockIdx.x, xcd = b & 7, idx = b >> 3;
    const int j = 4 * xcd + (idx & 3), mt = idx >> 2;
    const int wm = w >> 1, wn = w & 1;
    const unsigned lds0 = (unsigned)(size_t)lds;
    const int fr = lane & 15, g = lane >> 4, q4 = (lane >> 2) & 3, p4 = lane & 3;

    f32x4 acc[11][4], ach;
    {
        const unsigned srcA = (unsigned)((256 * mt + 32 * w) * 32 + lane * 8) * 2u;
        const int koct = w >> 1, mat = w & 1;
        const u16* W = mat ? Wu : Wg;
        unsigned srcB, srcX;
        const u16* baseX; int strideX; unsigned dX;
        {
            const int r = lane >> 3, swz = ((r >> 1) & 1) | ((koct & 1) << 1), c = (lane & 7) ^ (swz << 1);
            srcB = (unsigned)((8 * koct + r) * FF + 128 * j + 8 * c) * 2u;
            if (w < 2) {
                const int ko = lane >> 4, re = ((lane >> 1) & 7) ^ ((ko & 1) << 2), ce = (lane & 1) ^ w;
                srcX = (unsigned)(((w * 32 + j) * 576 + 8 * ko + re) * 16 + 8 * ce) * 2u;
                baseX = gx; strideX = 32 * 16; dX = lds0 + RE + w * 1024;
            } else if (w < 6) {
                const int ko = w - 2, swq = ((r >> 1) & 1) | ((ko & 1) << 1), cq = (lane & 7) ^ (swq << 1);
                srcX = (unsigned)((j * 576 + 8 * ko + r) * 64 + 8 * cq) * 2u;
                baseX = wqh; strideX = 32 * 64; dX = lds0 + RQ + ko * 1024;
            } else {
                const int hr = mt > 0 ? 256 * mt - 16 : 0;
                srcX = (unsigned)(hr * 64 + lane * 16);
                baseX = xnb; strideX = (int)BLK_ELEMS; dX = lds0 + RH;
            }
        }
        const int slotX = w < 2 ? 2048 : (w < 6 ? 4096 : 1024);
        const unsigned dA = lds0 + RA + (2 * w) * 1024, dB = lds0 + RB + (2 * w) * 1024;
        auto issue1 = [&](int t, int sl, int i) {
            const u16* ba = xnb + (size_t)t * BLK_ELEMS;
            const u16* bw = W + (size_t)t * 32 * FF;
            if (i == 0) glds16(bw, srcB, dB + sl * 16384);
            if (i == 1) glds16(bw + 64, srcB, dB + sl * 16384 + 1024);
            if (i == 2) glds16(baseX + (size_t)t * strideX, srcX, dX + sl * slotX);
            if (i == 3) glds16(ba, srcA, dA + sl * 16384);
            if (i == 4) glds16_o1k(ba, srcA, dA + sl * 16384);
        };
        auto prefetch = [&](int p) {
            const unsigned start = (unsigned)((xcd == 0 ? 0 : 704 + 640 * (xcd - 1)) * DM * 2);
            const unsigned len = (unsigned)((xcd == 0 ? 704 : 640) * DM * 2), lwd = (unsigned)(FF * DM * 2);
            p = p < 4 ? p : 3;
            unsigned o = (unsigned)((idx * 8 + w) * 4096 + p * 1024); o = o + 1024 <= len ? o : 0u;
            o += start;
            const char* src = o >= lwd ? (const char*)wo + (o - lwd) : (const char*)Wd + o;
            glds16(src, (unsigned)lane * 16u, lds0 + T_DUMMY);
        };
        STAMP(1, 0);
#pragma unroll
        for (int t = 0; t < 3; ++t) {
#pragma unroll
            for (int i = 0; i < 5; ++i) issue1(t, t, i);
            if (t == 1) prefetch(0);
        }

        const unsigned pa = lds0 + RA + blk_off(64 * wm + fr, g);
        const int swz = ((q4 >> 1) & 1) | ((g & 1) << 1);
        const unsigned pb0 = lds0 + RB + (g * 4 + wn) * 1024 + q4 * 128 + (swz << 5) + p4 * 8;
        const int emat = p4 >> 1;
        const unsigned pe0 = lds0 + RE + (emat * 4 + g) * 256 + ((q4 ^ ((g & 1) << 2)) * 32) + ((wn ^ emat) * 16) + (p4 & 1) * 8;
        const unsigned pq0 = lds0 + RQ + g * 1024 + q4 * 128 + (((2 * wn + (wm & 1)) ^ swz) << 5) + p4 * 8;
        LAS char* pbv[4]; LAS char* pqv[2];
#pragma unroll
        for (int u = 0; u < 4; ++u) pbv[u] = (LAS char*)(size_t)(pb0 ^ (u << 5));
        pqv[0] = (LAS char*)(size_t)pq0; pqv[1] = (LAS char*)(size_t)(pq0 ^ 32);
        LAS char* pev[2] = {(LAS char*)(size_t)pe0, (LAS char*)(size_t)(pe0 ^ 128)};
        LAS char* phv = lds + RH + blk_off(fr, g);
        auto rdB = [&](int sl, int nf, int t1) -> s16x4 {
            if (nf < 8) return tr_read(pbv[nf >> 1] + sl * 16384 + (nf & 1) * 2048 + t1 * 512);
            if (nf == 8) return tr_read(pev[t1] + sl * 2048);
            return tr_read(pqv[nf - 9] + sl * 4096 + t1 * 512);
        };
        auto rdA = [&](int sl, int m) -> bf16x8 { return row_frag((LAS char*)(size_t)pa + sl * 16384 + m * 1024); };
        auto rdH = [&](int sl) -> bf16x8 { return row_frag(phv + sl * 1024); };
#pragma unroll
        for (int n = 0; n < 11; ++n)
#pragma unroll
            for (int m = 0; m < 4; ++m) { acc[n][m] = (f32x4){0.f, 0.f, 0.f, 0.f}; asm volatile("" : "+v"(acc[n][m])); }
        ach = (f32x4){0.f, 0.f, 0.f, 0.f}; asm volatile("" : "+v"(ach));

        wait_vm<11>(); BAR();
        STAMP(1, 1);
        bf16x8 fa[2][2], fc[2], fh, fb[NB];
#pragma unroll
        for (int m = 0; m < 2; ++m) { fa[0][m] = rdA(0, m); fc[m] = rdA(0, 2 + m); }
#pragma unroll
        for (int i = 0; i < PF; ++i) { s16x4 lo = rdB(0, i, 0), hi = rdB(0, i, 1); fb[i] = __builtin_shufflevector(lo, hi, 0, 1, 2, 3, 4, 5, 6, 7); }
#define MM(C, A, B) mfma_ip(C, A, B)
        auto step = [&](auto S_, int t, bool do_issue, auto LAST1_, auto WAIT0_) {
            constexpr int S = decltype(S_)::value;
            constexpr bool has_next = !decltype(LAST1_)::value;
            constexpr bool wait0 = decltype(WAIT0_)::value;
            constexpr int sl = S, sn = (S + 1) & 3, cur = S & 1;
#pragma unroll
            for (int nf = 0; nf < 11; ++nf) {
                constexpr int dummy = 0; (void)dummy;
                const int r = 11 * S + nf;
                const int psl = (nf + PF < 11) ? sl : sn, nn = (nf + PF < 11) ? nf + PF : nf + PF - 11;
                const bool pf = (nf + PF < 11) || has_next;
                const bool lastg = (nf == 10);
                s16x4 lo, hi;
                if (!lastg) MM(acc[nf][0], fb[r % NB], fa[cur][0]); else MM(acc[nf][2], fb[r % NB], fc[0]);
                SCHED();
                if (nf == 0) {
                    if (!wait0) wait_vm<6>(); else wait_vm<0>();
                    BAR();
                }
                if (pf) lo = rdB(psl, nn, 0);
                SCHED();
                if (!lastg) MM(acc[nf][1], fb[r % NB], fa[cur][1]); else MM(acc[nf][3], fb[r % NB], fc[1]);
                SCHED();
                if (pf) hi = rdB(psl, nn, 1);
                if (lastg && has_next) fc[0] = rdA(sn, 2);
                SCHED();
                if (!lastg) MM(acc[nf][2], fb[r % NB], fc[0]); else MM(acc[nf][0], fb[r % NB], fa[cur][0]);
                SCHED();
                if (lastg && has_next) fc[1] = rdA(sn, 3);
                if (nf >= 4 && nf < 6 && has_next) fa[cur ^ 1][nf - 4] = rdA(sn, nf - 4);
                if (nf == 7) fh = rdH(sl);
                if ((nf < 4 || nf == 6) && do_issue) issue1(t + 3, (S + 3) & 3, nf < 4 ? nf : 4);
                if (nf == 8 && (S & 1) == 0 && do_issue) prefetch(1 + (t >> 1));
                SCHED();
                if (!lastg) MM(acc[nf][3], fb[r % NB], fc[1]); else MM(acc[nf][1], fb[r % NB], fa[cur][1]);
                SCHED();
                if (nf == 9) { MM(ach, fb[r % NB], fh); SCHED(); }
                if (pf) fb[(r + PF) % NB] = __builtin_shufflevector(lo, hi, 0, 1, 2, 3, 4, 5, 6, 7);
            }
        };
        using I0 = std::integral_constant<int, 0>; using I1 = std::integral_constant<int, 1>;
        using I2 = std::integral_constant<int, 2>; using I3 = std::integral_constant<int, 3>;
        using F = std::false_type; using T = std::true_type;
#pragma unroll 1
        for (int t0 = 0; t0 < 16; t0 += 4) {
            step(I0{}, t0, true, F{}, F{});
            step(I1{}, t0 + 1, true, F{}, F{});
            step(I2{}, t0 + 2, true, F{}, F{});
            step(I3{}, t0 + 3, t0 + 3 < 15, F{}, F{});
        }
        step(I0{}, 16, false, F{}, T{});
        step(I1{}, 17, false, T{}, T{});
    }
    asm volatile("s_nop 7\n\ts_nop 7" ::: "memory");
    STAMP(1, 4);
    k1_epilogue(acc, ach, lds, lds0, w, xcd, idx, j, mt, Wd, wo, hb);
}

constexpr int K2_RB = 0, K2_RE = 65536, K2_LDS = 8 * 18 * 1024;
constexpr int NSPLIT = 8;
constexpr int PART_SLOT = 18 * 512;
__device__ __forceinline__ void gload4(bf16x8 (&d)[4], const void* sbase, unsigned voff) {
    sbase = uni(sbase);
    asm volatile("s_nop 4\n\tglobal_load_dwordx4 %0, %4, %5\n\tglobal_load_dwordx4 %1, %4, %5 offset:1024\n\t"
                 "global_load_dwordx4 %2, %4, %5 offset:2048\n\tglobal_load_dwordx4 %3, %4, %5 offset:3072"
                 : "=&v"(d[0]), "=&v"(d[1]), "=&v"(d[2]), "=&v"(d[3]) : "v"(voff), "s"(sbase) : "memory");
}
__device__ __forceinline__ f32x4 selu(unsigned long long mask, f32x4 a, f32x4 b) {
    f32x4 r;
#pragma unroll
    for (int e = 0; e < 4; ++e) { float t; asm("v_cndmask_b32 %0, %1, %2, %3" : "=v"(t) : "v"(a[e]), "v"(b[e]), "s"(mask)); r[e] = t; }
    return r;
}
__global__ __launch_bounds__(512, 2) void k2_down(const u16* __restrict__ hb, const u16* __restrict__ Wd,
                                                  const u16* __restrict__ wo, const u16* __restrict__ dx, u16* __restrict__ part, unsigned* __restrict__ cnt,
                                                  const u16* __restrict__ x, u16* __restrict__ out) {
    extern __shared__ __attribute__((aligned(16))) char smem[];
    LAS char* lds = (LAS char*)smem;
    const int tid = threadIdx.x, lane = tid & 63;
    const int w = __builtin_amdgcn_readfirstlane(tid >> 6);
    const int b = blockIdx.x, split = b & 7, idx = b >> 3;
    const int mt = idx >> 2, nt = idx & 3;
    const int wm = w >> 1, kh = w & 1;
    const int T0 = split == 0 ? 0 : 11 + 10 * (split - 1);
    const int NT = split == 0 ? 11 : 10;

    const unsigned srcA = (unsigned)(kh * (int)BLK_ELEMS * 2 + blk_off(256 * mt + 64 * wm + (lane & 15), lane >> 4));
    const int koct = w >> 1, sub = w & 1;
    unsigned offB, offE;
    {
        const int r = lane >> 3, swz = ((r >> 1) & 1) | ((koct & 1) << 1), c = (lane & 7) ^ (swz << 1);
        offB = (unsigned)((32 * sub + 8 * koct + r) * DM + 128 * nt + 8 * c) * 2u;
        const int re = ((lane >> 1) & 7) ^ ((koct & 1) << 2), ce = (lane & 1);
        offE = (unsigned)((nt * 5184 + 32 * sub + 8 * koct + re) * 16 + 8 * ce) * 2u;
    }
    const unsigned lds0 = (unsigned)(size_t)lds;
    const unsigned dB = lds0 + K2_RB + (2 * w) * 1024, dE = lds0 + K2_RE + (sub * 4 + koct) * 256;
    bf16x8 fa[4][4];
    auto issue1 = [&](int t, int sl, int i, bf16x8 (&a)[4]) {
        const int T = T0 + t;
        const u16* Wt = T < 72 ? Wd + (size_t)T * 64 * DM : wo + (size_t)(T - 72) * 64 * DM;
        const u16* ba = hb + (size_t)T * 2 * BLK_ELEMS;
        if (i == 0) glds16(Wt, offB, dB + sl * 16384);
        if (i == 1) glds16(Wt + 64, offB, dB + sl * 16384 + 1024);
        if (i == 2) glds16_q(dx + (size_t)T * 64 * 16, offE, dE + sl * 2048);
        if (i == 3) gload4(a, ba, srcA);
    };
    STAMP(2, 0);
#pragma unroll
    for (int t = 0; t < 3; ++t)
#pragma unroll
        for (int i = 0; i < 4; ++i) issue1(t, t, i, fa[t]);

    const int fr = lane & 15, g = lane >> 4, q4 = (lane >> 2) & 3, p4 = lane & 3;
    const int swz = ((q4 >> 1) & 1) | ((g & 1) << 1);
    LAS char* pb[4];
#pragma unroll
    for (int u = 0; u < 4; ++u) pb[u] = lds + K2_RB + (g * 4 + kh * 2) * 1024 + q4 * 128 + ((u ^ swz) << 5) + p4 * 8;
    LAS char* pe0 = lds + K2_RE + (kh * 4 + g) * 256 + ((q4 ^ ((g & 1) << 2)) * 32) + p4 * 8;
    LAS char* pe1 = lds + K2_RE + (kh * 4 + g) * 256 + (((4 + q4) ^ ((g & 1) << 2)) * 32) + p4 * 8;
    auto rdB = [&](int sl, int nf, int t1) -> s16x4 {
        if (nf < 8) return tr_read(pb[nf & 3] + sl * 16384 + (nf >> 2) * 1024 + t1 * 512);
        return tr_read((t1 ? pe1 : pe0) + sl * 2048);
    };

    f32x4 acc[9][4];
#pragma unroll
    for (int n = 0; n < 9; ++n)
#pragma unroll
        for (int m = 0; m < 4; ++m) { acc[n][m] = (f32x4){0.f, 0.f, 0.f, 0.f}; asm volatile("" : "+v"(acc[n][m])); }

    wait_vm<14>(); BAR();
    STAMP(2, 1);
    constexpr int PF2 = 3, NB2 = 4;
    bf16x8 fb[NB2];
#pragma unroll
    for (int i = 0; i < PF2; ++i) { s16x4 lo = rdB(0, i, 0), hi = rdB(0, i, 1); fb[i] = __builtin_shufflevector(lo, hi, 0, 1, 2, 3, 4, 5, 6, 7); }
    auto step = [&](auto S_, int t) {
        constexpr int S = decltype(S_)::value;
        constexpr int sl = S, sn = (S + 1) & 3;
        const bool do_issue = t + 3 < NT, wait0 = t + 2 >= NT;
        asm volatile("" : "+v"(fa[sl][0]), "+v"(fa[sl][1]), "+v"(fa[sl][2]), "+v"(fa[sl][3]));
#pragma unroll
        for (int nf = 0; nf < 9; ++nf) {
            const int r = 9 * S + nf;
            const int psl = (nf + PF2 < 9) ? sl : sn, nn = (nf + PF2 < 9) ? nf + PF2 : nf + PF2 - 9;
            s16x4 lo, hi;
            mfma_ip(acc[nf][0], fb[r % NB2], fa[sl][0]);
            SCHED();
            if (nf == 0) {
                if (wait0) wait_vm<0>(); else wait_vm<7>();
                BAR();
            }
            lo = rdB(psl, nn, 0);
            SCHED();
            mfma_ip(acc[nf][1], fb[r % NB2], fa[sl][1]);
            SCHED();
            hi = rdB(psl, nn, 1);
            SCHED();
            mfma_ip(acc[nf][2], fb[r % NB2], fa[sl][2]);
            SCHED();
            if (nf >= 1 && nf < 5 && do_issue) issue1(t + 3, (S + 3) & 3, nf - 1, fa[(S + 3) & 3]);
            SCHED();
            mfma_ip(acc[nf][3], fb[r % NB2], fa[sl][3]);
            SCHED();
            fb[(r + PF2) % NB2] = __builtin_shufflevector(lo, hi, 0, 1, 2, 3, 4, 5, 6, 7);
        }
    };
    using I0 = std::integral_constant<int, 0>; using I1 = std::integral_constant<int, 1>;
    using I2 = std::integral_constant<int, 2>; using I3 = std::integral_constant<int, 3>;
#pragma unroll 1
    for (int t0 = 0; t0 < 12; t0 += 4) {
        step(I0{}, t0);
        step(I1{}, t0 + 1);
        if (t0 + 2 < NT) step(I2{}, t0 + 2);
        if (t0 + 3 < NT) step(I3{}, t0 + 3);
    }
    asm volatile("s_nop 7\n\ts_nop 7" ::: "memory");
    BAR();
    STAMP(2, 2);
    f32x4 val[18];
    {
        LAS char* xw = lds + w * 18432;
        LAS char* xr = lds + (w ^ 1) * 18432;
        if (kh) {
#pragma unroll
            for (int n = 0; n < 9; ++n)
#pragma unroll
                for (int mm = 0; mm < 2; ++mm) { const f32x4 t = acc[n][mm]; acc[n][mm] = acc[n][2 + mm]; acc[n][2 + mm] = t; }
        }
#pragma unroll
        for (int n = 0; n < 9; ++n)
#pragma unroll
            for (int mm = 0; mm < 2; ++mm) *(LAS f32x4*)(xw + (n * 2 + mm) * 1024 + lane * 16) = acc[n][2 + mm];
        asm volatile("s_waitcnt lgkmcnt(0)" ::: "memory");
        BAR();
#pragma unroll
        for (int i = 0; i < 18; ++i) val[i] = acc[i >> 1][i & 1] + *(LAS f32x4*)(xr + i * 1024 + lane * 16);
    }
    {
        const int grp = mt * 4 + nt;
        const __amdgpu_buffer_rsrc_t rp = __builtin_amdgcn_make_buffer_rsrc((void*)part, 0, NSPLIT * 32 * 8 * PART_SLOT, 0x00020000);
        const unsigned slot = (unsigned)((grp * 8 + w) * PART_SLOT + lane * 8);
        constexpr unsigned SPL = 32 * 8 * PART_SLOT;
        u32x2 xv[3]; unsigned xoff[3];
#pragma unroll
        for (int q = 0; q < 3; ++q) {
            int i = split + 8 * q; i = i < 18 ? i : split;
            const int n = i >> 1, m = 2 * kh + (i & 1);
            const int row = 256 * mt + 64 * wm + 16 * m + fr;
            const int col = (n < 8 ? 128 * nt + 16 * n : 512 + 16 * nt) + 4 * g;
            xoff[q] = (unsigned)(row * DM + col);
            xv[q] = *(const u32x2*)(x + xoff[q]);
        }
#pragma unroll
        for (int i = 0; i < 18; ++i) {
            if ((i & 7) != split) {
                u32x2 v; v[0] = pk_bf16(val[i][0], val[i][1]); v[1] = pk_bf16(val[i][2], val[i][3]);
                __builtin_amdgcn_raw_buffer_store_b64(v, rp, slot + split * SPL + i * 512, 0, 16);
            }
        }
        STAMP(2, 3);
        asm volatile("s_waitcnt vmcnt(0)" ::: "memory");
        BAR();
        STAMP(2, 4);
        unsigned* c = cnt + grp * 64;
        if (tid == 0) {
            __hip_atomic_fetch_add(c, 1u, __ATOMIC_RELAXED, __HIP_MEMORY_SCOPE_AGENT);
            unsigned spins = 0;
            while (__hip_atomic_load(c, __ATOMIC_RELAXED, __HIP_MEMORY_SCOPE_AGENT) < (unsigned)NSPLIT) {
                __builtin_amdgcn_s_sleep(1);
                if (++spins > (1u << 22)) break;
            }
        }
        BAR();
        STAMP(2, 5);
        u32x2 pv[3][7];
#pragma unroll
        for (int q = 0; q < 3; ++q) {
            const int i = split + 8 * q;
#pragma unroll
            for (int s2 = 1; s2 < NSPLIT; ++s2) {
                const int so = (split + s2) & 7;
                const unsigned off = i < 18 ? slot + so * SPL + i * 512 : 0xfffffff0u;
                pv[q][s2 - 1] = __builtin_amdgcn_raw_buffer_load_b64(rp, off, 0, 16);
            }
        }
#pragma unroll
        for (int q = 0; q < 3; ++q) {
            const int i = split + 8 * q;
            if (i < 18) {
                const int b8 = 8 * q;
                auto at = [&](int k) -> f32x4 { return val[(b8 + k) < 18 ? b8 + k : 17]; };
                const unsigned long long k1 = (split & 1) ? ~0ull : 0ull, k2 = (split & 2) ? ~0ull : 0ull, k4 = (split & 4) ? ~0ull : 0ull;
                f32x4 o = selu(k4, selu(k2, selu(k1, at(0), at(1)), selu(k1, at(2), at(3))), selu(k2, selu(k1, at(4), at(5)), selu(k1, at(6), at(7))));
#pragma unroll
                for (int s2 = 0; s2 < 7; ++s2) { o[0] += bf_lo(pv[q][s2][0]); o[1] += bf_hi(pv[q][s2][0]); o[2] += bf_lo(pv[q][s2][1]); o[3] += bf_hi(pv[q][s2][1]); }
                u32x2 v; v[0] = pk_bf16(o[0] + bf_lo(xv[q][0]), o[1] + bf_hi(xv[q][0])); v[1] = pk_bf16(o[2] + bf_lo(xv[q][1]), o[3] + bf_hi(xv[q][1]));
                *(u32x2*)(out + xoff[q]) = v;
            }
        }
    }
    STAMP(2, 15);
}

extern "C" void kernel_launch(void* const* d_in, const int* in_sizes, int n_in, void* d_out, int out_size, void* d_ws,
                              size_t ws_size, hipStream_t stream) {
    const u16* x = (const u16*)d_in[0];
    const u16* wqkv = (const u16*)d_in[1];
    const u16* wo = (const u16*)d_in[2];
    const u16* Wg = (const u16*)d_in[4];
    const u16* Wu = (const u16*)d_in[5];
    const u16* Wd = (const u16*)d_in[6];
    char* ws = (char*)d_ws;
    u16* xnb = (u16*)ws;
    u16* hb = (u16*)(ws + WS_HB);
    u16* part = (u16*)(ws + WS_PART);
    u16* wqh = (u16*)(ws + WS_WQH);
    u16* gx = (u16*)(ws + WS_GX);
    u16* dx = (u16*)(ws + WS_DX);
    unsigned* cnt = (unsigned*)(ws + WS_CNT);
    (void)hipFuncSetAttribute((const void*)k1_gateup, hipFuncAttributeMaxDynamicSharedMemorySize, K1_LDS);
    (void)hipFuncSetAttribute((const void*)k2_down, hipFuncAttributeMaxDynamicSharedMemorySize, K2_LDS);
    k0_ln<<<256, 256, 0, stream>>>(x, xnb, wqkv, wqh, Wg, Wu, gx, Wd, wo, dx, (const char*)ws, (const char*)d_out);
    k1_gateup<<<256, 512, K1_LDS, stream>>>(xnb, Wg, Wu, wqh, gx, Wd, wo, hb, cnt);
    k2_down<<<256, 512, K2_LDS, stream>>>(hb, Wd, wo, dx, part, cnt, x, (u16*)d_out);
}
```

```cpp
#define NO_WPF
#define HST_AUX 17
#include <hip/hip_runtime.h>
#include <hip/hip_bf16.h>
#include <stdint.h>
#include <type_traits>

typedef unsigned short u16;
typedef short bf16x8 __attribute__((ext_vector_type(8)));
typedef short s16x4 __attribute__((ext_vector_type(4)));
typedef float f32x4 __attribute__((ext_vector_type(4)));
typedef float f32x2 __attribute__((ext_vector_type(2)));
typedef __bf16 bf16x2_t __attribute__((ext_vector_type(2)));
typedef unsigned u32x4 __attribute__((ext_vector_type(4)));
typedef unsigned u32x2 __attribute__((ext_vector_type(2)));
#define LAS __attribute__((address_space(3)))
#define GAS __attribute__((address_space(1)))

constexpr int SEQ = 2048, DM = 576, FF = 4608;
constexpr int NKB_X = 18;
constexpr int NKB_H = 162;
constexpr size_t BLK_ELEMS = (size_t)SEQ * 32;

constexpr size_t WS_HB = 18 * 131072, WS_PART = WS_HB + 162 * 131072, WS_WQH = WS_PART + 4 * 64 * 8 * 10 * 512, WS_GX = WS_WQH + 32 * 576 * 64 * 2,
                 WS_DX = WS_GX + 2 * 32 * 576 * 16 * 2, WS_CNT = WS_DX + 4 * 5184 * 16 * 2, WS_END = WS_CNT + 64 * 256;

__device__ __forceinline__ unsigned pk_bf16(float lo, float hi) {
    f32x2 v = {lo, hi};
    bf16x2_t b = __builtin_convertvector(v, bf16x2_t);
    return __builtin_bit_cast(unsigned, b);
}
__device__ __forceinline__ float bf_lo(unsigned u) { return __uint_as_float(u << 16); }
__device__ __forceinline__ float bf_hi(unsigned u) { return __uint_as_float(u & 0xffff0000u); }
__device__ __forceinline__ float rbf(float x) { return bf_lo(pk_bf16(x, x)); }

__device__ __forceinline__ const void* uni(const void* p) {
    const unsigned long long v = (unsigned long long)p;
    const unsigned lo = __builtin_amdgcn_readfirstlane((unsigned)v), hi = __builtin_amdgcn_readfirstlane((unsigned)(v >> 32));
    return (const void*)(((unsigned long long)hi << 32) | lo);
}
__device__ __forceinline__ void glds16(const void* sbase, unsigned voff, unsigned lds) {
    sbase = uni(sbase); lds = __builtin_amdgcn_readfirstlane(lds);
    asm volatile("s_mov_b32 m0, %2\n\ts_nop 3\n\tglobal_load_lds_dwordx4 %0, %1"
                 :: "v"(voff), "s"(sbase), "s"(lds) : "memory", "m0");
}
__device__ __forceinline__ void glds16_o1k(const void* sbase, unsigned voff, unsigned lds) {
    sbase = uni(sbase); lds = __builtin_amdgcn_readfirstlane(lds);
    asm volatile("s_mov_b32 m0, %2\n\ts_nop 3\n\tglobal_load_lds_dwordx4 %0, %1 offset:1024"
                 :: "v"(voff), "s"(sbase), "s"(lds) : "memory", "m0");
}
__device__ __forceinline__ void glds16_q(const void* sbase, unsigned voff, unsigned lds) {
    sbase = uni(sbase); lds = __builtin_amdgcn_readfirstlane(lds);
    unsigned sv; unsigned long long ex;
    asm volatile("s_mov_b32 %0, m0\n\ts_mov_b64 %1, exec\n\ts_mov_b32 m0, %4\n\ts_mov_b64 exec, 0xffff\n\ts_nop 1\n\tglobal_load_lds_dwordx4 %2, %3\n\ts_mov_b64 exec, %1\n\ts_mov_b32 m0, %0"
                 : "=&s"(sv), "=&s"(ex) : "v"(voff), "s"(sbase), "s"(lds) : "memory");
}
__device__ __forceinline__ void glds16_m(const void* sbase, unsigned voff, unsigned lds) {
    sbase = uni(sbase); lds = __builtin_amdgcn_readfirstlane(lds);
    unsigned sv; unsigned long long ex;
    asm volatile("s_mov_b32 %0, m0\n\ts_mov_b64 %1, exec\n\ts_mov_b32 m0, %4\n\ts_mov_b32 exec_lo, -1\n\ts_mov_b32 exec_hi, 0xff\n\ts_nop 1\n\tglobal_load_lds_dwordx4 %2, %3\n\ts_mov_b64 exec, %1\n\ts_mov_b32 m0, %0"
                 : "=&s"(sv), "=&s"(ex) : "v"(voff), "s"(sbase), "s"(lds) : "memory");
}
#define SCHED() __builtin_amdgcn_sched_barrier(0)
#define STAMP(kid, i) do {} while (0)
#define WSTAMP(i) do {} while (0)
__device__ __forceinline__ s16x4 tr_read(LAS char* p) {
    return __builtin_bit_cast(s16x4, __builtin_amdgcn_ds_read_tr16_b64_v4i16((LAS s16x4*)p));
}
__device__ __forceinline__ bf16x8 tr_frag(LAS char* p, int off_t1) {
    s16x4 lo = tr_read(p), hi = tr_read(p + off_t1);
    return __builtin_shufflevector(lo, hi, 0, 1, 2, 3, 4, 5, 6, 7);
}
__device__ __forceinline__ bf16x8 row_frag(LAS char* p) { return *(LAS bf16x8*)p; }
#define MFMA16(a, b, c) __builtin_amdgcn_mfma_f32_16x16x32_bf16((a), (b), (c), 0, 0, 0)
__device__ __forceinline__ void mfma_ip(f32x4& c, bf16x8 a, bf16x8 b) { asm volatile("v_mfma_f32_16x16x32_bf16 %0, %1, %2, %0" : "+v"(c) : "v"(a), "v"(b)); }
#define WAIT_V(n) asm volatile("s_waitcnt vmcnt(" #n ")" ::: "memory")
#define BAR() do { asm volatile("" ::: "memory"); __builtin_amdgcn_s_barrier(); asm volatile("" ::: "memory"); } while (0)

__device__ __forceinline__ int blk_off(int row, int chunk) { return row * 64 + ((chunk ^ (((row >> 3) & 1) << 1)) << 4); }

__global__ __launch_bounds__(256) void k0_ln(const u16* __restrict__ x, u16* __restrict__ xnb,
                                            const u16* __restrict__ wqkv, u16* __restrict__ wqh,
                                            const u16* __restrict__ Wg, const u16* __restrict__ Wu, u16* __restrict__ gx,
                                            const u16* __restrict__ Wd, const u16* __restrict__ wo, u16* __restrict__ dx,
                                            const char* __restrict__ wsb, const char* __restrict__ outb) {
    if (threadIdx.x >= 64) {
        const int l = threadIdx.x - 64, head = blockIdx.x >> 3, k0 = (blockIdx.x & 7) * 72;
        unsigned v[12];
#pragma unroll
        for (int i = 0; i < 12; ++i) {
            const int id = l + 192 * i, k = k0 + (id >> 5), cp = id & 31;
            const int part = cp >= 18 ? 2 : (cp >= 9 ? 1 : 0), d2 = cp - part * 9;
            const int src = (part == 2 ? 0 : part + 1) * DM + 18 * head + 2 * d2;
            v[i] = cp < 27 ? *(const unsigned*)(wqkv + (size_t)k * (3 * DM) + src) : 0u;
        }
        u32x4 c[3];
#pragma unroll
        for (int i = 0; i < 2; ++i) {
            const int id = l + 192 * i;
            if (id < 288) {
                const int gid = blockIdx.x * 288 + id, mat = gid / 36864, r = gid - mat * 36864, k = r >> 6, ch = r & 63;
                c[i] = *(const u32x4*)((mat ? Wu : Wg) + (size_t)k * FF + 4096 + 8 * ch);
            }
        }
        if (l < 162) {
            const int gid = blockIdx.x * 162 + l, k = gid >> 3, ch = gid & 7;
            c[2] = *(const u32x4*)((k < FF ? Wd + (size_t)k * DM : wo + (size_t)(k - FF) * DM) + 512 + 8 * ch);
        }
#pragma unroll
        for (int i = 0; i < 12; ++i) {
            const int id = l + 192 * i;
            *(unsigned*)(wqh + ((size_t)head * 576 + k0) * 64 + 2 * id) = v[i];
        }
#pragma unroll
        for (int i = 0; i < 2; ++i) {
            const int id = l + 192 * i;
            if (id < 288) {
                const int gid = blockIdx.x * 288 + id, mat = gid / 36864, r = gid - mat * 36864, k = r >> 6, ch = r & 63;
                *(u32x4*)(gx + (((size_t)mat * 32 + (ch >> 1)) * 576 + k) * 16 + 8 * (ch & 1)) = c[i];
            }
        }
        if (l < 162) {
            const int gid = blockIdx.x * 162 + l, k = gid >> 3, ch = gid & 7;
            *(u32x4*)(dx + ((size_t)(ch >> 1) * 5184 + k) * 16 + 8 * (ch & 1)) = c[2];
        }
        return;
    }
    const int lane = threadIdx.x, i = lane & 7, row = blockIdx.x * 8 + (lane >> 3);
    u32x4 xv[9];
#pragma unroll
    for (int cc = 0; cc < 9; ++cc) xv[cc] = *(const u32x4*)(x + (size_t)row * DM + (i + 8 * cc) * 8);
    float s = 0.f;
#pragma unroll
    for (int cc = 0; cc < 9; ++cc)
#pragma unroll
        for (int e = 0; e < 4; ++e) s += bf_lo(xv[cc][e]) + bf_hi(xv[cc][e]);
    s += __shfl_xor(s, 1); s += __shfl_xor(s, 2); s += __shfl_xor(s, 4);
    const float mean = s * (1.0f / 576.0f);
    float q = 0.f;
#pragma unroll
    for (int cc = 0; cc < 9; ++cc)
#pragma unroll
        for (int e = 0; e < 4; ++e) { const float a = bf_lo(xv[cc][e]) - mean, c2 = bf_hi(xv[cc][e]) - mean; q += a * a + c2 * c2; }
    q += __shfl_xor(q, 1); q += __shfl_xor(q, 2); q += __shfl_xor(q, 4);
    const float mb = rbf(mean), vb = rbf(q * (1.0f / 576.0f));
    const float sb = rbf(sqrtf(rbf(vb + 1e-5f)));
    const float rs = 1.0f / sb;
#pragma unroll
    for (int cc = 0; cc < 9; ++cc) {
        u32x4 o;
#pragma unroll
        for (int e = 0; e < 4; ++e) {
            const unsigned d = pk_bf16(bf_lo(xv[cc][e]) - mb, bf_hi(xv[cc][e]) - mb);
            o[e] = pk_bf16(bf_lo(d) * rs, bf_hi(d) * rs);
        }
        const int blk = 2 * cc + (i >> 2);
        *(u32x4*)((char*)xnb + (size_t)blk * (BLK_ELEMS * 2) + blk_off(row, i & 3)) = o;
    }
}

constexpr int RA = 0, RB = 65536, RE = 131072, RQ = 139264, RH = 155648;
constexpr int T_DUMMY = 159744;
constexpr int K1_LDS = T_DUMMY + 1024;
constexpr int QS = 144;
#define PF 3
constexpr int NB = PF + 1;
template <int N> __device__ __forceinline__ void wait_vm() { asm volatile("s_waitcnt vmcnt(%0)" :: "i"(N) : "memory"); }
#define GLDS_MASKED(NAME, LO, HI) \
__device__ __forceinline__ void NAME(const void* sbase, unsigned voff, unsigned lds) { \
    sbase = uni(sbase); lds = __builtin_amdgcn_readfirstlane(lds); \
    unsigned sv; unsigned long long ex; \
    asm volatile("s_mov_b32 %0, m0\n\ts_mov_b64 %1, exec\n\ts_mov_b32 m0, %4\n\ts_mov_b32 exec_lo, " #LO "\n\ts_mov_b32 exec_hi, " #HI "\n\tglobal_load_lds_dwordx4 %2, %3\n\ts_mov_b64 exec, %1\n\ts_mov_b32 m0, %0" \
                 : "=&s"(sv), "=&s"(ex) : "v"(voff), "s"(sbase), "s"(lds) : "memory"); }
GLDS_MASKED(glds16_32, -1, 0)
GLDS_MASKED(glds16_8, 0xff, 0)

__device__ __forceinline__ void k1_epilogue(f32x4 (&acc)[11][4], f32x4& ach, LAS char* lds, unsigned lds0, int w, int xcd, int idx,
                                            int j, int mt, const u16* __restrict__ Wd, const u16* __restrict__ wo, u16* __restrict__ hb) {
    int tid = threadIdx.x; asm volatile("" : "+v"(tid));
    const int lane = tid & 63, fr = lane & 15, g = lane >> 4;
    const int wm = w >> 1, wn = w & 1;
    const __amdgpu_buffer_rsrc_t rh = __builtin_amdgcn_make_buffer_rsrc((void*)hb, 0, (int)(NKB_H * BLK_ELEMS * 2), 0x00020000);
    auto hs = [&](int m, int part) {
        const int row = 256 * mt + 64 * wm + 16 * m + fr;
        SCHED();
        if (part < 2) {
            const int pp = part;
            unsigned v[2][2];
#pragma unroll
            for (int hh = 0; hh < 2; ++hh) {
                const int gt = 2 * pp + hh;
                float hv[4];
#pragma unroll
                for (int e = 0; e < 4; ++e) hv[e] = fmaxf(acc[2 * gt][m][e], 0.f) * acc[2 * gt + 1][m][e];
                v[hh][0] = pk_bf16(hv[0], hv[1]); v[hh][1] = pk_bf16(hv[2], hv[3]);
            }
            auto r0 = __builtin_amdgcn_permlane16_swap(v[0][0], v[1][0], false, false);
            auto r1 = __builtin_amdgcn_permlane16_swap(v[0][1], v[1][1], false, false);
            u32x4 o; o[0] = r0[0]; o[1] = r1[0]; o[2] = r0[1]; o[3] = r1[1];
            const int blk = 4 * j + 2 * wn + pp, chunk = ((g & 1) << 1) | (g >> 1);
            __builtin_amdgcn_raw_buffer_store_b128(o, rh, (unsigned)(blk * (int)(BLK_ELEMS * 2) + blk_off(row, chunk)), 0, HST_AUX);
        } else {
            float hv[4];
#pragma unroll
            for (int e = 0; e < 4; ++e) {
                const unsigned mine = __float_as_uint(acc[8][m][e]);
                auto r = __builtin_amdgcn_permlane32_swap(mine, mine, false, false);
                hv[e] = fmaxf(__uint_as_float(r[0]), 0.f) * __uint_as_float(r[1]);
            }
            u32x2 v; v[0] = pk_bf16(hv[0], hv[1]); v[1] = pk_bf16(hv[2], hv[3]);
            const int blk = 128 + (j >> 1), chunk = (j & 1) * 2 + wn;
            const unsigned off = g < 2 ? (unsigned)(blk * (int)(BLK_ELEMS * 2) + blk_off(row, chunk) + g * 8) : 0xfffffff0u;
            __builtin_amdgcn_raw_buffer_store_b64(v, rh, off, 0, HST_AUX);
        }
        SCHED();
    };
    hs(0, 0);
    BAR();
    STAMP(1, 2);
    LAS char* qt = lds;
    {
#pragma unroll
        for (int u = 0; u < 2; ++u) {
            const int nn = 16 * (2 * wn + (u ^ (wm & 1))) + 4 * g;
            const int h0 = nn / 18, h1 = (nn + 2) / 18;
            const int o0 = h0 * 48 + (nn - h0 * 18) * 2, o1 = h1 * 48 + (nn + 2 - h1 * 18) * 2;
#pragma unroll
            for (int m = 0; m < 4; ++m) {
                LAS char* rp = qt + (16 + 64 * wm + 16 * m + fr) * QS;
                if (h0 < 3) *(LAS unsigned*)(rp + o0) = pk_bf16(acc[9 + u][m][0], acc[9 + u][m][1]);
                if (h1 < 3) *(LAS unsigned*)(rp + o1) = pk_bf16(acc[9 + u][m][2], acc[9 + u][m][3]);
            }
            if (u == 0 && (w == 0 || w == 2 || w == 1)) {
                LAS char* rp = qt + fr * QS;
                if (h0 < 3) *(LAS unsigned*)(rp + o0) = pk_bf16(ach[0], ach[1]);
                if (h1 < 3) *(LAS unsigned*)(rp + o1) = pk_bf16(ach[2], ach[3]);
            }
            if (u == 0) hs(0, 1);
        }
    }
    asm volatile("s_waitcnt lgkmcnt(0)" ::: "memory");
    BAR();
    STAMP(1, 3);
    {
        const int s = tid >> 1, half = tid & 1;
        const int lrow = 16 + s, grow = 256 * mt + s;
        auto ld18 = [&](LAS const char* p, unsigned (&v)[9]) {
            const u32x4 a = *(LAS const u32x4*)p, c = *(LAS const u32x4*)(p + 16);
            v[0] = a[0]; v[1] = a[1]; v[2] = a[2]; v[3] = a[3]; v[4] = c[0]; v[5] = c[1]; v[6] = c[2]; v[7] = c[3];
            v[8] = *(LAS const unsigned*)(p + 32);
        };
        hs(0, 2);
        unsigned qv[9];
        ld18(qt + lrow * QS + 96, qv);
        float lg[4];
#pragma unroll
        for (int ww = 0; ww < 4; ++ww) {
            const int wi = 4 * half + ww;
            unsigned kv[9];
            ld18(qt + (lrow - 2 * wi) * QS, kv);
            float a = 0.f;
#pragma unroll
            for (int i = 0; i < 9; ++i) a = __builtin_amdgcn_fdot2_f32_bf16(__builtin_bit_cast(bf16x2_t, qv[i]), __builtin_bit_cast(bf16x2_t, kv[i]), a, false);
            a = rbf(rbf(a) * 0.23570226039551584f);
            lg[ww] = (grow - 2 * wi >= 0) ? a : -1e9f;
            if (ww == 0) hs(1, 0);
            if (ww == 2) hs(1, 1);
        }
        float mx = fmaxf(fmaxf(lg[0], lg[1]), fmaxf(lg[2], lg[3]));
        mx = fmaxf(mx, __shfl_xor(mx, 1));
        float ex[4], sum = 0.f;
#pragma unroll
        for (int ww = 0; ww < 4; ++ww) { ex[ww] = __expf(lg[ww] - mx); sum += ex[ww]; }
        sum += __shfl_xor(sum, 1);
        const float inv = 1.0f / sum;
        hs(1, 2);
        float o[18];
#pragma unroll
        for (int i = 0; i < 18; ++i) o[i] = 0.f;
#pragma unroll
        for (int ww = 0; ww < 4; ++ww) {
            const int wi = 4 * half + ww;
            const float pw = (grow - 2 * wi >= 0) ? rbf(ex[ww] * inv) : 0.f;
            unsigned vv[9];
            ld18(qt + (lrow - 2 * wi) * QS + 48, vv);
#pragma unroll
            for (int i = 0; i < 9; ++i) { o[2 * i] += pw * bf_lo(vv[i]); o[2 * i + 1] += pw * bf_hi(vv[i]); }
            if (ww < 3) hs(2, ww);
        }
        hs(3, 0);
#pragma unroll
        for (int ii = 0; ii < 5; ++ii) {
            const float s0 = half ? o[2 * ii] : o[(10 + 2 * ii) % 18], s1 = half ? o[2 * ii + 1] : o[(11 + 2 * ii) % 18];
            const float r0 = __shfl_xor(s0, 1), r1 = __shfl_xor(s1, 1);
            const float m0 = half ? o[(10 + 2 * ii) % 18] : o[2 * ii], m1 = half ? o[(11 + 2 * ii) % 18] : o[2 * ii + 1];
            const int i = half * 5 + ii;
            if (i < 9) {
                const int col = FF + 18 * j + 2 * i;
                *(unsigned*)((char*)hb + (size_t)(col >> 5) * (BLK_ELEMS * 2) + blk_off(grow, (col >> 3) & 3) + (col & 7) * 2) = pk_bf16(m0 + r0, m1 + r1);
            }
            if (ii == 2) hs(3, 1);
        }
        hs(3, 2);
    }
    STAMP(1, 15);
}

__global__ __launch_bounds__(512, 2) void k1_gateup(const u16* __restrict__ xnb, const u16* __restrict__ Wg,
                                                    const u16* __restrict__ Wu, const u16* __restrict__ wqh, const u16* __restrict__ gx,
                                                    const u16* __restrict__ Wd, const u16* __restrict__ wo, u16* __restrict__ hb, unsigned* __restrict__ cnt) {
    extern __shared__ __attribute__((aligned(16))) char smem[];
    LAS char* lds = (LAS char*)smem;
    const int tid = threadIdx.x, lane = tid & 63;
    if (blockIdx.x < 64 && tid == 0) cnt[blockIdx.x * 64] = 0u;
    const int w = __builtin_amdgcn_readfirstlane(tid >> 6);
    const int b = blockIdx.x, xcd = b & 7, idx = b >> 3;
    const int j = 4 * xcd + (idx & 3), mt = idx >> 2;
    const int wm = w >> 1, wn = w & 1;
    const unsigned lds0 = (unsigned)(size_t)lds;
    const int fr = lane & 15, g = lane >> 4, q4 = (lane >> 2) & 3, p4 = lane & 3;

    f32x4 acc[11][4], ach;
    {
        const unsigned srcA = (unsigned)((256 * mt + 32 * w) * 32 + lane * 8) * 2u;
        const int koct = w >> 1, mat = w & 1;
        const u16* W = mat ? Wu : Wg;
        unsigned srcB, srcX;
        const u16* baseX; int strideX; unsigned dX;
        {
            const int r = lane >> 3, swz = ((r >> 1) & 1) | ((koct & 1) << 1), c = (lane & 7) ^ (swz << 1);
            srcB = (unsigned)((8 * koct + r) * FF + 128 * j + 8 * c) * 2u;
            if (w < 2) {
                const int ko = lane >> 4, re = ((lane >> 1) & 7) ^ ((ko & 1) << 2), ce = (lane & 1) ^ w;
                srcX = (unsigned)(((w * 32 + j) * 576 + 8 * ko + re) * 16 + 8 * ce) * 2u;
                baseX = gx; strideX = 32 * 16; dX = lds0 + RE + w * 1024;
            } else if (w < 6) {
                const int ko = w - 2, swq = ((r >> 1) & 1) | ((ko & 1) << 1), cq = (lane & 7) ^ (swq << 1);
                srcX = (unsigned)((j * 576 + 8 * ko + r) * 64 + 8 * cq) * 2u;
                baseX = wqh; strideX = 32 * 64; dX = lds0 + RQ + ko * 1024;
            } else {
                const int hr = mt > 0 ? 256 * mt - 16 : 0;
                srcX = (unsigned)(hr * 64 + lane * 16);
                baseX = xnb; strideX = (int)BLK_ELEMS; dX = lds0 + RH;
            }
        }
        const int slotX = w < 2 ? 2048 : (w < 6 ? 4096 : 1024);
        const unsigned dA = lds0 + RA + (2 * w) * 1024, dB = lds0 + RB + (2 * w) * 1024;
        auto issue1 = [&](int t, int sl, int i) {
            const u16* ba = xnb + (size_t)t * BLK_ELEMS;
            const u16* bw = W + (size_t)t * 32 * FF;
            if (i == 0) glds16(bw, srcB, dB + sl * 16384);
            if (i == 1) glds16(bw + 64, srcB, dB + sl * 16384 + 1024);
            if (i == 2) glds16(baseX + (size_t)t * strideX, srcX, dX + sl * slotX);
            if (i == 3) glds16(ba, srcA, dA + sl * 16384);
            if (i == 4) glds16_o1k(ba, srcA, dA + sl * 16384);
        };
        auto prefetch = [&](int p) {
            const int split = xcd >> 1;
            const unsigned start = (unsigned)((split == 0 ? 0 : 1344 + 1280 * (split - 1)) * DM * 2);
            const unsigned len = (unsigned)((split == 0 ? 1344 : 1280) * DM * 2), lwd = (unsigned)(704 * DM * 2);
            p = p < 6 ? p : 5;
            unsigned o = (unsigned)((idx * 8 + w) * 6144 + p * 1024); o = o + 1024 <= len ? o : 0u;
            const char* src = (split == 3 && o >= lwd) ? (const char*)wo + (o - lwd) : (const char*)Wd + start + o;
            glds16(src, (unsigned)lane * 16u, lds0 + T_DUMMY);
        };
        STAMP(1, 0);
#pragma unroll
        for (int t = 0; t < 3; ++t) {
#pragma unroll
            for (int i = 0; i < 5; ++i) issue1(t, t, i);
            if (t == 1) prefetch(0);
        }

        const unsigned pa = lds0 + RA + blk_off(64 * wm + fr, g);
        const int swz = ((q4 >> 1) & 1) | ((g & 1) << 1);
        const unsigned pb0 = lds0 + RB + (g * 4 + wn) * 1024 + q4 * 128 + (swz << 5) + p4 * 8;
        const int emat = p4 >> 1;
        const unsigned pe0 = lds0 + RE + (emat * 4 + g) * 256 + ((q4 ^ ((g & 1) << 2)) * 32) + ((wn ^ emat) * 16) + (p4 & 1) * 8;
        const unsigned pq0 = lds0 + RQ + g * 1024 + q4 * 128 + (((2 * wn + (wm & 1)) ^ swz) << 5) + p4 * 8;
        LAS char* pbv[4]; LAS char* pqv[2];
#pragma unroll
        for (int u = 0; u < 4; ++u) pbv[u] = (LAS char*)(size_t)(pb0 ^ (u << 5));
        pqv[0] = (LAS char*)(size_t)pq0; pqv[1] = (LAS char*)(size_t)(pq0 ^ 32);
        LAS char* pev[2] = {(LAS char*)(size_t)pe0, (LAS char*)(size_t)(pe0 ^ 128)};
        LAS char* phv = lds + RH + blk_off(fr, g);
        auto rdB = [&](int sl, int nf, int t1) -> s16x4 {
            if (nf < 8) return tr_read(pbv[nf >> 1] + sl * 16384 + (nf & 1) * 2048 + t1 * 512);
            if (nf == 8) return tr_read(pev[t1] + sl * 2048);
            return tr_read(pqv[nf - 9] + sl * 4096 + t1 * 512);
        };
        auto rdA = [&](int sl, int m) -> bf16x8 { return row_frag((LAS char*)(size_t)pa + sl * 16384 + m * 1024); };
        auto rdH = [&](int sl) -> bf16x8 { return row_frag(phv + sl * 1024); };
#pragma unroll
        for (int n = 0; n < 11; ++n)
#pragma unroll
            for (int m = 0; m < 4; ++m) { acc[n][m] = (f32x4){0.f, 0.f, 0.f, 0.f}; asm volatile("" : "+v"(acc[n][m])); }
        ach = (f32x4){0.f, 0.f, 0.f, 0.f}; asm volatile("" : "+v"(ach));

        wait_vm<11>(); BAR();
        STAMP(1, 1);
        bf16x8 fa[2][2], fc[2], fh, fb[NB];
#pragma unroll
        for (int m = 0; m < 2; ++m) { fa[0][m] = rdA(0, m); fc[m] = rdA(0, 2 + m); }
#pragma unroll
        for (int i = 0; i < PF; ++i) { s16x4 lo = rdB(0, i, 0), hi = rdB(0, i, 1); fb[i] = __builtin_shufflevector(lo, hi, 0, 1, 2, 3, 4, 5, 6, 7); }
#define MM(C, A, B) mfma_ip(C, A, B)
        auto step = [&](auto S_, int t, bool do_issue, auto LAST1_, auto WAIT0_) {
            constexpr int S = decltype(S_)::value;
            constexpr bool has_next = !decltype(LAST1_)::value;
            constexpr bool wait0 = decltype(WAIT0_)::value;
            constexpr int sl = S, sn = (S + 1) & 3, cur = S & 1;
#pragma unroll
            for (int nf = 0; nf < 11; ++nf) {
                constexpr int dummy = 0; (void)dummy;
                const int r = 11 * S + nf;
                const int psl = (nf + PF < 11) ? sl : sn, nn = (nf + PF < 11) ? nf + PF : nf + PF - 11;
                const bool pf = (nf + PF < 11) || has_next;
                const bool lastg = (nf == 10);
                s16x4 lo, hi;
                if (!lastg) MM(acc[nf][0], fb[r % NB], fa[cur][0]); else MM(acc[nf][2], fb[r % NB], fc[0]);
                SCHED();
                if (nf == 0) {
                    if (!wait0) wait_vm<6>(); else wait_vm<0>();
                    BAR();
                }
                if (pf) lo = rdB(psl, nn, 0);
                SCHED();
                if (!lastg) MM(acc[nf][1], fb[r % NB], fa[cur][1]); else MM(acc[nf][3], fb[r % NB], fc[1]);
                SCHED();
                if (pf) hi = rdB(psl, nn, 1);
                if (lastg && has_next) fc[0] = rdA(sn, 2);
                SCHED();
                if (!lastg) MM(acc[nf][2], fb[r % NB], fc[0]); else MM(acc[nf][0], fb[r % NB], fa[cur][0]);
                SCHED();
                if (lastg && has_next) fc[1] = rdA(sn, 3);
                if (nf >= 4 && nf < 6 && has_next) fa[cur ^ 1][nf - 4] = rdA(sn, nf - 4);
                if (nf == 7) fh = rdH(sl);
                if ((nf < 4 || nf == 6) && do_issue) issue1(t + 3, (S + 3) & 3, nf < 4 ? nf : 4);
                if (nf == 8 && (S & 1) == 0 && do_issue) prefetch(1 + (t >> 1));
                SCHED();
                if (!lastg) MM(acc[nf][3], fb[r % NB], fc[1]); else MM(acc[nf][1], fb[r % NB], fa[cur][1]);
                SCHED();
                if (nf == 9) { MM(ach, fb[r % NB], fh); SCHED(); }
                if (pf) fb[(r + PF) % NB] = __builtin_shufflevector(lo, hi, 0, 1, 2, 3, 4, 5, 6, 7);
            }
        };
        using I0 = std::integral_constant<int, 0>; using I1 = std::integral_constant<int, 1>;
        using I2 = std::integral_constant<int, 2>; using I3 = std::integral_constant<int, 3>;
        using F = std::false_type; using T = std::true_type;
#pragma unroll 1
        for (int t0 = 0; t0 < 16; t0 += 4) {
            step(I0{}, t0, true, F{}, F{});
            step(I1{}, t0 + 1, true, F{}, F{});
            step(I2{}, t0 + 2, true, F{}, F{});
            step(I3{}, t0 + 3, t0 + 3 < 15, F{}, F{});
        }
        step(I0{}, 16, false, F{}, T{});
        step(I1{}, 17, false, T{}, T{});
    }
    asm volatile("s_nop 7\n\ts_nop 7" ::: "memory");
    STAMP(1, 4);
    k1_epilogue(acc, ach, lds, lds0, w, xcd, idx, j, mt, Wd, wo, hb);
}

constexpr int K2_NT = 21;
constexpr int K2_RB = 0, K2_RE = 65536, K2_LDS = 81920;
constexpr int NSPLIT = 4;
__device__ __forceinline__ void gload2(bf16x8& d0, bf16x8& d1, const void* sbase, unsigned voff) {
    sbase = uni(sbase);
#define AFLAG ""
    asm volatile("s_nop 4\n\tglobal_load_dwordx4 %0, %2, %3" AFLAG "\n\tglobal_load_dwordx4 %1, %2, %3 offset:1024" AFLAG
                 : "=&v"(d0), "=&v"(d1) : "v"(voff), "s"(sbase) : "memory");
}
__global__ __launch_bounds__(512, 2) void k2_down(const u16* __restrict__ hb, const u16* __restrict__ Wd,
                                                  const u16* __restrict__ wo, const u16* __restrict__ dx, u16* __restrict__ part, unsigned* __restrict__ cnt,
                                                  const u16* __restrict__ x, u16* __restrict__ out) {
    extern __shared__ __attribute__((aligned(16))) char smem[];
    LAS char* lds = (LAS char*)smem;
    const int tid = threadIdx.x, lane = tid & 63;
    const int w = __builtin_amdgcn_readfirstlane(tid >> 6);
    const int b = blockIdx.x, xcd = b & 7, idx = b >> 3;
    const int split = xcd >> 1, mt = (xcd & 1) * 8 + (idx >> 2), nt = idx & 3;
    const int wm = w >> 1, kh = w & 1;
    const int T0 = split == 0 ? 0 : 21 + 20 * (split - 1);

    const unsigned srcA = (unsigned)(kh * (int)BLK_ELEMS * 2 + blk_off(128 * mt + 32 * wm + (lane & 15), lane >> 4));
    const int koct = w >> 1, sub = w & 1;
    unsigned offB, offE;
    {
        const int r = lane >> 3, swz = ((r >> 1) & 1) | ((koct & 1) << 1), c = (lane & 7) ^ (swz << 1);
        offB = (unsigned)((32 * sub + 8 * koct + r) * DM + 128 * nt + 8 * c) * 2u;
        const int re = ((lane >> 1) & 7) ^ ((koct & 1) << 2), ce = (lane & 1);
        offE = (unsigned)((nt * 5184 + 32 * sub + 8 * koct + re) * 16 + 8 * ce) * 2u;
    }
    const unsigned lds0 = (unsigned)(size_t)lds;
    const unsigned dB = lds0 + K2_RB + (2 * w) * 1024, dE = lds0 + K2_RE + (sub * 4 + koct) * 256;
    bf16x8 fa[4][2];
    auto issue1 = [&](int t, int sl, int i, bf16x8& a0, bf16x8& a1) {
        int T = T0 + t; if (t == K2_NT - 1 && split != 0) T -= 1;
        const u16* Wt = T < 72 ? Wd + (size_t)T * 64 * DM : wo + (size_t)(T - 72) * 64 * DM;
        const u16* ba = hb + (size_t)T * 2 * BLK_ELEMS;
        if (i == 0) glds16(Wt, offB, dB + sl * 16384);
        if (i == 1) glds16(Wt + 64, offB, dB + sl * 16384 + 1024);
        if (i == 2) glds16_q(dx + (size_t)T * 64 * 16, offE, dE + sl * 2048);
        if (i == 3) gload2(a0, a1, ba, srcA);
    };
    STAMP(2, 0);
#pragma unroll
    for (int t = 0; t < 3; ++t)
#pragma unroll
        for (int i = 0; i < 4; ++i) issue1(t, t, i, fa[t][0], fa[t][1]);

    const int fr = lane & 15, g = lane >> 4, q4 = (lane >> 2) & 3, p4 = lane & 3;
    const int swz = ((q4 >> 1) & 1) | ((g & 1) << 1);
    LAS char* pb[4];
#pragma unroll
    for (int u = 0; u < 4; ++u) pb[u] = lds + K2_RB + (g * 4 + kh * 2) * 1024 + q4 * 128 + ((u ^ swz) << 5) + p4 * 8;
    LAS char* pe0 = lds + K2_RE + (kh * 4 + g) * 256 + ((q4 ^ ((g & 1) << 2)) * 32) + p4 * 8;
    LAS char* pe1 = lds + K2_RE + (kh * 4 + g) * 256 + (((4 + q4) ^ ((g & 1) << 2)) * 32) + p4 * 8;
    auto rdB = [&](int sl, int nf, int t1) -> s16x4 {
        if (nf < 8) return tr_read(pb[nf & 3] + sl * 16384 + (nf >> 2) * 1024 + t1 * 512);
        return tr_read((t1 ? pe1 : pe0) + sl * 2048);
    };

    f32x4 acc[9][2];
#pragma unroll
    for (int n = 0; n < 9; ++n)
#pragma unroll
        for (int m = 0; m < 2; ++m) { acc[n][m] = (f32x4){0.f, 0.f, 0.f, 0.f}; asm volatile("" : "+v"(acc[n][m])); }

    wait_vm<10>(); BAR();
    STAMP(2, 1);
    constexpr int PF2 = 4;
    bf16x8 fb[9];
#pragma unroll
    for (int i = 0; i < PF2; ++i) { s16x4 lo = rdB(0, i, 0), hi = rdB(0, i, 1); fb[i] = __builtin_shufflevector(lo, hi, 0, 1, 2, 3, 4, 5, 6, 7); }
    auto step = [&](auto S_, int t, bool do_issue, bool wait0, auto LAST_) {
        constexpr int S = decltype(S_)::value;
        constexpr bool has_next = !decltype(LAST_)::value;
        constexpr int sl = S, sn = (S + 1) & 3;
        asm volatile("" : "+v"(fa[sl][0]), "+v"(fa[sl][1]));
        if (!has_next && split != 0) {
#pragma unroll
            for (int m = 0; m < 2; ++m) fa[sl][m] = (bf16x8){0, 0, 0, 0, 0, 0, 0, 0};
            asm volatile("s_nop 1" : "+v"(fa[sl][0]), "+v"(fa[sl][1]));
        }
#pragma unroll
        for (int nf = 0; nf < 9; ++nf) {
            const int psl = (nf + PF2 < 9) ? sl : sn, nn = (nf + PF2 < 9) ? nf + PF2 : nf + PF2 - 9;
            const bool pf = (nf + PF2 < 9) || has_next;
            s16x4 lo, hi;
            mfma_ip(acc[nf][0], fb[nf], fa[sl][0]);
            SCHED();
            if (nf == 0) {
                if (!wait0) wait_vm<5>(); else wait_vm<0>();
                BAR();
            }
            if (pf) lo = rdB(psl, nn, 0);
            SCHED();
            mfma_ip(acc[nf][1], fb[nf], fa[sl][1]);
            SCHED();
            if (pf) hi = rdB(psl, nn, 1);
            if (nf >= 1 && nf < 5 && do_issue) issue1(t + 3, (S + 3) & 3, nf - 1, fa[(S + 3) & 3][0], fa[(S + 3) & 3][1]);
            SCHED();
            if (pf) fb[nn] = __builtin_shufflevector(lo, hi, 0, 1, 2, 3, 4, 5, 6, 7);
        }
    };
    using I0 = std::integral_constant<int, 0>; using I1 = std::integral_constant<int, 1>;
    using I2 = std::integral_constant<int, 2>; using I3 = std::integral_constant<int, 3>;
#pragma unroll 1
    for (int t0 = 0; t0 < 20; t0 += 4) {
        step(I0{}, t0, true, false, std::false_type{});
        step(I1{}, t0 + 1, true, false, std::false_type{});
        step(I2{}, t0 + 2, t0 + 2 < 18, false, std::false_type{});
        step(I3{}, t0 + 3, t0 + 3 < 18, t0 + 3 == 19, std::false_type{});
    }
    step(I0{}, 20, false, true, std::true_type{});
    asm volatile("s_nop 7\n\ts_nop 7" ::: "memory");
    BAR();
    STAMP(2, 2);
    f32x4 val[10];
    {
        LAS char* xw = lds + w * 10240;
        LAS char* xr = lds + (w ^ 1) * 10240;
#pragma unroll
        for (int n = 0; n < 9; ++n) {
            const bool give = kh ? (n < 5) : (n >= 5);
            if (give) {
#pragma unroll
                for (int m = 0; m < 2; ++m) *(LAS f32x4*)(xw + ((kh ? n : n - 5) * 2 + m) * 1024 + lane * 16) = acc[n][m];
            }
        }
        asm volatile("s_waitcnt lgkmcnt(0)" ::: "memory");
        BAR();
#pragma unroll
        for (int i = 0; i < 10; ++i) {
            const f32x4 mine = kh ? acc[(5 + (i >> 1)) < 9 ? 5 + (i >> 1) : 8][i & 1] : acc[i >> 1][i & 1];
            val[i] = mine + *(LAS f32x4*)(xr + i * 1024 + lane * 16);
        }
    }
    {
        const int nit = kh ? 8 : 10;
        const int grp = mt * 4 + nt;
        const __amdgpu_buffer_rsrc_t rp = __builtin_amdgcn_make_buffer_rsrc((void*)part, 0, NSPLIT * 64 * 8 * 10 * 512, 0x00020000);
        const unsigned slot = (unsigned)((grp * 8 + w) * 10 * 512 + lane * 8);
        constexpr unsigned SPL = 64 * 8 * 10 * 512;
        u32x2 xv[3]; unsigned xoff[3];
#pragma unroll
        for (int q = 0; q < 3; ++q) {
            int i = split + 4 * q; i = i < nit ? i : split;
            const int n = kh ? 5 + (i >> 1) : (i >> 1), m = i & 1;
            const int row = 128 * mt + 32 * wm + 16 * m + fr;
            const int col = (n < 8 ? 128 * nt + 16 * n : 512 + 16 * nt) + 4 * g;
            xoff[q] = (unsigned)(row * DM + col);
            xv[q] = *(const u32x2*)(x + xoff[q]);
        }
#pragma unroll
        for (int i = 0; i < 10; ++i) {
            if (i < nit && (i & 3) != split) {
                u32x2 v; v[0] = pk_bf16(val[i][0], val[i][1]); v[1] = pk_bf16(val[i][2], val[i][3]);
                __builtin_amdgcn_raw_buffer_store_b64(v, rp, slot + split * SPL + i * 512, 0, 16);
            }
        }
        STAMP(2, 3);
        asm volatile("s_waitcnt vmcnt(0)" ::: "memory");
        BAR();
        STAMP(2, 4);
        unsigned* c = cnt + grp * 64;
        if (tid == 0) {
            __hip_atomic_fetch_add(c, 1u, __ATOMIC_RELAXED, __HIP_MEMORY_SCOPE_AGENT);
            unsigned spins = 0;
            while (__hip_atomic_load(c, __ATOMIC_RELAXED, __HIP_MEMORY_SCOPE_AGENT) < (unsigned)NSPLIT) {
                __builtin_amdgcn_s_sleep(1);
                if (++spins > (1u << 22)) break;
            }
        }
        BAR();
        STAMP(2, 5);
        u32x2 pv[3][3];
#pragma unroll
        for (int q = 0; q < 3; ++q) {
            const int i = split + 4 * q;
#pragma unroll
            for (int s2 = 1; s2 < NSPLIT; ++s2) {
                const int so = (split + s2) & 3;
                const unsigned off = i < nit ? slot + so * SPL + i * 512 : 0xfffffff0u;
                pv[q][s2 - 1] = __builtin_amdgcn_raw_buffer_load_b64(rp, off, 0, 16);
            }
        }
#pragma unroll
        for (int q = 0; q < 3; ++q) {
            const int i = split + 4 * q;
            if (i < nit) {
                f32x4 o = val[0];
#pragma unroll
                for (int ii = 0; ii < 10; ++ii) if (ii == i) o = val[ii];
#pragma unroll
                for (int s2 = 0; s2 < 3; ++s2) { o[0] += bf_lo(pv[q][s2][0]); o[1] += bf_hi(pv[q][s2][0]); o[2] += bf_lo(pv[q][s2][1]); o[3] += bf_hi(pv[q][s2][1]); }
                u32x2 v; v[0] = pk_bf16(o[0] + bf_lo(xv[q][0]), o[1] + bf_hi(xv[q][0])); v[1] = pk_bf16(o[2] + bf_lo(xv[q][1]), o[3] + bf_hi(xv[q][1]));
                *(u32x2*)(out + xoff[q]) = v;
            }
        }
    }
    STAMP(2, 15);
}

extern "C" void kernel_launch(void* const* d_in, const int* in_sizes, int n_in, void* d_out, int out_size, void* d_ws,
                              size_t ws_size, hipStream_t stream) {
    const u16* x = (const u16*)d_in[0];
    const u16* wqkv = (const u16*)d_in[1];
    const u16* wo = (const u16*)d_in[2];
    const u16* Wg = (const u16*)d_in[4];
    const u16* Wu = (const u16*)d_in[5];
    const u16* Wd = (const u16*)d_in[6];
    char* ws = (char*)d_ws;
    u16* xnb = (u16*)ws;
    u16* hb = (u16*)(ws + WS_HB);
    u16* part = (u16*)(ws + WS_PART);
    u16* wqh = (u16*)(ws + WS_WQH);
    u16* gx = (u16*)(ws + WS_GX);
    u16* dx = (u16*)(ws + WS_DX);
    unsigned* cnt = (unsigned*)(ws + WS_CNT);
    (void)hipFuncSetAttribute((const void*)k1_gateup, hipFuncAttributeMaxDynamicSharedMemorySize, K1_LDS);
    (void)hipFuncSetAttribute((const void*)k2_down, hipFuncAttributeMaxDynamicSharedMemorySize, K2_LDS);
    k0_ln<<<256, 256, 0, stream>>>(x, xnb, wqkv, wqh, Wg, Wu, gx, Wd, wo, dx, (const char*)ws, (const char*)d_out);
    k1_gateup<<<256, 512, K1_LDS, stream>>>(xnb, Wg, Wu, wqh, gx, Wd, wo, hb, cnt);
    k2_down<<<256, 512, K2_LDS, stream>>>(hb, Wd, wo, dx, part, cnt, x, (u16*)d_out);
}
```

```cpp
#define NO_WPF
#include <hip/hip_runtime.h>
#include <hip/hip_bf16.h>
#include <stdint.h>
#include <type_traits>

typedef unsigned short u16;
typedef short bf16x8 __attribute__((ext_vector_type(8)));
typedef short s16x4 __attribute__((ext_vector_type(4)));
typedef float f32x4 __attribute__((ext_vector_type(4)));
typedef float f32x2 __attribute__((ext_vector_type(2)));
typedef __bf16 bf16x2_t __attribute__((ext_vector_type(2)));
typedef unsigned u32x4 __attribute__((ext_vector_type(4)));
typedef unsigned u32x2 __attribute__((ext_vector_type(2)));
#define LAS __attribute__((address_space(3)))
#define GAS __attribute__((address_space(1)))

constexpr int SEQ = 2048, DM = 576, FF = 4608;
constexpr int NKB_X = 18;
constexpr int NKB_H = 162;
constexpr size_t BLK_ELEMS = (size_t)SEQ * 32;

constexpr size_t WS_HB = 18 * 131072, WS_PART = WS_HB + 162 * 131072, WS_WQH = WS_PART + 4 * 64 * 8 * 10 * 512, WS_GX = WS_WQH + 32 * 576 * 64 * 2,
                 WS_DX = WS_GX + 2 * 32 * 576 * 16 * 2, WS_CNT = WS_DX + 4 * 5184 * 16 * 2, WS_END = WS_CNT + 64 * 256;

__device__ __forceinline__ unsigned pk_bf16(float lo, float hi) {
    f32x2 v = {lo, hi};
    bf16x2_t b = __builtin_convertvector(v, bf16x2_t);
    return __builtin_bit_cast(unsigned, b);
}
__device__ __forceinline__ float bf_lo(unsigned u) { return __uint_as_float(u << 16); }
__device__ __forceinline__ float bf_hi(unsigned u) { return __uint_as_float(u & 0xffff0000u); }
__device__ __forceinline__ float rbf(float x) { return bf_lo(pk_bf16(x, x)); }

__device__ __forceinline__ const void* uni(const void* p) {
    const unsigned long long v = (unsigned long long)p;
    const unsigned lo = __builtin_amdgcn_readfirstlane((unsigned)v), hi = __builtin_amdgcn_readfirstlane((unsigned)(v >> 32));
    return (const void*)(((unsigned long long)hi << 32) | lo);
}
__device__ __forceinline__ void glds16(const void* sbase, unsigned voff, unsigned lds) {
    sbase = uni(sbase); lds = __builtin_amdgcn_readfirstlane(lds);
    asm volatile("s_mov_b32 m0, %2\n\ts_nop 3\n\tglobal_load_lds_dwordx4 %0, %1"
                 :: "v"(voff), "s"(sbase), "s"(lds) : "memory", "m0");
}
__device__ __forceinline__ void glds16_o1k(const void* sbase, unsigned voff, unsigned lds) {
    sbase = uni(sbase); lds = __builtin_amdgcn_readfirstlane(lds);
    asm volatile("s_mov_b32 m0, %2\n\ts_nop 3\n\tglobal_load_lds_dwordx4 %0, %1 offset:1024"
                 :: "v"(voff), "s"(sbase), "s"(lds) : "memory", "m0");
}
__device__ __forceinline__ void glds16_q(const void* sbase, unsigned voff, unsigned lds) {
    sbase = uni(sbase); lds = __builtin_amdgcn_readfirstlane(lds);
    unsigned sv; unsigned long long ex;
    asm volatile("s_mov_b32 %0, m0\n\ts_mov_b64 %1, exec\n\ts_mov_b32 m0, %4\n\ts_mov_b64 exec, 0xffff\n\ts_nop 1\n\tglobal_load_lds_dwordx4 %2, %3\n\ts_mov_b64 exec, %1\n\ts_mov_b32 m0, %0"
                 : "=&s"(sv), "=&s"(ex) : "v"(voff), "s"(sbase), "s"(lds) : "memory");
}
__device__ __forceinline__ void glds16_m(const void* sbase, unsigned voff, unsigned lds) {
    sbase = uni(sbase); lds = __builtin_amdgcn_readfirstlane(lds);
    unsigned sv; unsigned long long ex;
    asm volatile("s_mov_b32 %0, m0\n\ts_mov_b64 %1, exec\n\ts_mov_b32 m0, %4\n\ts_mov_b32 exec_lo, -1\n\ts_mov_b32 exec_hi, 0xff\n\ts_nop 1\n\tglobal_load_lds_dwordx4 %2, %3\n\ts_mov_b64 exec, %1\n\ts_mov_b32 m0, %0"
                 : "=&s"(sv), "=&s"(ex) : "v"(voff), "s"(sbase), "s"(lds) : "memory");
}
#define SCHED() __builtin_amdgcn_sched_barrier(0)
#define STAMP(kid, i) do {} while (0)
#define WSTAMP(i) do {} while (0)
__device__ __forceinline__ s16x4 tr_read(LAS char* p) {
    return __builtin_bit_cast(s16x4, __builtin_amdgcn_ds_read_tr16_b64_v4i16((LAS s16x4*)p));
}
__device__ __forceinline__ bf16x8 tr_frag(LAS char* p, int off_t1) {
    s16x4 lo = tr_read(p), hi = tr_read(p + off_t1);
    return __builtin_shufflevector(lo, hi, 0, 1, 2, 3, 4, 5, 6, 7);
}
__device__ __forceinline__ bf16x8 row_frag(LAS char* p) { return *(LAS bf16x8*)p; }
#define MFMA16(a, b, c) __builtin_amdgcn_mfma_f32_16x16x32_bf16((a), (b), (c), 0, 0, 0)
__device__ __forceinline__ void mfma_ip(f32x4& c, bf16x8 a, bf16x8 b) { asm volatile("v_mfma_f32_16x16x32_bf16 %0, %1, %2, %0" : "+v"(c) : "v"(a), "v"(b)); }
#define WAIT_V(n) asm volatile("s_waitcnt vmcnt(" #n ")" ::: "memory")
#define BAR() do { asm volatile("" ::: "memory"); __builtin_amdgcn_s_barrier(); asm volatile("" ::: "memory"); } while (0)

__device__ __forceinline__ int blk_off(int row, int chunk) { return row * 64 + ((chunk ^ (((row >> 3) & 1) << 1)) << 4); }

__global__ __launch_bounds__(256) void k0_ln(const u16* __restrict__ x, u16* __restrict__ xnb,
                                            const u16* __restrict__ wqkv, u16* __restrict__ wqh,
                                            const u16* __restrict__ Wg, const u16* __restrict__ Wu, u16* __restrict__ gx,
                                            const u16* __restrict__ Wd, const u16* __restrict__ wo, u16* __restrict__ dx,
                                            const char* __restrict__ wsb, const char* __restrict__ outb) {
    if (threadIdx.x >= 64) {
        const int l = threadIdx.x - 64, head = blockIdx.x >> 3, k0 = (blockIdx.x & 7) * 72;
        unsigned v[12];
#pragma unroll
        for (int i = 0; i < 12; ++i) {
            const int id = l + 192 * i, k = k0 + (id >> 5), cp = id & 31;
            const int part = cp >= 18 ? 2 : (cp >= 9 ? 1 : 0), d2 = cp - part * 9;
            const int src = (part == 2 ? 0 : part + 1) * DM + 18 * head + 2 * d2;
            v[i] = cp < 27 ? *(const unsigned*)(wqkv + (size_t)k * (3 * DM) + src) : 0u;
        }
        u32x4 c[3];
#pragma unroll
        for (int i = 0; i < 2; ++i) {
            const int id = l + 192 * i;
            if (id < 288) {
                const int gid = blockIdx.x * 288 + id, mat = gid / 36864, r = gid - mat * 36864, k = r >> 6, ch = r & 63;
                c[i] = *(const u32x4*)((mat ? Wu : Wg) + (size_t)k * FF + 4096 + 8 * ch);
            }
        }
        if (l < 162) {
            const int gid = blockIdx.x * 162 + l, k = gid >> 3, ch = gid & 7;
            c[2] = *(const u32x4*)((k < FF ? Wd + (size_t)k * DM : wo + (size_t)(k - FF) * DM) + 512 + 8 * ch);
        }
#pragma unroll
        for (int i = 0; i < 12; ++i) {
            const int id = l + 192 * i;
            *(unsigned*)(wqh + ((size_t)head * 576 + k0) * 64 + 2 * id) = v[i];
        }
#pragma unroll
        for (int i = 0; i < 2; ++i) {
            const int id = l + 192 * i;
            if (id < 288) {
                const int gid = blockIdx.x * 288 + id, mat = gid / 36864, r = gid - mat * 36864, k = r >> 6, ch = r & 63;
                *(u32x4*)(gx + (((size_t)mat * 32 + (ch >> 1)) * 576 + k) * 16 + 8 * (ch & 1)) = c[i];
            }
        }
        if (l < 162) {
            const int gid = blockIdx.x * 162 + l, k = gid >> 3, ch = gid & 7;
            *(u32x4*)(dx + ((size_t)(ch >> 1) * 5184 + k) * 16 + 8 * (ch & 1)) = c[2];
        }
        if (blockIdx.x < 8) {
            unsigned long long pc; int o1, o2;
            asm volatile("s_getpc_b64 %0\n.Lcpf%=:\n\ts_mov_b32 %1, _Z9k1_gateupPKtS0_S0_S0_S0_S0_S0_PtPj-.Lcpf%=\n\ts_mov_b32 %2, _Z7k2_downPKtS0_S0_S0_PtPjS0_S1_-.Lcpf%="
                         : "=s"(pc), "=s"(o1), "=s"(o2));
            const char* c1 = (const char*)(pc + o1);
            const char* c2 = (const char*)(pc + o2);
            unsigned a0, a1;
            const char* p1 = c1 + (size_t)l * 64;
            const char* p2 = (l < 96 ? c2 : c1 + 12288 - 96 * 64) + (size_t)l * 64;
            asm volatile("global_load_dword %0, %2, off\n\tglobal_load_dword %1, %3, off\n\ts_waitcnt vmcnt(0)" : "=&v"(a0), "=&v"(a1) : "v"(p1), "v"(p2) : "memory");
        }
        return;
    }
    const int lane = threadIdx.x, i = lane & 7, row = blockIdx.x * 8 + (lane >> 3);
    u32x4 xv[9];
#pragma unroll
    for (int cc = 0; cc < 9; ++cc) xv[cc] = *(const u32x4*)(x + (size_t)row * DM + (i + 8 * cc) * 8);
    float s = 0.f;
#pragma unroll
    for (int cc = 0; cc < 9; ++cc)
#pragma unroll
        for (int e = 0; e < 4; ++e) s += bf_lo(xv[cc][e]) + bf_hi(xv[cc][e]);
    s += __shfl_xor(s, 1); s += __shfl_xor(s, 2); s += __shfl_xor(s, 4);
    const float mean = s * (1.0f / 576.0f);
    float q = 0.f;
#pragma unroll
    for (int cc = 0; cc < 9; ++cc)
#pragma unroll
        for (int e = 0; e < 4; ++e) { const float a = bf_lo(xv[cc][e]) - mean, c2 = bf_hi(xv[cc][e]) - mean; q += a * a + c2 * c2; }
    q += __shfl_xor(q, 1); q += __shfl_xor(q, 2); q += __shfl_xor(q, 4);
    const float mb = rbf(mean), vb = rbf(q * (1.0f / 576.0f));
    const float sb = rbf(sqrtf(rbf(vb + 1e-5f)));
    const float rs = 1.0f / sb;
#pragma unroll
    for (int cc = 0; cc < 9; ++cc) {
        u32x4 o;
#pragma unroll
        for (int e = 0; e < 4; ++e) {
            const unsigned d = pk_bf16(bf_lo(xv[cc][e]) - mb, bf_hi(xv[cc][e]) - mb);
            o[e] = pk_bf16(bf_lo(d) * rs, bf_hi(d) * rs);
        }
        const int blk = 2 * cc + (i >> 2);
        *(u32x4*)((char*)xnb + (size_t)blk * (BLK_ELEMS * 2) + blk_off(row, i & 3)) = o;
    }
}

constexpr int RA = 0, RB = 65536, RE = 131072, RQ = 139264, RH = 155648;
constexpr int T_DUMMY = 159744;
constexpr int K1_LDS = T_DUMMY + 1024;
constexpr int QS = 144;
#define HST_AUX 16
#define PF 3
constexpr int NB = PF + 1;
template <int N> __device__ __forceinline__ void wait_vm() { asm volatile("s_waitcnt vmcnt(%0)" :: "i"(N) : "memory"); }
#define GLDS_MASKED(NAME, LO, HI) \
__device__ __forceinline__ void NAME(const void* sbase, unsigned voff, unsigned lds) { \
    sbase = uni(sbase); lds = __builtin_amdgcn_readfirstlane(lds); \
    unsigned sv; unsigned long long ex; \
    asm volatile("s_mov_b32 %0, m0\n\ts_mov_b64 %1, exec\n\ts_mov_b32 m0, %4\n\ts_mov_b32 exec_lo, " #LO "\n\ts_mov_b32 exec_hi, " #HI "\n\tglobal_load_lds_dwordx4 %2, %3\n\ts_mov_b64 exec, %1\n\ts_mov_b32 m0, %0" \
                 : "=&s"(sv), "=&s"(ex) : "v"(voff), "s"(sbase), "s"(lds) : "memory"); }
GLDS_MASKED(glds16_32, -1, 0)
GLDS_MASKED(glds16_8, 0xff, 0)

__device__ __forceinline__ void k1_epilogue(f32x4 (&acc)[11][4], f32x4& ach, LAS char* lds, unsigned lds0, int w, int xcd, int idx,
                                            int j, int mt, const u16* __restrict__ Wd, const u16* __restrict__ wo, u16* __restrict__ hb) {
    int tid = threadIdx.x; asm volatile("" : "+v"(tid));
    const int lane = tid & 63, fr = lane & 15, g = lane >> 4;
    const int wm = w >> 1, wn = w & 1;
    const __amdgpu_buffer_rsrc_t rh = __builtin_amdgcn_make_buffer_rsrc((void*)hb, 0, (int)(NKB_H * BLK_ELEMS * 2), 0x00020000);
    auto hs = [&](int m, int part) {
        const int row = 256 * mt + 64 * wm + 16 * m + fr;
        SCHED();
        if (part < 2) {
            const int pp = part;
            unsigned v[2][2];
#pragma unroll
            for (int hh = 0; hh < 2; ++hh) {
                const int gt = 2 * pp + hh;
                float hv[4];
#pragma unroll
                for (int e = 0; e < 4; ++e) hv[e] = fmaxf(acc[2 * gt][m][e], 0.f) * acc[2 * gt + 1][m][e];
                v[hh][0] = pk_bf16(hv[0], hv[1]); v[hh][1] = pk_bf16(hv[2], hv[3]);
            }
            auto r0 = __builtin_amdgcn_permlane16_swap(v[0][0], v[1][0], false, false);
            auto r1 = __builtin_amdgcn_permlane16_swap(v[0][1], v[1][1], false, false);
            u32x4 o; o[0] = r0[0]; o[1] = r1[0]; o[2] = r0[1]; o[3] = r1[1];
            const int blk = 4 * j + 2 * wn + pp, chunk = ((g & 1) << 1) | (g >> 1);
            __builtin_amdgcn_raw_buffer_store_b128(o, rh, (unsigned)(blk * (int)(BLK_ELEMS * 2) + blk_off(row, chunk)), 0, HST_AUX);
        } else {
            float hv[4];
#pragma unroll
            for (int e = 0; e < 4; ++e) {
                const unsigned mine = __float_as_uint(acc[8][m][e]);
                auto r = __builtin_amdgcn_permlane32_swap(mine, mine, false, false);
                hv[e] = fmaxf(__uint_as_float(r[0]), 0.f) * __uint_as_float(r[1]);
            }
            u32x2 v; v[0] = pk_bf16(hv[0], hv[1]); v[1] = pk_bf16(hv[2], hv[3]);
            const int blk = 128 + (j >> 1), chunk = (j & 1) * 2 + wn;
            const unsigned off = g < 2 ? (unsigned)(blk * (int)(BLK_ELEMS * 2) + blk_off(row, chunk) + g * 8) : 0xfffffff0u;
            __builtin_amdgcn_raw_buffer_store_b64(v, rh, off, 0, HST_AUX);
        }
        SCHED();
    };
    hs(0, 0);
    BAR();
    STAMP(1, 2);
    LAS char* qt = lds;
    {
#pragma unroll
        for (int u = 0; u < 2; ++u) {
            const int nn = 16 * (2 * wn + (u ^ (wm & 1))) + 4 * g;
            const int h0 = nn / 18, h1 = (nn + 2) / 18;
            const int o0 = h0 * 48 + (nn - h0 * 18) * 2, o1 = h1 * 48 + (nn + 2 - h1 * 18) * 2;
#pragma unroll
            for (int m = 0; m < 4; ++m) {
                LAS char* rp = qt + (16 + 64 * wm + 16 * m + fr) * QS;
                if (h0 < 3) *(LAS unsigned*)(rp + o0) = pk_bf16(acc[9 + u][m][0], acc[9 + u][m][1]);
                if (h1 < 3) *(LAS unsigned*)(rp + o1) = pk_bf16(acc[9 + u][m][2], acc[9 + u][m][3]);
            }
            if (u == 0 && (w == 0 || w == 2 || w == 1)) {
                LAS char* rp = qt + fr * QS;
                if (h0 < 3) *(LAS unsigned*)(rp + o0) = pk_bf16(ach[0], ach[1]);
                if (h1 < 3) *(LAS unsigned*)(rp + o1) = pk_bf16(ach[2], ach[3]);
            }
            if (u == 0) hs(0, 1);
        }
    }
    asm volatile("s_waitcnt lgkmcnt(0)" ::: "memory");
    BAR();
    STAMP(1, 3);
    {
        const int s = tid >> 1, half = tid & 1;
        const int lrow = 16 + s, grow = 256 * mt + s;
        auto ld18 = [&](LAS const char* p, unsigned (&v)[9]) {
            const u32x4 a = *(LAS const u32x4*)p, c = *(LAS const u32x4*)(p + 16);
            v[0] = a[0]; v[1] = a[1]; v[2] = a[2]; v[3] = a[3]; v[4] = c[0]; v[5] = c[1]; v[6] = c[2]; v[7] = c[3];
            v[8] = *(LAS const unsigned*)(p + 32);
        };
        hs(0, 2);
        unsigned qv[9];
        ld18(qt + lrow * QS + 96, qv);
        float lg[4];
#pragma unroll
        for (int ww = 0; ww < 4; ++ww) {
            const int wi = 4 * half + ww;
            unsigned kv[9];
            ld18(qt + (lrow - 2 * wi) * QS, kv);
            float a = 0.f;
#pragma unroll
            for (int i = 0; i < 9; ++i) a = __builtin_amdgcn_fdot2_f32_bf16(__builtin_bit_cast(bf16x2_t, qv[i]), __builtin_bit_cast(bf16x2_t, kv[i]), a, false);
            a = rbf(rbf(a) * 0.23570226039551584f);
            lg[ww] = (grow - 2 * wi >= 0) ? a : -1e9f;
            if (ww == 0) hs(1, 0);
            if (ww == 2) hs(1, 1);
        }
        float mx = fmaxf(fmaxf(lg[0], lg[1]), fmaxf(lg[2], lg[3]));
        mx = fmaxf(mx, __shfl_xor(mx, 1));
        float ex[4], sum = 0.f;
#pragma unroll
        for (int ww = 0; ww < 4; ++ww) { ex[ww] = __expf(lg[ww] - mx); sum += ex[ww]; }
        sum += __shfl_xor(sum, 1);
        const float inv = 1.0f / sum;
        hs(1, 2);
        float o[18];
#pragma unroll
        for (int i = 0; i < 18; ++i) o[i] = 0.f;
#pragma unroll
        for (int ww = 0; ww < 4; ++ww) {
            const int wi = 4 * half + ww;
            const float pw = (grow - 2 * wi >= 0) ? rbf(ex[ww] * inv) : 0.f;
            unsigned vv[9];
            ld18(qt + (lrow - 2 * wi) * QS + 48, vv);
#pragma unroll
            for (int i = 0; i < 9; ++i) { o[2 * i] += pw * bf_lo(vv[i]); o[2 * i + 1] += pw * bf_hi(vv[i]); }
            if (ww < 3) hs(2, ww);
        }
        hs(3, 0);
#pragma unroll
        for (int ii = 0; ii < 5; ++ii) {
            const float s0 = half ? o[2 * ii] : o[(10 + 2 * ii) % 18], s1 = half ? o[2 * ii + 1] : o[(11 + 2 * ii) % 18];
            const float r0 = __shfl_xor(s0, 1), r1 = __shfl_xor(s1, 1);
            const float m0 = half ? o[(10 + 2 * ii) % 18] : o[2 * ii], m1 = half ? o[(11 + 2 * ii) % 18] : o[2 * ii + 1];
            const int i = half * 5 + ii;
            if (i < 9) {
                const int col = FF + 18 * j + 2 * i;
                *(unsigned*)((char*)hb + (size_t)(col >> 5) * (BLK_ELEMS * 2) + blk_off(grow, (col >> 3) & 3) + (col & 7) * 2) = pk_bf16(m0 + r0, m1 + r1);
            }
            if (ii == 2) hs(3, 1);
        }
        hs(3, 2);
    }
    STAMP(1, 15);
}

__global__ __launch_bounds__(512, 2) void k1_gateup(const u16* __restrict__ xnb, const u16* __restrict__ Wg,
                                                    const u16* __restrict__ Wu, const u16* __restrict__ wqh, const u16* __restrict__ gx,
                                                    const u16* __restrict__ Wd, const u16* __restrict__ wo, u16* __restrict__ hb, unsigned* __restrict__ cnt) {
    extern __shared__ __attribute__((aligned(16))) char smem[];
    LAS char* lds = (LAS char*)smem;
    const int tid = threadIdx.x, lane = tid & 63;
    if (blockIdx.x < 64 && tid == 0) cnt[blockIdx.x * 64] = 0u;
    const int w = __builtin_amdgcn_readfirstlane(tid >> 6);
    const int b = blockIdx.x, xcd = b & 7, idx = b >> 3;
    const int j = 4 * xcd + (idx & 3), mt = idx >> 2;
    const int wm = w >> 1, wn = w & 1;
    const unsigned lds0 = (unsigned)(size_t)lds;
    const int fr = lane & 15, g = lane >> 4, q4 = (lane >> 2) & 3, p4 = lane & 3;

    f32x4 acc[11][4], ach;
    {
        const unsigned srcA = (unsigned)((256 * mt + 32 * w) * 32 + lane * 8) * 2u;
        const int koct = w >> 1, mat = w & 1;
        const u16* W = mat ? Wu : Wg;
        unsigned srcB, srcX;
        const u16* baseX; int strideX; unsigned dX;
        {
            const int r = lane >> 3, swz = ((r >> 1) & 1) | ((koct & 1) << 1), c = (lane & 7) ^ (swz << 1);
            srcB = (unsigned)((8 * koct + r) * FF + 128 * j + 8 * c) * 2u;
            if (w < 2) {
                const int ko = lane >> 4, re = ((lane >> 1) & 7) ^ ((ko & 1) << 2), ce = (lane & 1) ^ w;
                srcX = (unsigned)(((w * 32 + j) * 576 + 8 * ko + re) * 16 + 8 * ce) * 2u;
                baseX = gx; strideX = 32 * 16; dX = lds0 + RE + w * 1024;
            } else if (w < 6) {
                const int ko = w - 2, swq = ((r >> 1) & 1) | ((ko & 1) << 1), cq = (lane & 7) ^ (swq << 1);
                srcX = (unsigned)((j * 576 + 8 * ko + r) * 64 + 8 * cq) * 2u;
                baseX = wqh; strideX = 32 * 64; dX = lds0 + RQ + ko * 1024;
            } else {
                const int hr = mt > 0 ? 256 * mt - 16 : 0;
                srcX = (unsigned)(hr * 64 + lane * 16);
                baseX = xnb; strideX = (int)BLK_ELEMS; dX = lds0 + RH;
            }
        }
        const int slotX = w < 2 ? 2048 : (w < 6 ? 4096 : 1024);
        const unsigned dA = lds0 + RA + (2 * w) * 1024, dB = lds0 + RB + (2 * w) * 1024;
        auto issue1 = [&](int t, int sl, int i) {
            const u16* ba = xnb + (size_t)t * BLK_ELEMS;
            const u16* bw = W + (size_t)t * 32 * FF;
            if (i == 0) glds16(bw, srcB, dB + sl * 16384);
            if (i == 1) glds16(bw + 64, srcB, dB + sl * 16384 + 1024);
            if (i == 2) glds16(baseX + (size_t)t * strideX, srcX, dX + sl * slotX);
            if (i == 3) glds16(ba, srcA, dA + sl * 16384);
            if (i == 4) glds16_o1k(ba, srcA, dA + sl * 16384);
        };
        auto prefetch = [&](int p) {
            const int split = xcd >> 1;
            const unsigned start = (unsigned)((split == 0 ? 0 : 1344 + 1280 * (split - 1)) * DM * 2);
            const unsigned len = (unsigned)((split == 0 ? 1344 : 1280) * DM * 2), lwd = (unsigned)(704 * DM * 2);
            p = p < 6 ? p : 5;
            unsigned o = (unsigned)((idx * 8 + w) * 6144 + p * 1024); o = o + 1024 <= len ? o : 0u;
            const char* src = (split == 3 && o >= lwd) ? (const char*)wo + (o - lwd) : (const char*)Wd + start + o;
            glds16(src, (unsigned)lane * 16u, lds0 + T_DUMMY);
        };
        STAMP(1, 0);
#pragma unroll
        for (int t = 0; t < 3; ++t) {
#pragma unroll
            for (int i = 0; i < 5; ++i) issue1(t, t, i);
            if (t == 1) prefetch(0);
        }

        const unsigned pa = lds0 + RA + blk_off(64 * wm + fr, g);
        const int swz = ((q4 >> 1) & 1) | ((g & 1) << 1);
        const unsigned pb0 = lds0 + RB + (g * 4 + wn) * 1024 + q4 * 128 + (swz << 5) + p4 * 8;
        const int emat = p4 >> 1;
        const unsigned pe0 = lds0 + RE + (emat * 4 + g) * 256 + ((q4 ^ ((g & 1) << 2)) * 32) + ((wn ^ emat) * 16) + (p4 & 1) * 8;
        const unsigned pq0 = lds0 + RQ + g * 1024 + q4 * 128 + (((2 * wn + (wm & 1)) ^ swz) << 5) + p4 * 8;
        LAS char* pbv[4]; LAS char* pqv[2];
#pragma unroll
        for (int u = 0; u < 4; ++u) pbv[u] = (LAS char*)(size_t)(pb0 ^ (u << 5));
        pqv[0] = (LAS char*)(size_t)pq0; pqv[1] = (LAS char*)(size_t)(pq0 ^ 32);
        LAS char* pev[2] = {(LAS char*)(size_t)pe0, (LAS char*)(size_t)(pe0 ^ 128)};
        LAS char* phv = lds + RH + blk_off(fr, g);
        auto rdB = [&](int sl, int nf, int t1) -> s16x4 {
            if (nf < 8) return tr_read(pbv[nf >> 1] + sl * 16384 + (nf & 1) * 2048 + t1 * 512);
            if (nf == 8) return tr_read(pev[t1] + sl * 2048);
            return tr_read(pqv[nf - 9] + sl * 4096 + t1 * 512);
        };
        auto rdA = [&](int sl, int m) -> bf16x8 { return row_frag((LAS char*)(size_t)pa + sl * 16384 + m * 1024); };
        auto rdH = [&](int sl) -> bf16x8 { return row_frag(phv + sl * 1024); };
#pragma unroll
        for (int n = 0; n < 11; ++n)
#pragma unroll
            for (int m = 0; m < 4; ++m) { acc[n][m] = (f32x4){0.f, 0.f, 0.f, 0.f}; asm volatile("" : "+v"(acc[n][m])); }
        ach = (f32x4){0.f, 0.f, 0.f, 0.f}; asm volatile("" : "+v"(ach));

        wait_vm<11>(); BAR();
        STAMP(1, 1);
        bf16x8 fa[2][2], fc[2], fh, fb[NB];
#pragma unroll
        for (int m = 0; m < 2; ++m) { fa[0][m] = rdA(0, m); fc[m] = rdA(0, 2 + m); }
#pragma unroll
        for (int i = 0; i < PF; ++i) { s16x4 lo = rdB(0, i, 0), hi = rdB(0, i, 1); fb[i] = __builtin_shufflevector(lo, hi, 0, 1, 2, 3, 4, 5, 6, 7); }
#define MM(C, A, B) mfma_ip(C, A, B)
        auto step = [&](auto S_, int t, bool do_issue, auto LAST1_, auto WAIT0_) {
            constexpr int S = decltype(S_)::value;
            constexpr bool has_next = !decltype(LAST1_)::value;
            constexpr bool wait0 = decltype(WAIT0_)::value;
            constexpr int sl = S, sn = (S + 1) & 3, cur = S & 1;
#pragma unroll
            for (int nf = 0; nf < 11; ++nf) {
                constexpr int dummy = 0; (void)dummy;
                const int r = 11 * S + nf;
                const int psl = (nf + PF < 11) ? sl : sn, nn = (nf + PF < 11) ? nf + PF : nf + PF - 11;
                const bool pf = (nf + PF < 11) || has_next;
                const bool lastg = (nf == 10);
                s16x4 lo, hi;
                if (!lastg) MM(acc[nf][0], fb[r % NB], fa[cur][0]); else MM(acc[nf][2], fb[r % NB], fc[0]);
                SCHED();
                if (nf == 0) {
                    if (!wait0) wait_vm<6>(); else wait_vm<0>();
                    BAR();
                }
                if (pf) lo = rdB(psl, nn, 0);
                SCHED();
                if (!lastg) MM(acc[nf][1], fb[r % NB], fa[cur][1]); else MM(acc[nf][3], fb[r % NB], fc[1]);
                SCHED();
                if (pf) hi = rdB(psl, nn, 1);
                if (lastg && has_next) fc[0] = rdA(sn, 2);
                SCHED();
                if (!lastg) MM(acc[nf][2], fb[r % NB], fc[0]); else MM(acc[nf][0], fb[r % NB], fa[cur][0]);
                SCHED();
                if (lastg && has_next) fc[1] = rdA(sn, 3);
                if (nf >= 4 && nf < 6 && has_next) fa[cur ^ 1][nf - 4] = rdA(sn, nf - 4);
                if (nf == 7) fh = rdH(sl);
                if ((nf < 4 || nf == 6) && do_issue) issue1(t + 3, (S + 3) & 3, nf < 4 ? nf : 4);
                if (nf == 8 && (S & 1) == 0 && do_issue) prefetch(1 + (t >> 1));
                SCHED();
                if (!lastg) MM(acc[nf][3], fb[r % NB], fc[1]); else MM(acc[nf][1], fb[r % NB], fa[cur][1]);
                SCHED();
                if (nf == 9) { MM(ach, fb[r % NB], fh); SCHED(); }
                if (pf) fb[(r + PF) % NB] = __builtin_shufflevector(lo, hi, 0, 1, 2, 3, 4, 5, 6, 7);
            }
        };
        using I0 = std::integral_constant<int, 0>; using I1 = std::integral_constant<int, 1>;
        using I2 = std::integral_constant<int, 2>; using I3 = std::integral_constant<int, 3>;
        using F = std::false_type; using T = std::true_type;
#pragma unroll 1
        for (int t0 = 0; t0 < 16; t0 += 4) {
            step(I0{}, t0, true, F{}, F{});
            step(I1{}, t0 + 1, true, F{}, F{});
            step(I2{}, t0 + 2, true, F{}, F{});
            step(I3{}, t0 + 3, t0 + 3 < 15, F{}, F{});
        }
        step(I0{}, 16, false, F{}, T{});
        step(I1{}, 17, false, T{}, T{});
    }
    asm volatile("s_nop 7\n\ts_nop 7" ::: "memory");
    STAMP(1, 4);
    k1_epilogue(acc, ach, lds, lds0, w, xcd, idx, j, mt, Wd, wo, hb);
}

constexpr int K2_NT = 21;
constexpr int K2_RB = 0, K2_RE = 65536, K2_LDS = 81920;
constexpr int NSPLIT = 4;
__device__ __forceinline__ void gload2(bf16x8& d0, bf16x8& d1, const void* sbase, unsigned voff) {
    sbase = uni(sbase);
#define AFLAG ""
    asm volatile("s_nop 4\n\tglobal_load_dwordx4 %0, %2, %3" AFLAG "\n\tglobal_load_dwordx4 %1, %2, %3 offset:1024" AFLAG
                 : "=&v"(d0), "=&v"(d1) : "v"(voff), "s"(sbase) : "memory");
}
__global__ __launch_bounds__(512, 2) void k2_down(const u16* __restrict__ hb, const u16* __restrict__ Wd,
                                                  const u16* __restrict__ wo, const u16* __restrict__ dx, u16* __restrict__ part, unsigned* __restrict__ cnt,
                                                  const u16* __restrict__ x, u16* __restrict__ out) {
    extern __shared__ __attribute__((aligned(16))) char smem[];
    LAS char* lds = (LAS char*)smem;
    const int tid = threadIdx.x, lane = tid & 63;
    const int w = __builtin_amdgcn_readfirstlane(tid >> 6);
    const int b = blockIdx.x, xcd = b & 7, idx = b >> 3;
    const int split = xcd >> 1, mt = (xcd & 1) * 8 + (idx >> 2), nt = idx & 3;
    const int wm = w >> 1, kh = w & 1;
    const int T0 = split == 0 ? 0 : 21 + 20 * (split - 1);

    const unsigned srcA = (unsigned)(kh * (int)BLK_ELEMS * 2 + blk_off(128 * mt + 32 * wm + (lane & 15), lane >> 4));
    const int koct = w >> 1, sub = w & 1;
    unsigned offB, offE;
    {
        const int r = lane >> 3, swz = ((r >> 1) & 1) | ((koct & 1) << 1), c = (lane & 7) ^ (swz << 1);
        offB = (unsigned)((32 * sub + 8 * koct + r) * DM + 128 * nt + 8 * c) * 2u;
        const int re = ((lane >> 1) & 7) ^ ((koct & 1) << 2), ce = (lane & 1);
        offE = (unsigned)((nt * 5184 + 32 * sub + 8 * koct + re) * 16 + 8 * ce) * 2u;
    }
    const unsigned lds0 = (unsigned)(size_t)lds;
    const unsigned dB = lds0 + K2_RB + (2 * w) * 1024, dE = lds0 + K2_RE + (sub * 4 + koct) * 256;
    bf16x8 fa[4][2];
    auto issue1 = [&](int t, int sl, int i, bf16x8& a0, bf16x8& a1) {
        int T = T0 + t; if (t == K2_NT - 1 && split != 0) T -= 1;
        const u16* Wt = T < 72 ? Wd + (size_t)T * 64 * DM : wo + (size_t)(T - 72) * 64 * DM;
        const u16* ba = hb + (size_t)T * 2 * BLK_ELEMS;
        if (i == 0) glds16(Wt, offB, dB + sl * 16384);
        if (i == 1) glds16(Wt + 64, offB, dB + sl * 16384 + 1024);
        if (i == 2) glds16_q(dx + (size_t)T * 64 * 16, offE, dE + sl * 2048);
        if (i == 3) gload2(a0, a1, ba, srcA);
    };
    STAMP(2, 0);
#pragma unroll
    for (int t = 0; t < 3; ++t)
#pragma unroll
        for (int i = 0; i < 4; ++i) issue1(t, t, i, fa[t][0], fa[t][1]);

    const int fr = lane & 15, g = lane >> 4, q4 = (lane >> 2) & 3, p4 = lane & 3;
    const int swz = ((q4 >> 1) & 1) | ((g & 1) << 1);
    LAS char* pb[4];
#pragma unroll
    for (int u = 0; u < 4; ++u) pb[u] = lds + K2_RB + (g * 4 + kh * 2) * 1024 + q4 * 128 + ((u ^ swz) << 5) + p4 * 8;
    LAS char* pe0 = lds + K2_RE + (kh * 4 + g) * 256 + ((q4 ^ ((g & 1) << 2)) * 32) + p4 * 8;
    LAS char* pe1 = lds + K2_RE + (kh * 4 + g) * 256 + (((4 + q4) ^ ((g & 1) << 2)) * 32) + p4 * 8;
    auto rdB = [&](int sl, int nf, int t1) -> s16x4 {
        if (nf < 8) return tr_read(pb[nf & 3] + sl * 16384 + (nf >> 2) * 1024 + t1 * 512);
        return tr_read((t1 ? pe1 : pe0) + sl * 2048);
    };

    f32x4 acc[9][2];
#pragma unroll
    for (int n = 0; n < 9; ++n)
#pragma unroll
        for (int m = 0; m < 2; ++m) { acc[n][m] = (f32x4){0.f, 0.f, 0.f, 0.f}; asm volatile("" : "+v"(acc[n][m])); }

    wait_vm<10>(); BAR();
    STAMP(2, 1);
    constexpr int PF2 = 4;
    bf16x8 fb[9];
#pragma unroll
    for (int i = 0; i < PF2; ++i) { s16x4 lo = rdB(0, i, 0), hi = rdB(0, i, 1); fb[i] = __builtin_shufflevector(lo, hi, 0, 1, 2, 3, 4, 5, 6, 7); }
    auto step = [&](auto S_, int t, bool do_issue, bool wait0, auto LAST_) {
        constexpr int S = decltype(S_)::value;
        constexpr bool has_next = !decltype(LAST_)::value;
        constexpr int sl = S, sn = (S + 1) & 3;
        asm volatile("" : "+v"(fa[sl][0]), "+v"(fa[sl][1]));
        if (!has_next && split != 0) {
#pragma unroll
            for (int m = 0; m < 2; ++m) fa[sl][m] = (bf16x8){0, 0, 0, 0, 0, 0, 0, 0};
            asm volatile("s_nop 1" : "+v"(fa[sl][0]), "+v"(fa[sl][1]));
        }
#pragma unroll
        for (int nf = 0; nf < 9; ++nf) {
            const int psl = (nf + PF2 < 9) ? sl : sn, nn = (nf + PF2 < 9) ? nf + PF2 : nf + PF2 - 9;
            const bool pf = (nf + PF2 < 9) || has_next;
            s16x4 lo, hi;
            mfma_ip(acc[nf][0], fb[nf], fa[sl][0]);
            SCHED();
            if (nf == 0) {
                if (!wait0) wait_vm<5>(); else wait_vm<0>();
                BAR();
            }
            if (pf) lo = rdB(psl, nn, 0);
            SCHED();
            mfma_ip(acc[nf][1], fb[nf], fa[sl][1]);
            SCHED();
            if (pf) hi = rdB(psl, nn, 1);
            if (nf >= 1 && nf < 5 && do_issue) issue1(t + 3, (S + 3) & 3, nf - 1, fa[(S + 3) & 3][0], fa[(S + 3) & 3][1]);
            SCHED();
            if (pf) fb[nn] = __builtin_shufflevector(lo, hi, 0, 1, 2, 3, 4, 5, 6, 7);
        }
    };
    using I0 = std::integral_constant<int, 0>; using I1 = std::integral_constant<int, 1>;
    using I2 = std::integral_constant<int, 2>; using I3 = std::integral_constant<int, 3>;
#pragma unroll 1
    for (int t0 = 0; t0 < 20; t0 += 4) {
        step(I0{}, t0, true, false, std::false_type{});
        step(I1{}, t0 + 1, true, false, std::false_type{});
        step(I2{}, t0 + 2, t0 + 2 < 18, false, std::false_type{});
        step(I3{}, t0 + 3, t0 + 3 < 18, t0 + 3 == 19, std::false_type{});
    }
    step(I0{}, 20, false, true, std::true_type{});
    asm volatile("s_nop 7\n\ts_nop 7" ::: "memory");
    BAR();
    STAMP(2, 2);
    f32x4 val[10];
    {
        LAS char* xw = lds + w * 10240;
        LAS char* xr = lds + (w ^ 1) * 10240;
#pragma unroll
        for (int n = 0; n < 9; ++n) {
            const bool give = kh ? (n < 5) : (n >= 5);
            if (give) {
#pragma unroll
                for (int m = 0; m < 2; ++m) *(LAS f32x4*)(xw + ((kh ? n : n - 5) * 2 + m) * 1024 + lane * 16) = acc[n][m];
            }
        }
        asm volatile("s_waitcnt lgkmcnt(0)" ::: "memory");
        BAR();
#pragma unroll
        for (int i = 0; i < 10; ++i) {
            const f32x4 mine = kh ? acc[(5 + (i >> 1)) < 9 ? 5 + (i >> 1) : 8][i & 1] : acc[i >> 1][i & 1];
            val[i] = mine + *(LAS f32x4*)(xr + i * 1024 + lane * 16);
        }
    }
    {
        const int nit = kh ? 8 : 10;
        const int grp = mt * 4 + nt;
        const __amdgpu_buffer_rsrc_t rp = __builtin_amdgcn_make_buffer_rsrc((void*)part, 0, NSPLIT * 64 * 8 * 10 * 512, 0x00020000);
        const unsigned slot = (unsigned)((grp * 8 + w) * 10 * 512 + lane * 8);
        constexpr unsigned SPL = 64 * 8 * 10 * 512;
        u32x2 xv[3]; unsigned xoff[3];
#pragma unroll
        for (int q = 0; q < 3; ++q) {
            int i = split + 4 * q; i = i < nit ? i : split;
            const int n = kh ? 5 + (i >> 1) : (i >> 1), m = i & 1;
            const int row = 128 * mt + 32 * wm + 16 * m + fr;
            const int col = (n < 8 ? 128 * nt + 16 * n : 512 + 16 * nt) + 4 * g;
            xoff[q] = (unsigned)(row * DM + col);
            xv[q] = *(const u32x2*)(x + xoff[q]);
        }
#pragma unroll
        for (int i = 0; i < 10; ++i) {
            if (i < nit && (i & 3) != split) {
                u32x2 v; v[0] = pk_bf16(val[i][0], val[i][1]); v[1] = pk_bf16(val[i][2], val[i][3]);
                __builtin_amdgcn_raw_buffer_store_b64(v, rp, slot + split * SPL + i * 512, 0, 16);
            }
        }
        STAMP(2, 3);
        asm volatile("s_waitcnt vmcnt(0)" ::: "memory");
        BAR();
        STAMP(2, 4);
        unsigned* c = cnt + grp * 64;
        if (tid == 0) {
            __hip_atomic_fetch_add(c, 1u, __ATOMIC_RELAXED, __HIP_MEMORY_SCOPE_AGENT);
            unsigned spins = 0;
            while (__hip_atomic_load(c, __ATOMIC_RELAXED, __HIP_MEMORY_SCOPE_AGENT) < (unsigned)NSPLIT) {
                __builtin_amdgcn_s_sleep(1);
                if (++spins > (1u << 22)) break;
            }
        }
        BAR();
        STAMP(2, 5);
        u32x2 pv[3][3];
#pragma unroll
        for (int q = 0; q < 3; ++q) {
            const int i = split + 4 * q;
#pragma unroll
            for (int s2 = 1; s2 < NSPLIT; ++s2) {
                const int so = (split + s2) & 3;
                const unsigned off = i < nit ? slot + so * SPL + i * 512 : 0xfffffff0u;
                pv[q][s2 - 1] = __builtin_amdgcn_raw_buffer_load_b64(rp, off, 0, 16);
            }
        }
#pragma unroll
        for (int q = 0; q < 3; ++q) {
            const int i = split + 4 * q;
            if (i < nit) {
                f32x4 o = val[0];
#pragma unroll
                for (int ii = 0; ii < 10; ++ii) if (ii == i) o = val[ii];
#pragma unroll
                for (int s2 = 0; s2 < 3; ++s2) { o[0] += bf_lo(pv[q][s2][0]); o[1] += bf_hi(pv[q][s2][0]); o[2] += bf_lo(pv[q][s2][1]); o[3] += bf_hi(pv[q][s2][1]); }
                u32x2 v; v[0] = pk_bf16(o[0] + bf_lo(xv[q][0]), o[1] + bf_hi(xv[q][0])); v[1] = pk_bf16(o[2] + bf_lo(xv[q][1]), o[3] + bf_hi(xv[q][1]));
                *(u32x2*)(out + xoff[q]) = v;
            }
        }
    }
    STAMP(2, 15);
}

extern "C" void kernel_launch(void* const* d_in, const int* in_sizes, int n_in, void* d_out, int out_size, void* d_ws,
                              size_t ws_size, hipStream_t stream) {
    const u16* x = (const u16*)d_in[0];
    const u16* wqkv = (const u16*)d_in[1];
    const u16* wo = (const u16*)d_in[2];
    const u16* Wg = (const u16*)d_in[4];
    const u16* Wu = (const u16*)d_in[5];
    const u16* Wd = (const u16*)d_in[6];
    char* ws = (char*)d_ws;
    u16* xnb = (u16*)ws;
    u16* hb = (u16*)(ws + WS_HB);
    u16* part = (u16*)(ws + WS_PART);
    u16* wqh = (u16*)(ws + WS_WQH);
    u16* gx = (u16*)(ws + WS_GX);
    u16* dx = (u16*)(ws + WS_DX);
    unsigned* cnt = (unsigned*)(ws + WS_CNT);
    (void)hipFuncSetAttribute((const void*)k1_gateup, hipFuncAttributeMaxDynamicSharedMemorySize, K1_LDS);
    (void)hipFuncSetAttribute((const void*)k2_down, hipFuncAttributeMaxDynamicSharedMemorySize, K2_LDS);
    k0_ln<<<256, 256, 0, stream>>>(x, xnb, wqkv, wqh, Wg, Wu, gx, Wd, wo, dx, (const char*)ws, (const char*)d_out);
    k1_gateup<<<256, 512, K1_LDS, stream>>>(xnb, Wg, Wu, wqh, gx, Wd, wo, hb, cnt);
    k2_down<<<256, 512, K2_LDS, stream>>>(hb, Wd, wo, dx, part, cnt, x, (u16*)d_out);
}
```

```cpp
#include <hip/hip_runtime.h>
#include <hip/hip_bf16.h>
#include <stdint.h>
#include <type_traits>

typedef unsigned short u16;
typedef short bf16x8 __attribute__((ext_vector_type(8)));
typedef short s16x4 __attribute__((ext_vector_type(4)));
typedef float f32x4 __attribute__((ext_vector_type(4)));
typedef float f32x2 __attribute__((ext_vector_type(2)));
typedef __bf16 bf16x2_t __attribute__((ext_vector_type(2)));
typedef unsigned u32x4 __attribute__((ext_vector_type(4)));
typedef unsigned u32x2 __attribute__((ext_vector_type(2)));
#define LAS __attribute__((address_space(3)))
#define GAS __attribute__((address_space(1)))

constexpr int SEQ = 2048, DM = 576, FF = 4608;
constexpr int NKB_X = 18;
constexpr int NKB_H = 162;
constexpr size_t BLK_ELEMS = (size_t)SEQ * 32;

constexpr size_t WS_HB = 18 * 131072, WS_PART = WS_HB + 162 * 131072, WS_WQH = WS_PART + 4 * 64 * 8 * 10 * 512, WS_GX = WS_WQH + 32 * 576 * 64 * 2,
                 WS_DX = WS_GX + 2 * 32 * 576 * 16 * 2, WS_CNT = WS_DX + 4 * 5184 * 16 * 2, WS_END = WS_CNT + 64 * 256;

__device__ __forceinline__ unsigned pk_bf16(float lo, float hi) {
    f32x2 v = {lo, hi};
    bf16x2_t b = __builtin_convertvector(v, bf16x2_t);
    return __builtin_bit_cast(unsigned, b);
}
__device__ __forceinline__ float bf_lo(unsigned u) { return __uint_as_float(u << 16); }
__device__ __forceinline__ float bf_hi(unsigned u) { return __uint_as_float(u & 0xffff0000u); }
__device__ __forceinline__ float rbf(float x) { return bf_lo(pk_bf16(x, x)); }

__device__ __forceinline__ const void* uni(const void* p) {
    const unsigned long long v = (unsigned long long)p;
    const unsigned lo = __builtin_amdgcn_readfirstlane((unsigned)v), hi = __builtin_amdgcn_readfirstlane((unsigned)(v >> 32));
    return (const void*)(((unsigned long long)hi << 32) | lo);
}
__device__ __forceinline__ void glds16(const void* sbase, unsigned voff, unsigned lds) {
    sbase = uni(sbase); lds = __builtin_amdgcn_readfirstlane(lds);
    asm volatile("s_mov_b32 m0, %2\n\ts_nop 3\n\tglobal_load_lds_dwordx4 %0, %1"
                 :: "v"(voff), "s"(sbase), "s"(lds) : "memory", "m0");
}
__device__ __forceinline__ void glds16_o1k(const void* sbase, unsigned voff, unsigned lds) {
    sbase = uni(sbase); lds = __builtin_amdgcn_readfirstlane(lds);
    asm volatile("s_mov_b32 m0, %2\n\ts_nop 3\n\tglobal_load_lds_dwordx4 %0, %1 offset:1024"
                 :: "v"(voff), "s"(sbase), "s"(lds) : "memory", "m0");
}
__device__ __forceinline__ void glds16_q(const void* sbase, unsigned voff, unsigned lds) {
    sbase = uni(sbase); lds = __builtin_amdgcn_readfirstlane(lds);
    unsigned sv; unsigned long long ex;
    asm volatile("s_mov_b32 %0, m0\n\ts_mov_b64 %1, exec\n\ts_mov_b32 m0, %4\n\ts_mov_b64 exec, 0xffff\n\ts_nop 1\n\tglobal_load_lds_dwordx4 %2, %3\n\ts_mov_b64 exec, %1\n\ts_mov_b32 m0, %0"
                 : "=&s"(sv), "=&s"(ex) : "v"(voff), "s"(sbase), "s"(lds) : "memory");
}
#define SCHED() __builtin_amdgcn_sched_barrier(0)
__device__ __forceinline__ s16x4 tr_read(LAS char* p) {
    return __builtin_bit_cast(s16x4, __builtin_amdgcn_ds_read_tr16_b64_v4i16((LAS s16x4*)p));
}
__device__ __forceinline__ bf16x8 row_frag(LAS char* p) { return *(LAS bf16x8*)p; }
__device__ __forceinline__ void mfma_ip(f32x4& c, bf16x8 a, bf16x8 b) { asm volatile("v_mfma_f32_16x16x32_bf16 %0, %1, %2, %0" : "+v"(c) : "v"(a), "v"(b)); }
#define BAR() do { asm volatile("" ::: "memory"); __builtin_amdgcn_s_barrier(); asm volatile("" ::: "memory"); } while (0)

__device__ __forceinline__ int blk_off(int row, int chunk) { return row * 64 + ((chunk ^ (((row >> 3) & 1) << 1)) << 4); }

__global__ __launch_bounds__(256) void k0_ln(const u16* __restrict__ x, u16* __restrict__ xnb,
                                            const u16* __restrict__ wqkv, u16* __restrict__ wqh,
                                            const u16* __restrict__ Wg, const u16* __restrict__ Wu, u16* __restrict__ gx,
                                            const u16* __restrict__ Wd, const u16* __restrict__ wo, u16* __restrict__ dx) {
    if (threadIdx.x >= 64) {
        const int l = threadIdx.x - 64, head = blockIdx.x >> 3, k0 = (blockIdx.x & 7) * 72;
        unsigned v[12];
#pragma unroll
        for (int i = 0; i < 12; ++i) {
            const int id = l + 192 * i, k = k0 + (id >> 5), cp = id & 31;
            const int part = cp >= 18 ? 2 : (cp >= 9 ? 1 : 0), d2 = cp - part * 9;
            const int src = (part == 2 ? 0 : part + 1) * DM + 18 * head + 2 * d2;
            v[i] = cp < 27 ? *(const unsigned*)(wqkv + (size_t)k * (3 * DM) + src) : 0u;
        }
        u32x4 c[3];
#pragma unroll
        for (int i = 0; i < 2; ++i) {
            const int id = l + 192 * i;
            if (id < 288) {
                const int gid = blockIdx.x * 288 + id, mat = gid / 36864, r = gid - mat * 36864, k = r >> 6, ch = r & 63;
                c[i] = *(const u32x4*)((mat ? Wu : Wg) + (size_t)k * FF + 4096 + 8 * ch);
            }
        }
        if (l < 162) {
            const int gid = blockIdx.x * 162 + l, k = gid >> 3, ch = gid & 7;
            c[2] = *(const u32x4*)((k < FF ? Wd + (size_t)k * DM : wo + (size_t)(k - FF) * DM) + 512 + 8 * ch);
        }
#pragma unroll
        for (int i = 0; i < 12; ++i) {
            const int id = l + 192 * i;
            *(unsigned*)(wqh + ((size_t)head * 576 + k0) * 64 + 2 * id) = v[i];
        }
#pragma unroll
        for (int i = 0; i < 2; ++i) {
            const int id = l + 192 * i;
            if (id < 288) {
                const int gid = blockIdx.x * 288 + id, mat = gid / 36864, r = gid - mat * 36864, k = r >> 6, ch = r & 63;
                *(u32x4*)(gx + (((size_t)mat * 32 + (ch >> 1)) * 576 + k) * 16 + 8 * (ch & 1)) = c[i];
            }
        }
        if (l < 162) {
            const int gid = blockIdx.x * 162 + l, k = gid >> 3, ch = gid & 7;
            *(u32x4*)(dx + ((size_t)(ch >> 1) * 5184 + k) * 16 + 8 * (ch & 1)) = c[2];
        }
        if (blockIdx.x < 8) {
            unsigned long long pc; int o1, o2;
            asm volatile("s_getpc_b64 %0\n.Lcpf%=:\n\ts_mov_b32 %1, _Z9k1_gateupPKtS0_S0_S0_S0_S0_S0_PtPj-.Lcpf%=\n\ts_mov_b32 %2, _Z7k2_downPKtS0_S0_S0_PtPjS0_S1_-.Lcpf%="
                         : "=s"(pc), "=s"(o1), "=s"(o2));
            const char* c1 = (const char*)(pc + o1);
            const char* c2 = (const char*)(pc + o2);
            unsigned a0, a1;
            const char* p1 = c1 + (size_t)l * 64;
            const char* p2 = c2 + (size_t)(l < 96 ? l : 0) * 64;
            asm volatile("global_load_dword %0, %2, off\n\tglobal_load_dword %1, %3, off\n\ts_waitcnt vmcnt(0)" : "=&v"(a0), "=&v"(a1) : "v"(p1), "v"(p2) : "memory");
        }
        return;
    }
    const int lane = threadIdx.x, i = lane & 7, row = blockIdx.x * 8 + (lane >> 3);
    u32x4 xv[9];
#pragma unroll
    for (int cc = 0; cc < 9; ++cc) xv[cc] = *(const u32x4*)(x + (size_t)row * DM + (i + 8 * cc) * 8);
    float s = 0.f;
#pragma unroll
    for (int cc = 0; cc < 9; ++cc)
#pragma unroll
        for (int e = 0; e < 4; ++e) s += bf_lo(xv[cc][e]) + bf_hi(xv[cc][e]);
    s += __shfl_xor(s, 1); s += __shfl_xor(s, 2); s += __shfl_xor(s, 4);
    const float mean = s * (1.0f / 576.0f);
    float q = 0.f;
#pragma unroll
    for (int cc = 0; cc < 9; ++cc)
#pragma unroll
        for (int e = 0; e < 4; ++e) { const float a = bf_lo(xv[cc][e]) - mean, c2 = bf_hi(xv[cc][e]) - mean; q += a * a + c2 * c2; }
    q += __shfl_xor(q, 1); q += __shfl_xor(q, 2); q += __shfl_xor(q, 4);
    const float mb = rbf(mean), vb = rbf(q * (1.0f / 576.0f));
    const float sb = rbf(sqrtf(rbf(vb + 1e-5f)));
    const float rs = 1.0f / sb;
#pragma unroll
    for (int cc = 0; cc < 9; ++cc) {
        u32x4 o;
#pragma unroll
        for (int e = 0; e < 4; ++e) {
            const unsigned d = pk_bf16(bf_lo(xv[cc][e]) - mb, bf_hi(xv[cc][e]) - mb);
            o[e] = pk_bf16(bf_lo(d) * rs, bf_hi(d) * rs);
        }
        const int blk = 2 * cc + (i >> 2);
        *(u32x4*)((char*)xnb + (size_t)blk * (BLK_ELEMS * 2) + blk_off(row, i & 3)) = o;
    }
}

constexpr int RA = 0, RB = 65536, RE = 131072, RQ = 139264, RH = 155648;
constexpr int T_DUMMY = 159744;
constexpr int K1_LDS = T_DUMMY + 1024;
constexpr int QS = 144;
constexpr int HST_AUX = 16;
constexpr int PF = 3;
constexpr int NB = PF + 1;
template <int N> __device__ __forceinline__ void wait_vm() { asm volatile("s_waitcnt vmcnt(%0)" :: "i"(N) : "memory"); }

__device__ __forceinline__ void k1_epilogue(f32x4 (&acc)[11][4], f32x4& ach, LAS char* lds, unsigned lds0, int w, int xcd, int idx,
                                            int j, int mt, const u16* __restrict__ Wd, const u16* __restrict__ wo, u16* __restrict__ hb) {
    int tid = threadIdx.x; asm volatile("" : "+v"(tid));
    const int lane = tid & 63, fr = lane & 15, g = lane >> 4;
    const int wm = w >> 1, wn = w & 1;
    const __amdgpu_buffer_rsrc_t rh = __builtin_amdgcn_make_buffer_rsrc((void*)hb, 0, (int)(NKB_H * BLK_ELEMS * 2), 0x00020000);
    auto hs = [&](int m, int part) {
        const int row = 256 * mt + 64 * wm + 16 * m + fr;
        SCHED();
        if (part < 2) {
            const int pp = part;
            unsigned v[2][2];
#pragma unroll
            for (int hh = 0; hh < 2; ++hh) {
                const int gt = 2 * pp + hh;
                float hv[4];
#pragma unroll
                for (int e = 0; e < 4; ++e) hv[e] = fmaxf(acc[2 * gt][m][e], 0.f) * acc[2 * gt + 1][m][e];
                v[hh][0] = pk_bf16(hv[0], hv[1]); v[hh][1] = pk_bf16(hv[2], hv[3]);
            }
            auto r0 = __builtin_amdgcn_permlane16_swap(v[0][0], v[1][0], false, false);
            auto r1 = __builtin_amdgcn_permlane16_swap(v[0][1], v[1][1], false, false);
            u32x4 o; o[0] = r0[0]; o[1] = r1[0]; o[2] = r0[1]; o[3] = r1[1];
            const int blk = 4 * j + 2 * wn + pp, chunk = ((g & 1) << 1) | (g >> 1);
            __builtin_amdgcn_raw_buffer_store_b128(o, rh, (unsigned)(blk * (int)(BLK_ELEMS * 2) + blk_off(row, chunk)), 0, HST_AUX);
        } else {
            float hv[4];
#pragma unroll
            for (int e = 0; e < 4; ++e) {
                const unsigned mine = __float_as_uint(acc[8][m][e]);
                auto r = __builtin_amdgcn_permlane32_swap(mine, mine, false, false);
                hv[e] = fmaxf(__uint_as_float(r[0]), 0.f) * __uint_as_float(r[1]);
            }
            u32x2 v; v[0] = pk_bf16(hv[0], hv[1]); v[1] = pk_bf16(hv[2], hv[3]);
            const int blk = 128 + (j >> 1), chunk = (j & 1) * 2 + wn;
            const unsigned off = g < 2 ? (unsigned)(blk * (int)(BLK_ELEMS * 2) + blk_off(row, chunk) + g * 8) : 0xfffffff0u;
            __builtin_amdgcn_raw_buffer_store_b64(v, rh, off, 0, HST_AUX);
        }
        SCHED();
    };
    hs(0, 0);
    BAR();
    LAS char* qt = lds;
    {
#pragma unroll
        for (int u = 0; u < 2; ++u) {
            const int nn = 16 * (2 * wn + (u ^ (wm & 1))) + 4 * g;
            const int h0 = nn / 18, h1 = (nn + 2) / 18;
            const int o0 = h0 * 48 + (nn - h0 * 18) * 2, o1 = h1 * 48 + (nn + 2 - h1 * 18) * 2;
#pragma unroll
            for (int m = 0; m < 4; ++m) {
                LAS char* rp = qt + (16 + 64 * wm + 16 * m + fr) * QS;
                if (h0 < 3) *(LAS unsigned*)(rp + o0) = pk_bf16(acc[9 + u][m][0], acc[9 + u][m][1]);
                if (h1 < 3) *(LAS unsigned*)(rp + o1) = pk_bf16(acc[9 + u][m][2], acc[9 + u][m][3]);
            }
            if (u == 0 && (w == 0 || w == 2 || w == 1)) {
                LAS char* rp = qt + fr * QS;
                if (h0 < 3) *(LAS unsigned*)(rp + o0) = pk_bf16(ach[0], ach[1]);
                if (h1 < 3) *(LAS unsigned*)(rp + o1) = pk_bf16(ach[2], ach[3]);
            }
            if (u == 0) hs(0, 1);
        }
    }
    asm volatile("s_waitcnt lgkmcnt(0)" ::: "memory");
    BAR();
    {
        const int s = tid >> 1, half = tid & 1;
        const int lrow = 16 + s, grow = 256 * mt + s;
        auto ld18 = [&](LAS const char* p, unsigned (&v)[9]) {
            const u32x4 a = *(LAS const u32x4*)p, c = *(LAS const u32x4*)(p + 16);
            v[0] = a[0]; v[1] = a[1]; v[2] = a[2]; v[3] = a[3]; v[4] = c[0]; v[5] = c[1]; v[6] = c[2]; v[7] = c[3];
            v[8] = *(LAS const unsigned*)(p + 32);
        };
        hs(0, 2);
        unsigned qv[9];
        ld18(qt + lrow * QS + 96, qv);
        float lg[4];
#pragma unroll
        for (int ww = 0; ww < 4; ++ww) {
            const int wi = 4 * half + ww;
            unsigned kv[9];
            ld18(qt + (lrow - 2 * wi) * QS, kv);
            float a = 0.f;
#pragma unroll
            for (int i = 0; i < 9; ++i) a = __builtin_amdgcn_fdot2_f32_bf16(__builtin_bit_cast(bf16x2_t, qv[i]), __builtin_bit_cast(bf16x2_t, kv[i]), a, false);
            a = rbf(rbf(a) * 0.23570226039551584f);
            lg[ww] = (grow - 2 * wi >= 0) ? a : -1e9f;
            if (ww == 0) hs(1, 0);
            if (ww == 2) hs(1, 1);
        }
        float mx = fmaxf(fmaxf(lg[0], lg[1]), fmaxf(lg[2], lg[3]));
        mx = fmaxf(mx, __shfl_xor(mx, 1));
        float ex[4], sum = 0.f;
#pragma unroll
        for (int ww = 0; ww < 4; ++ww) { ex[ww] = __expf(lg[ww] - mx); sum += ex[ww]; }
        sum += __shfl_xor(sum, 1);
        const float inv = 1.0f / sum;
        hs(1, 2);
        float o[18];
#pragma unroll
        for (int i = 0; i < 18; ++i) o[i] = 0.f;
#pragma unroll
        for (int ww = 0; ww < 4; ++ww) {
            const int wi = 4 * half + ww;
            const float pw = (grow - 2 * wi >= 0) ? rbf(ex[ww] * inv) : 0.f;
            unsigned vv[9];
            ld18(qt + (lrow - 2 * wi) * QS + 48, vv);
#pragma unroll
            for (int i = 0; i < 9; ++i) { o[2 * i] += pw * bf_lo(vv[i]); o[2 * i + 1] += pw * bf_hi(vv[i]); }
            if (ww < 3) hs(2, ww);
        }
        hs(3, 0);
#pragma unroll
        for (int ii = 0; ii < 5; ++ii) {
            const float s0 = half ? o[2 * ii] : o[(10 + 2 * ii) % 18], s1 = half ? o[2 * ii + 1] : o[(11 + 2 * ii) % 18];
            const float r0 = __shfl_xor(s0, 1), r1 = __shfl_xor(s1, 1);
            const float m0 = half ? o[(10 + 2 * ii) % 18] : o[2 * ii], m1 = half ? o[(11 + 2 * ii) % 18] : o[2 * ii + 1];
            const int i = half * 5 + ii;
            if (i < 9) {
                const int col = FF + 18 * j + 2 * i;
                *(unsigned*)((char*)hb + (size_t)(col >> 5) * (BLK_ELEMS * 2) + blk_off(grow, (col >> 3) & 3) + (col & 7) * 2) = pk_bf16(m0 + r0, m1 + r1);
            }
            if (ii == 2) hs(3, 1);
        }
        hs(3, 2);
    }
}

__global__ __launch_bounds__(512, 2) void k1_gateup(const u16* __restrict__ xnb, const u16* __restrict__ Wg,
                                                    const u16* __restrict__ Wu, const u16* __restrict__ wqh, const u16* __restrict__ gx,
                                                    const u16* __restrict__ Wd, const u16* __restrict__ wo, u16* __restrict__ hb, unsigned* __restrict__ cnt) {
    extern __shared__ __attribute__((aligned(16))) char smem[];
    LAS char* lds = (LAS char*)smem;
    const int tid = threadIdx.x, lane = tid & 63;
    if (blockIdx.x < 64 && tid == 0) cnt[blockIdx.x * 64] = 0u;
    const int w = __builtin_amdgcn_readfirstlane(tid >> 6);
    const int b = blockIdx.x, xcd = b & 7, idx = b >> 3;
    const int j = 4 * xcd + (idx & 3), mt = idx >> 2;
    const int wm = w >> 1, wn = w & 1;
    const unsigned lds0 = (unsigned)(size_t)lds;
    const int fr = lane & 15, g = lane >> 4, q4 = (lane >> 2) & 3, p4 = lane & 3;

    f32x4 acc[11][4], ach;
    {
        const unsigned srcA = (unsigned)((256 * mt + 32 * w) * 32 + lane * 8) * 2u;
        const int koct = w >> 1, mat = w & 1;
        const u16* W = mat ? Wu : Wg;
        unsigned srcB, srcX;
        const u16* baseX; int strideX; unsigned dX;
        {
            const int r = lane >> 3, swz = ((r >> 1) & 1) | ((koct & 1) << 1), c = (lane & 7) ^ (swz << 1);
            srcB = (unsigned)((8 * koct + r) * FF + 128 * j + 8 * c) * 2u;
            if (w < 2) {
                const int ko = lane >> 4, re = ((lane >> 1) & 7) ^ ((ko & 1) << 2), ce = (lane & 1) ^ w;
                srcX = (unsigned)(((w * 32 + j) * 576 + 8 * ko + re) * 16 + 8 * ce) * 2u;
                baseX = gx; strideX = 32 * 16; dX = lds0 + RE + w * 1024;
            } else if (w < 6) {
                const int ko = w - 2, swq = ((r >> 1) & 1) | ((ko & 1) << 1), cq = (lane & 7) ^ (swq << 1);
                srcX = (unsigned)((j * 576 + 8 * ko + r) * 64 + 8 * cq) * 2u;
                baseX = wqh; strideX = 32 * 64; dX = lds0 + RQ + ko * 1024;
            } else {
                const int hr = mt > 0 ? 256 * mt - 16 : 0;
                srcX = (unsigned)(hr * 64 + lane * 16);
                baseX = xnb; strideX = (int)BLK_ELEMS; dX = lds0 + RH;
            }
        }
        const int slotX = w < 2 ? 2048 : (w < 6 ? 4096 : 1024);
        const unsigned dA = lds0 + RA + (2 * w) * 1024, dB = lds0 + RB + (2 * w) * 1024;
        auto issue1 = [&](int t, int sl, int i) {
            const u16* ba = xnb + (size_t)t * BLK_ELEMS;
            const u16* bw = W + (size_t)t * 32 * FF;
            if (i == 0) glds16(bw, srcB, dB + sl * 16384);
            if (i == 1) glds16(bw + 64, srcB, dB + sl * 16384 + 1024);
            if (i == 2) glds16(baseX + (size_t)t * strideX, srcX, dX + sl * slotX);
            if (i == 3) glds16(ba, srcA, dA + sl * 16384);
            if (i == 4) glds16_o1k(ba, srcA, dA + sl * 16384);
        };
        auto prefetch = [&](int p) {
            const int split = xcd >> 1;
            const unsigned start = (unsigned)((split == 0 ? 0 : 1344 + 1280 * (split - 1)) * DM * 2);
            const unsigned len = (unsigned)((split == 0 ? 1344 : 1280) * DM * 2), lwd = (unsigned)(704 * DM * 2);
            p = p < 6 ? p : 5;
            unsigned o = (unsigned)((idx * 8 + w) * 6144 + p * 1024); o = o + 1024 <= len ? o : 0u;
            const char* src = (split == 3 && o >= lwd) ? (const char*)wo + (o - lwd) : (const char*)Wd + start + o;
            glds16(src, (unsigned)lane * 16u, lds0 + T_DUMMY);
        };
#pragma unroll
        for (int t = 0; t < 3; ++t) {
#pragma unroll
            for (int i = 0; i < 5; ++i) issue1(t, t, i);
            if (t == 1) prefetch(0);
        }

        const unsigned pa = lds0 + RA + blk_off(64 * wm + fr, g);
        const int swz = ((q4 >> 1) & 1) | ((g & 1) << 1);
        const unsigned pb0 = lds0 + RB + (g * 4 + wn) * 1024 + q4 * 128 + (swz << 5) + p4 * 8;
        const int emat = p4 >> 1;
        const unsigned pe0 = lds0 + RE + (emat * 4 + g) * 256 + ((q4 ^ ((g & 1) << 2)) * 32) + ((wn ^ emat) * 16) + (p4 & 1) * 8;
        const unsigned pq0 = lds0 + RQ + g * 1024 + q4 * 128 + (((2 * wn + (wm & 1)) ^ swz) << 5) + p4 * 8;
        LAS char* pbv[4]; LAS char* pqv[2];
#pragma unroll
        for (int u = 0; u < 4; ++u) pbv[u] = (LAS char*)(size_t)(pb0 ^ (u << 5));
        pqv[0] = (LAS char*)(size_t)pq0; pqv[1] = (LAS char*)(size_t)(pq0 ^ 32);
        LAS char* pev[2] = {(LAS char*)(size_t)pe0, (LAS char*)(size_t)(pe0 ^ 128)};
        LAS char* phv = lds + RH + blk_off(fr, g);
        auto rdB = [&](int sl, int nf, int t1) -> s16x4 {
            if (nf < 8) return tr_read(pbv[nf >> 1] + sl * 16384 + (nf & 1) * 2048 + t1 * 512);
            if (nf == 8) return tr_read(pev[t1] + sl * 2048);
            return tr_read(pqv[nf - 9] + sl * 4096 + t1 * 512);
        };
        auto rdA = [&](int sl, int m) -> bf16x8 { return row_frag((LAS char*)(size_t)pa + sl * 16384 + m * 1024); };
        auto rdH = [&](int sl) -> bf16x8 { return row_frag(phv + sl * 1024); };
#pragma unroll
        for (int n = 0; n < 11; ++n)
#pragma unroll
            for (int m = 0; m < 4; ++m) { acc[n][m] = (f32x4){0.f, 0.f, 0.f, 0.f}; asm volatile("" : "+v"(acc[n][m])); }
        ach = (f32x4){0.f, 0.f, 0.f, 0.f}; asm volatile("" : "+v"(ach));

        wait_vm<11>(); BAR();
        bf16x8 fa[2][2], fc[2], fh, fb[NB];
#pragma unroll
        for (int m = 0; m < 2; ++m) { fa[0][m] = rdA(0, m); fc[m] = rdA(0, 2 + m); }
#pragma unroll
        for (int i = 0; i < PF; ++i) { s16x4 lo = rdB(0, i, 0), hi = rdB(0, i, 1); fb[i] = __builtin_shufflevector(lo, hi, 0, 1, 2, 3, 4, 5, 6, 7); }
#define MM(C, A, B) mfma_ip(C, A, B)
        auto step = [&](auto S_, int t, bool do_issue, auto LAST1_, auto WAIT0_) {
            constexpr int S = decltype(S_)::value;
            constexpr bool has_next = !decltype(LAST1_)::value;
            constexpr bool wait0 = decltype(WAIT0_)::value;
            constexpr int sl = S, sn = (S + 1) & 3, cur = S & 1;
#pragma unroll
            for (int nf = 0; nf < 11; ++nf) {
                constexpr int dummy = 0; (void)dummy;
                const int r = 11 * S + nf;
                const int psl = (nf + PF < 11) ? sl : sn, nn = (nf + PF < 11) ? nf + PF : nf + PF - 11;
                const bool pf = (nf + PF < 11) || has_next;
                const bool lastg = (nf == 10);
                s16x4 lo, hi;
                if (!lastg) MM(acc[nf][0], fb[r % NB], fa[cur][0]); else MM(acc[nf][2], fb[r % NB], fc[0]);
                SCHED();
                if (nf == 0) {
                    if (!wait0) wait_vm<6>(); else wait_vm<0>();
                    BAR();
                }
                if (pf) lo = rdB(psl, nn, 0);
                SCHED();
                if (!lastg) MM(acc[nf][1], fb[r % NB], fa[cur][1]); else MM(acc[nf][3], fb[r % NB], fc[1]);
                SCHED();
                if (pf) hi = rdB(psl, nn, 1);
                if (lastg && has_next) fc[0] = rdA(sn, 2);
                SCHED();
                if (!lastg) MM(acc[nf][2], fb[r % NB], fc[0]); else MM(acc[nf][0], fb[r % NB], fa[cur][0]);
                SCHED();
                if (lastg && has_next) fc[1] = rdA(sn, 3);
                if (nf >= 4 && nf < 6 && has_next) fa[cur ^ 1][nf - 4] = rdA(sn, nf - 4);
                if (nf == 7) fh = rdH(sl);
                if ((nf < 4 || nf == 6) && do_issue) issue1(t + 3, (S + 3) & 3, nf < 4 ? nf : 4);
                if (nf == 8 && (S & 1) == 0 && do_issue) prefetch(1 + (t >> 1));
                SCHED();
                if (!lastg) MM(acc[nf][3], fb[r % NB], fc[1]); else MM(acc[nf][1], fb[r % NB], fa[cur][1]);
                SCHED();
                if (nf == 9) { MM(ach, fb[r % NB], fh); SCHED(); }
                if (pf) fb[(r + PF) % NB] = __builtin_shufflevector(lo, hi, 0, 1, 2, 3, 4, 5, 6, 7);
            }
        };
        using I0 = std::integral_constant<int, 0>; using I1 = std::integral_constant<int, 1>;
        using I2 = std::integral_constant<int, 2>; using I3 = std::integral_constant<int, 3>;
        using F = std::false_type; using T = std::true_type;
#pragma unroll 1
        for (int t0 = 0; t0 < 16; t0 += 4) {
            step(I0{}, t0, true, F{}, F{});
            step(I1{}, t0 + 1, true, F{}, F{});
            step(I2{}, t0 + 2, true, F{}, F{});
            step(I3{}, t0 + 3, t0 + 3 < 15, F{}, F{});
        }
        step(I0{}, 16, false, F{}, T{});
        step(I1{}, 17, false, T{}, T{});
    }
    asm volatile("s_nop 7\n\ts_nop 7" ::: "memory");
    k1_epilogue(acc, ach, lds, lds0, w, xcd, idx, j, mt, Wd, wo, hb);
}

constexpr int K2_NT = 21;
constexpr int K2_RB = 0, K2_RE = 65536, K2_LDS = 81920;
constexpr int NSPLIT = 4;
__device__ __forceinline__ void gload2(bf16x8& d0, bf16x8& d1, const void* sbase, unsigned voff) {
    sbase = uni(sbase);
    asm volatile("s_nop 4\n\tglobal_load_dwordx4 %0, %2, %3\n\tglobal_load_dwordx4 %1, %2, %3 offset:1024"
                 : "=&v"(d0), "=&v"(d1) : "v"(voff), "s"(sbase) : "memory");
}
__global__ __launch_bounds__(512, 2) void k2_down(const u16* __restrict__ hb, const u16* __restrict__ Wd,
                                                  const u16* __restrict__ wo, const u16* __restrict__ dx, u16* __restrict__ part, unsigned* __restrict__ cnt,
                                                  const u16* __restrict__ x, u16* __restrict__ out) {
    extern __shared__ __attribute__((aligned(16))) char smem[];
    LAS char* lds = (LAS char*)smem;
    const int tid = threadIdx.x, lane = tid & 63;
    const int w = __builtin_amdgcn_readfirstlane(tid >> 6);
    const int b = blockIdx.x, xcd = b & 7, idx = b >> 3;
    const int split = xcd >> 1, mt = (xcd & 1) * 8 + (idx >> 2), nt = idx & 3;
    const int wm = w >> 1, kh = w & 1;
    const int T0 = split == 0 ? 0 : 21 + 20 * (split - 1);

    const unsigned srcA = (unsigned)(kh * (int)BLK_ELEMS * 2 + blk_off(128 * mt + 32 * wm + (lane & 15), lane >> 4));
    const int koct = w >> 1, sub = w & 1;
    unsigned offB, offE;
    {
        const int r = lane >> 3, swz = ((r >> 1) & 1) | ((koct & 1) << 1), c = (lane & 7) ^ (swz << 1);
        offB = (unsigned)((32 * sub + 8 * koct + r) * DM + 128 * nt + 8 * c) * 2u;
        const int re = ((lane >> 1) & 7) ^ ((koct & 1) << 2), ce = (lane & 1);
        offE = (unsigned)((nt * 5184 + 32 * sub + 8 * koct + re) * 16 + 8 * ce) * 2u;
    }
    const unsigned lds0 = (unsigned)(size_t)lds;
    const unsigned dB = lds0 + K2_RB + (2 * w) * 1024, dE = lds0 + K2_RE + (sub * 4 + koct) * 256;
    bf16x8 fa[4][2];
    auto issue1 = [&](int t, int sl, int i, bf16x8& a0, bf16x8& a1) {
        int T = T0 + t; if (t == K2_NT - 1 && split != 0) T -= 1;
        const u16* Wt = T < 72 ? Wd + (size_t)T * 64 * DM : wo + (size_t)(T - 72) * 64 * DM;
        const u16* ba = hb + (size_t)T * 2 * BLK_ELEMS;
        if (i == 0) glds16(Wt, offB, dB + sl * 16384);
        if (i == 1) glds16(Wt + 64, offB, dB + sl * 16384 + 1024);
        if (i == 2) glds16_q(dx + (size_t)T * 64 * 16, offE, dE + sl * 2048);
        if (i == 3) gload2(a0, a1, ba, srcA);
    };
#pragma unroll
    for (int t = 0; t < 3; ++t)
#pragma unroll
        for (int i = 0; i < 4; ++i) issue1(t, t, i, fa[t][0], fa[t][1]);

    const int fr = lane & 15, g = lane >> 4, q4 = (lane >> 2) & 3, p4 = lane & 3;
    const int swz = ((q4 >> 1) & 1) | ((g & 1) << 1);
    LAS char* pb[4];
#pragma unroll
    for (int u = 0; u < 4; ++u) pb[u] = lds + K2_RB + (g * 4 + kh * 2) * 1024 + q4 * 128 + ((u ^ swz) << 5) + p4 * 8;
    LAS char* pe0 = lds + K2_RE + (kh * 4 + g) * 256 + ((q4 ^ ((g & 1) << 2)) * 32) + p4 * 8;
    LAS char* pe1 = lds + K2_RE + (kh * 4 + g) * 256 + (((4 + q4) ^ ((g & 1) << 2)) * 32) + p4 * 8;
    auto rdB = [&](int sl, int nf, int t1) -> s16x4 {
        if (nf < 8) return tr_read(pb[nf & 3] + sl * 16384 + (nf >> 2) * 1024 + t1 * 512);
        return tr_read((t1 ? pe1 : pe0) + sl * 2048);
    };

    f32x4 acc[9][2];
#pragma unroll
    for (int n = 0; n < 9; ++n)
#pragma unroll
        for (int m = 0; m < 2; ++m) { acc[n][m] = (f32x4){0.f, 0.f, 0.f, 0.f}; asm volatile("" : "+v"(acc[n][m])); }

    wait_vm<10>(); BAR();
    constexpr int PF2 = 4;
    bf16x8 fb[9];
#pragma unroll
    for (int i = 0; i < PF2; ++i) { s16x4 lo = rdB(0, i, 0), hi = rdB(0, i, 1); fb[i] = __builtin_shufflevector(lo, hi, 0, 1, 2, 3, 4, 5, 6, 7); }
    auto step = [&](auto S_, int t, bool do_issue, bool wait0, auto LAST_) {
        constexpr int S = decltype(S_)::value;
        constexpr bool has_next = !decltype(LAST_)::value;
        constexpr int sl = S, sn = (S + 1) & 3;
        asm volatile("" : "+v"(fa[sl][0]), "+v"(fa[sl][1]));
        if (!has_next && split != 0) {
#pragma unroll
            for (int m = 0; m < 2; ++m) fa[sl][m] = (bf16x8){0, 0, 0, 0, 0, 0, 0, 0};
            asm volatile("s_nop 1" : "+v"(fa[sl][0]), "+v"(fa[sl][1]));
        }
#pragma unroll
        for (int nf = 0; nf < 9; ++nf) {
            const int psl = (nf + PF2 < 9) ? sl : sn, nn = (nf + PF2 < 9) ? nf + PF2 : nf + PF2 - 9;
            const bool pf = (nf + PF2 < 9) || has_next;
            s16x4 lo, hi;
            mfma_ip(acc[nf][0], fb[nf], fa[sl][0]);
            SCHED();
            if (nf == 0) {
                if (!wait0) wait_vm<5>(); else wait_vm<0>();
                BAR();
            }
            if (pf) lo = rdB(psl, nn, 0);
            SCHED();
            mfma_ip(acc[nf][1], fb[nf], fa[sl][1]);
            SCHED();
            if (pf) hi = rdB(psl, nn, 1);
            if (nf >= 1 && nf < 5 && do_issue) issue1(t + 3, (S + 3) & 3, nf - 1, fa[(S + 3) & 3][0], fa[(S + 3) & 3][1]);
            SCHED();
            if (pf) fb[nn] = __builtin_shufflevector(lo, hi, 0, 1, 2, 3, 4, 5, 6, 7);
        }
    };
    using I0 = std::integral_constant<int, 0>; using I1 = std::integral_constant<int, 1>;
    using I2 = std::integral_constant<int, 2>; using I3 = std::integral_constant<int, 3>;
#pragma unroll 1
    for (int t0 = 0; t0 < 20; t0 += 4) {
        step(I0{}, t0, true, false, std::false_type{});
        step(I1{}, t0 + 1, true, false, std::false_type{});
        step(I2{}, t0 + 2, t0 + 2 < 18, false, std::false_type{});
        step(I3{}, t0 + 3, t0 + 3 < 18, t0 + 3 == 19, std::false_type{});
    }
    step(I0{}, 20, false, true, std::true_type{});
    asm volatile("s_nop 7\n\ts_nop 7" ::: "memory");
    BAR();
    f32x4 val[10];
    {
        LAS char* xw = lds + w * 10240;
        LAS char* xr = lds + (w ^ 1) * 10240;
#pragma unroll
        for (int n = 0; n < 9; ++n) {
            const bool give = kh ? (n < 5) : (n >= 5);
            if (give) {
#pragma unroll
                for (int m = 0; m < 2; ++m) *(LAS f32x4*)(xw + ((kh ? n : n - 5) * 2 + m) * 1024 + lane * 16) = acc[n][m];
            }
        }
        asm volatile("s_waitcnt lgkmcnt(0)" ::: "memory");
        BAR();
#pragma unroll
        for (int i = 0; i < 10; ++i) {
            const f32x4 mine = kh ? acc[(5 + (i >> 1)) < 9 ? 5 + (i >> 1) : 8][i & 1] : acc[i >> 1][i & 1];
            val[i] = mine + *(LAS f32x4*)(xr + i * 1024 + lane * 16);
        }
    }
    {
        const int nit = kh ? 8 : 10;
        const int grp = mt * 4 + nt;
        const __amdgpu_buffer_rsrc_t rp = __builtin_amdgcn_make_buffer_rsrc((void*)part, 0, NSPLIT * 64 * 8 * 10 * 512, 0x00020000);
        const unsigned slot = (unsigned)((grp * 8 + w) * 10 * 512 + lane * 8);
        constexpr unsigned SPL = 64 * 8 * 10 * 512;
        u32x2 xv[3]; unsigned xoff[3];
#pragma unroll
        for (int q = 0; q < 3; ++q) {
            int i = split + 4 * q; i = i < nit ? i : split;
            const int n = kh ? 5 + (i >> 1) : (i >> 1), m = i & 1;
            const int row = 128 * mt + 32 * wm + 16 * m + fr;
            const int col = (n < 8 ? 128 * nt + 16 * n : 512 + 16 * nt) + 4 * g;
            xoff[q] = (unsigned)(row * DM + col);
            xv[q] = *(const u32x2*)(x + xoff[q]);
        }
#pragma unroll
        for (int i = 0; i < 10; ++i) {
            if (i < nit && (i & 3) != split) {
                u32x2 v; v[0] = pk_bf16(val[i][0], val[i][1]); v[1] = pk_bf16(val[i][2], val[i][3]);
                __builtin_amdgcn_raw_buffer_store_b64(v, rp, slot + split * SPL + i * 512, 0, 16);
            }
        }
        asm volatile("s_waitcnt vmcnt(0)" ::: "memory");
        BAR();
        unsigned* c = cnt + grp * 64;
        if (tid == 0) {
            __hip_atomic_fetch_add(c, 1u, __ATOMIC_RELAXED, __HIP_MEMORY_SCOPE_AGENT);
            unsigned spins = 0;
            while (__hip_atomic_load(c, __ATOMIC_RELAXED, __HIP_MEMORY_SCOPE_AGENT) < (unsigned)NSPLIT) {
                __builtin_amdgcn_s_sleep(1);
                if (++spins > (1u << 22)) break;
            }
        }
        BAR();
        u32x2 pv[3][3];
#pragma unroll
        for (int q = 0; q < 3; ++q) {
            const int i = split + 4 * q;
#pragma unroll
            for (int s2 = 1; s2 < NSPLIT; ++s2) {
                const int so = (split + s2) & 3;
                const unsigned off = i < nit ? slot + so * SPL + i * 512 : 0xfffffff0u;
                pv[q][s2 - 1] = __builtin_amdgcn_raw_buffer_load_b64(rp, off, 0, 16);
            }
        }
#pragma unroll
        for (int q = 0; q < 3; ++q) {
            const int i = split + 4 * q;
            if (i < nit) {
                f32x4 o = val[0];
#pragma unroll
                for (int ii = 0; ii < 10; ++ii) if (ii == i) o = val[ii];
#pragma unroll
                for (int s2 = 0; s2 < 3; ++s2) { o[0] += bf_lo(pv[q][s2][0]); o[1] += bf_hi(pv[q][s2][0]); o[2] += bf_lo(pv[q][s2][1]); o[3] += bf_hi(pv[q][s2][1]); }
                u32x2 v; v[0] = pk_bf16(o[0] + bf_lo(xv[q][0]), o[1] + bf_hi(xv[q][0])); v[1] = pk_bf16(o[2] + bf_lo(xv[q][1]), o[3] + bf_hi(xv[q][1]));
                *(u32x2*)(out + xoff[q]) = v;
            }
        }
    }
}

extern "C" void kernel_launch(void* const* d_in, const int* in_sizes, int n_in, void* d_out, int out_size, void* d_ws,
                              size_t ws_size, hipStream_t stream) {
    const u16* x = (const u16*)d_in[0];
    const u16* wqkv = (const u16*)d_in[1];
    const u16* wo = (const u16*)d_in[2];
    const u16* Wg = (const u16*)d_in[4];
    const u16* Wu = (const u16*)d_in[5];
    const u16* Wd = (const u16*)d_in[6];
    char* ws = (char*)d_ws;
    u16* xnb = (u16*)ws;
    u16* hb = (u16*)(ws + WS_HB);
    u16* part = (u16*)(ws + WS_PART);
    u16* wqh = (u16*)(ws + WS_WQH);
    u16* gx = (u16*)(ws + WS_GX);
    u16* dx = (u16*)(ws + WS_DX);
    unsigned* cnt = (unsigned*)(ws + WS_CNT);
    (void)hipFuncSetAttribute((const void*)k1_gateup, hipFuncAttributeMaxDynamicSharedMemorySize, K1_LDS);
    (void)hipFuncSetAttribute((const void*)k2_down, hipFuncAttributeMaxDynamicSharedMemorySize, K2_LDS);
    k0_ln<<<256, 256, 0, stream>>>(x, xnb, wqkv, wqh, Wg, Wu, gx, Wd, wo, dx);
    k1_gateup<<<256, 512, K1_LDS, stream>>>(xnb, Wg, Wu, wqh, gx, Wd, wo, hb, cnt);
    k2_down<<<256, 512, K2_LDS, stream>>>(hb, Wd, wo, dx, part, cnt, x, (u16*)d_out);
}
```
